```python
import torch

def _softmax(x, axis=-1):
    return torch.softmax(x, dim=axis)

def _gelu(x):
    return torch.nn.functional.gelu(x, approximate='tanh')

def _silu(x):
    return torch.nn.functional.silu(x)

def _sigmoid(x):
    return torch.sigmoid(x)

def _layernorm(x, axis=-1, eps=1e-05):
    m = x.mean(dim=axis, keepdim=True)
    v = x.var(dim=axis, keepdim=True, unbiased=False)
    return (x - m) / torch.sqrt(v + eps)

def _scatter_add(tgt, idx, src, axis=0):
    return tgt.clone().index_add_(axis, idx, src)

def _segment_sum(src, seg, n):
    out = torch.zeros((n,) + src.shape[1:], dtype=src.dtype)
    return out.index_add_(0, seg, src)

def _pad(x, pw):
    flat = tuple((v for pair in reversed(pw) for v in pair))
    return torch.nn.functional.pad(x, flat)
kernel_name = 'adaLN_cond__ssm_conv_s4d__glu_gated_swiglu__prompt'

def setup_inputs():

    _w = lambda a: (lambda t: t.to(torch.bfloat16) if t.is_floating_point() else t)(torch.as_tensor(a))

    def _A_setup():
        Bsz, L, D, N = (1, 2048, 448, 256)
        return {'u': _w((lambda *s: torch.randn(*s, dtype=torch.bfloat16) * 0.05)(Bsz, L, D)), 'log_A_real': _w((lambda *s: torch.randn(*s, dtype=torch.bfloat16) * 3.2)(D, N) * 0.5), 'A_imag': _w((lambda *s: torch.randn(*s, dtype=torch.bfloat16) * 0.05)(D, N) * 3.14), 'C_real': _w((lambda *s: torch.randn(*s, dtype=torch.bfloat16) * 0.2)(D, N) / N ** 0.5), 'C_imag': _w((lambda *s: torch.randn(*s, dtype=torch.bfloat16) * 0.2)(D, N) / N ** 0.5), 'log_dt': _w((lambda *s: torch.randn(*s, dtype=torch.bfloat16) * 0.05)(D) * 0.5 - 4.0), 'D_skip': _w((lambda *s: torch.randn(*s, dtype=torch.bfloat16) * 0.05)(D))}
    _A_in = _A_setup()

    def _B_setup():
        B, S, D, F = (1, 2048, 448, 1792)
        return {'x': _w((lambda *s: torch.randn(*s, dtype=torch.bfloat16) * 0.05)(B, S, D)), 'w1': _w((lambda *s: torch.randn(*s, dtype=torch.bfloat16) * 0.4)(D, F) * (2.0 / D) ** 0.5), 'w3': _w((lambda *s: torch.randn(*s, dtype=torch.bfloat16) * 0.4)(D, F) * (2.0 / D) ** 0.5), 'w2': _w((lambda *s: torch.randn(*s, dtype=torch.bfloat16) * 0.4)(F, D) * (2.0 / F) ** 0.5)}
    _B_in = _B_setup()
    return {'A_u': _A_in['u'], 'A_log_A_real': _A_in['log_A_real'], 'A_A_imag': _A_in['A_imag'], 'A_C_real': _A_in['C_real'], 'A_C_imag': _A_in['C_imag'], 'A_log_dt': _A_in['log_dt'], 'A_D_skip': _A_in['D_skip'], 'B_x': _B_in['x'], 'B_w1': _B_in['w1'], 'B_w3': _B_in['w3'], 'B_w2': _B_in['w2'], 'c': _w((lambda *s: torch.randn(*s, dtype=torch.bfloat16) * 0.4)(1, 448) * 8.0), 'w_mod': _w((lambda *s: torch.randn(*s, dtype=torch.bfloat16) * 0.4)(448, 6 * 448) * 448 ** (-0.5) * 8.0)}

def reference(A_u, A_log_A_real, A_A_imag, A_C_real, A_C_imag, A_log_dt, A_D_skip, B_x, B_w1, B_w3, B_w2, c, w_mod):

    def _A_ref(u, log_A_real, A_imag, C_real, C_imag, log_dt, D_skip):
        L = u.shape[1]
        dt = torch.exp(log_dt)
        A = -torch.exp(log_A_real) + 1j * A_imag
        C = (C_real + 1j * C_imag) * dt[:, None]
        pos = (lambda x, dt: x.to(dt))(torch.arange(L), log_dt.dtype)
        P = torch.exp((A * dt[:, None])[:, :, None] * pos[None, None, :])
        K = 2.0 * torch.einsum('dn,dnl->dl', C, P).real
        L2 = 2 * L
        u_t = (lambda x, *d: x.permute(*d))(u, 0, 2, 1)
        Kf = (lambda x, *a, **k: torch.fft.rfft(x.float(), *a, **k))(K, L2)
        Uf = (lambda x, *a, **k: torch.fft.rfft(x.float(), *a, **k))(u_t, L2)
        y = (lambda x, *a, **k: torch.fft.irfft(x, *a, **k).to(torch.bfloat16))(Uf * Kf[None, :, :], L2)[:, :, :L]
        y = (lambda x, *d: x.permute(*d))(y, 0, 2, 1)
        return y + u * D_skip[None, None, :]

    def _B_ref(x, w1, w3, w2):
        gate = torch.einsum('bsd,df->bsf', x, w1)
        up = torch.einsum('bsd,df->bsf', x, w3)
        h = _silu(gate) * up
        out = torch.einsum('bsf,fd->bsd', h, w2)
        return out
    x = A_u
    mod = (_silu(c) @ w_mod).reshape(c.shape[0], 6, 1, -1)
    sa, ca, ga, sb, cb, gb = (mod[:, 0].clone(), mod[:, 1].clone(), mod[:, 2].clone(), mod[:, 3].clone(), mod[:, 4].clone(), mod[:, 5].clone())
    h = _layernorm(x, axis=-1) * (1 + ca) + sa
    x = x + ga * _A_ref(h, A_log_A_real, A_A_imag, A_C_real, A_C_imag, A_log_dt, A_D_skip)
    h = _layernorm(x, axis=-1) * (1 + cb) + sb
    x = x + gb * _B_ref(h, B_w1, B_w3, B_w2)
    return x
```

```cpp
#include <hip/hip_runtime.h>
#include <hip/hip_bf16.h>
typedef unsigned short u16;
#define L_ 2048
#define D_ 448
#define N_ 256
#define F_ 1792
__device__ __forceinline__ float bf2f(u16 v) { return __uint_as_float(((unsigned)v) << 16); }
__device__ __forceinline__ u16 f2bf(float f) { __hip_bfloat16 b = __float2bfloat16(f); return *(u16*)&b; }
__device__ __forceinline__ float rbf(float f) { return bf2f(f2bf(f)); }

__global__ void k_mod(const u16* c, const u16* wm, float* mod) {
  int j = blockIdx.x * blockDim.x + threadIdx.x;
  if (j >= 6 * D_) return;
  float acc = 0;
  for (int k = 0; k < D_; ++k) { float cv = bf2f(c[k]); float s = rbf(cv / (1.f + expf(-cv))); acc += s * bf2f(wm[k * 6 * D_ + j]); }
  mod[j] = rbf(acc);
}
__global__ void k_ln(const u16* x, const float* sh, const float* sc, u16* h) {
  int l = blockIdx.x; int t = threadIdx.x;
  float v[7]; float s = 0;
  for (int i = 0; i < 7; ++i) { v[i] = bf2f(x[l * D_ + t + 64 * i]); s += v[i]; }
  for (int o = 32; o; o >>= 1) s += __shfl_xor(s, o);
  float m = s / D_; float q = 0;
  for (int i = 0; i < 7; ++i) q += (v[i] - m) * (v[i] - m);
  for (int o = 32; o; o >>= 1) q += __shfl_xor(q, o);
  float var = rbf(q / D_); float mb = rbf(m);
  float sd = rbf(sqrtf(rbf(var + 1e-5f)));
  for (int i = 0; i < 7; ++i) { int d = t + 64 * i; float xn = rbf(rbf(v[i] - mb) / sd); float r = rbf(rbf(xn * rbf(1.f + sc[d])) + sh[d]); h[l * D_ + d] = f2bf(r); }
}
__global__ void k_kgen(const u16* logA, const u16* Aim, const u16* Cr, const u16* Ci, const u16* logdt, float* K) {
  int d = blockIdx.x; int l = blockIdx.y * blockDim.x + threadIdx.x;
  float dt = rbf(expf(bf2f(logdt[d])));
  float p = rbf((float)l);
  float acc = 0;
  for (int n = 0; n < N_; ++n) {
    float ar = -rbf(expf(bf2f(logA[d * N_ + n]))) * dt, ai = bf2f(Aim[d * N_ + n]) * dt;
    float cr = bf2f(Cr[d * N_ + n]) * dt, ci = bf2f(Ci[d * N_ + n]) * dt;
    float e = expf(ar * p); float sn, cs; sincosf(ai * p, &sn, &cs);
    acc += e * (cr * cs - ci * sn);
  }
  K[d * L_ + l] = 2.f * acc;
}
__global__ void k_conv(const u16* x, const u16* h, const float* K, const u16* Dsk, const float* ga, u16* x1) {
  int d = blockIdx.x; __shared__ float sk[L_], sh[L_];
  for (int i = threadIdx.x; i < L_; i += blockDim.x) { sk[i] = K[d * L_ + i]; sh[i] = bf2f(h[i * D_ + d]); }
  __syncthreads();
  for (int l = threadIdx.x; l < L_; l += blockDim.x) {
    float acc = 0;
    for (int m = 0; m <= l; ++m) acc += sk[l - m] * sh[m];
    float y = rbf(acc);
    float a = rbf(y + rbf(sh[l] * bf2f(Dsk[d])));
    x1[l * D_ + d] = f2bf(bf2f(x[l * D_ + d]) + rbf(ga[d] * a));
  }
}
__global__ void k_g1(const u16* h, const u16* w1, const u16* w3, u16* H) {
  int f = blockIdx.x * blockDim.x + threadIdx.x; int l = blockIdx.y;
  float g = 0, u = 0;
  for (int k = 0; k < D_; ++k) { float a = bf2f(h[l * D_ + k]); g += a * bf2f(w1[k * F_ + f]); u += a * bf2f(w3[k * F_ + f]); }
  g = rbf(g); u = rbf(u);
  float s = rbf(g / (1.f + expf(-g)));
  H[l * F_ + f] = f2bf(s * u);
}
__global__ void k_g2(const u16* H, const u16* w2, const u16* x1, const float* gb, u16* out) {
  int d = blockIdx.x * blockDim.x + threadIdx.x; int l = blockIdx.y;
  float o = 0;
  for (int k = 0; k < F_; ++k) o += bf2f(H[l * F_ + k]) * bf2f(w2[k * D_ + d]);
  o = rbf(o);
  out[l * D_ + d] = f2bf(bf2f(x1[l * D_ + d]) + rbf(gb[d] * o));
}
extern "C" void kernel_launch(void* const* d_in, const int* in_sizes, int n_in, void* d_out, int out_size, void* d_ws, size_t ws_size, hipStream_t stream) {
  const u16* x = (const u16*)d_in[0]; const u16* logA = (const u16*)d_in[1]; const u16* Aim = (const u16*)d_in[2];
  const u16* Cr = (const u16*)d_in[3]; const u16* Ci = (const u16*)d_in[4]; const u16* logdt = (const u16*)d_in[5]; const u16* Dsk = (const u16*)d_in[6];
  const u16* w1 = (const u16*)d_in[8]; const u16* w3 = (const u16*)d_in[9]; const u16* w2 = (const u16*)d_in[10]; const u16* c = (const u16*)d_in[11]; const u16* wm = (const u16*)d_in[12];
  char* ws = (char*)d_ws;
  float* mod = (float*)ws; u16* h1 = (u16*)(ws + 65536); float* K = (float*)(ws + 65536 + 2097152); u16* x1 = (u16*)(ws + 65536 + 2097152 + 4194304);
  u16* h2 = (u16*)(ws + 65536 + 2097152 * 2 + 4194304); u16* H = (u16*)(ws + 65536 + 2097152 * 3 + 4194304);
  k_mod<<<(6 * D_ + 63) / 64, 64, 0, stream>>>(c, wm, mod);
  k_ln<<<L_, 64, 0, stream>>>(x, mod, mod + D_, h1);
  k_kgen<<<dim3(D_, L_ / 256), 256, 0, stream>>>(logA, Aim, Cr, Ci, logdt, K);
  k_conv<<<D_, 256, 0, stream>>>(x, h1, K, Dsk, mod + 2 * D_, x1);
  k_ln<<<L_, 64, 0, stream>>>(x1, mod + 3 * D_, mod + 4 * D_, h2);
  k_g1<<<dim3(F_ / 256, L_), 256, 0, stream>>>(h2, w1, w3, H);
  k_g2<<<dim3(D_ / 64, L_), 64, 0, stream>>>(H, w2, x1, mod + 5 * D_, (u16*)d_out);
}
```

```cpp
#include <hip/hip_runtime.h>

typedef unsigned short u16;
typedef unsigned u32;
typedef u32 u32x4 __attribute__((ext_vector_type(4)));
typedef u32 u32x2 __attribute__((ext_vector_type(2)));
typedef short s16x4 __attribute__((ext_vector_type(4)));
typedef __bf16 bf16x8 __attribute__((ext_vector_type(8)));
typedef __bf16 bf16x2 __attribute__((ext_vector_type(2)));
typedef _Float16 f16x8 __attribute__((ext_vector_type(8)));
typedef _Float16 f16x2 __attribute__((ext_vector_type(2)));
typedef float f32x2 __attribute__((ext_vector_type(2)));
typedef float f32x4 __attribute__((ext_vector_type(4)));
typedef float f32x16 __attribute__((ext_vector_type(16)));
#define DI __device__ __forceinline__
#define LDSP(T, p) ((__attribute__((address_space(3))) T*)(p))

constexpr int L_ = 2048, D_ = 448, N_ = 256, F_ = 1792;

constexpr size_t WS_MODP = 0;
constexpr size_t WS_MODF = 45056;
constexpr size_t WS_XX = 65536;
constexpr size_t WS_KREV = WS_XX + 128 * 448 * 64;
constexpr int KREV_STRIDE = 2064;
constexpr size_t WS_X1T = WS_KREV + 448 * KREV_STRIDE * 2;
constexpr size_t WS_W13 = WS_X1T + 64 * 448 * 64;
constexpr size_t WS_W2F = WS_W13 + 112 * 29 * 1024;
constexpr size_t WS_HF = WS_W2F + 14 * 112 * 1024;
constexpr size_t WS_END = WS_HF + 64 * 112 * 1024;

DI float bflo(u32 v) { return __uint_as_float(v << 16); }
DI float bfhi(u32 v) { return __uint_as_float(v & 0xffff0000u); }
DI float bf2f(u16 v) { return __uint_as_float(((u32)v) << 16); }
DI u32 pkbf(float lo, float hi) { f32x2 p = {lo, hi}; return __builtin_bit_cast(u32, __builtin_convertvector(p, bf16x2)); }
DI u32 pkh(float lo, float hi) { f32x2 p = {lo, hi}; return __builtin_bit_cast(u32, __builtin_convertvector(p, f16x2)); }
DI float rbf(float f) { return bflo(pkbf(f, f)); }
DI float dot2(u32 a, u32 b, float c) { return __builtin_amdgcn_fdot2_f32_bf16(__builtin_bit_cast(bf16x2, a), __builtin_bit_cast(bf16x2, b), c, false); }
DI float fexp(float x) { return __builtin_amdgcn_exp2f(x * 1.4426950408889634f); }
template <int CTRL> DI float dppf(float x) { return __int_as_float(__builtin_amdgcn_update_dpp(0, __float_as_int(x), CTRL, 0xf, 0xf, false)); }
DI float sum16(float x) { x += dppf<0x128>(x); x += dppf<0x124>(x); x += dppf<0x122>(x); x += dppf<0x121>(x); return x; }
DI float max16(float x) { x = fmaxf(x, dppf<0x128>(x)); x = fmaxf(x, dppf<0x124>(x)); x = fmaxf(x, dppf<0x122>(x)); x = fmaxf(x, dppf<0x121>(x)); return x; }
DI float add32(float x) { auto r = __builtin_amdgcn_permlane32_swap(__float_as_uint(x), __float_as_uint(x), false, false); return __uint_as_float(r[0]) + __uint_as_float(r[1]); }
DI float max64(float x) {
  x = max16(x);
  auto r = __builtin_amdgcn_permlane16_swap(__float_as_uint(x), __float_as_uint(x), false, false);
  x = fmaxf(__uint_as_float(r[0]), __uint_as_float(r[1]));
  auto q = __builtin_amdgcn_permlane32_swap(__float_as_uint(x), __float_as_uint(x), false, false);
  return fmaxf(__uint_as_float(q[0]), __uint_as_float(q[1]));
}
DI u32x2 trread(const char* p) { return __builtin_bit_cast(u32x2, __builtin_amdgcn_ds_read_tr16_b64_v4i16(LDSP(s16x4, p))); }
DI float modsum(const float* modp, int j) { return rbf((modp[j] + modp[2688 + j]) + (modp[2 * 2688 + j] + modp[3 * 2688 + j])); }

struct cplx { float r, i; };
DI cplx cmul(cplx a, cplx b) { cplx c; c.r = a.r * b.r - a.i * b.i; c.i = a.r * b.i + a.i * b.r; return c; }

#define STAMP(buf, slot, idx) do { } while (0)

constexpr int K1_NA = 128, K1_NB = 168, K1_NC = 448;
constexpr int K1_LDS = 4 * 32 * 272 + 4160;

__global__ __launch_bounds__(256) void k1_prep(const u16* __restrict__ x, const u16* __restrict__ logA, const u16* __restrict__ Aim,
                                               const u16* __restrict__ Cr, const u16* __restrict__ Ci, const u16* __restrict__ logdt,
                                               const u16* __restrict__ cvec, const u16* __restrict__ wmod, char* __restrict__ ws,
                                               unsigned long long* stamps) {
  __shared__ __attribute__((aligned(16))) char smem[K1_LDS];
  const int tid = threadIdx.x, lane = tid & 63, w = tid >> 6;
  int b = blockIdx.x;
  STAMP(stamps, b, 0);
  if (b < K1_NA) {
    const int row = tid >> 4, p = tid & 15;
    const u32x2* src = (const u32x2*)(x + (size_t)(b * 16 + row) * D_);
    u32x2 v[7];
#pragma unroll
    for (int i = 0; i < 7; ++i) v[i] = src[p + 16 * i];
    float f[28];
    float s = 0.f;
#pragma unroll
    for (int i = 0; i < 7; ++i) {
      f[4 * i] = bflo(v[i].x); f[4 * i + 1] = bfhi(v[i].x); f[4 * i + 2] = bflo(v[i].y); f[4 * i + 3] = bfhi(v[i].y);
      s += (f[4 * i] + f[4 * i + 1]) + (f[4 * i + 2] + f[4 * i + 3]);
    }
    s = sum16(s);
    const float m = s * (1.f / D_);
    float q = 0.f;
#pragma unroll
    for (int i = 0; i < 28; ++i) { float t = f[i] - m; q += t * t; }
    q = sum16(q);
    const float mb = rbf(m);
    const float sd = rbf(__builtin_amdgcn_sqrtf(rbf(rbf(q * (1.f / D_)) + 1e-5f)));
    const float rs = 1.f / sd;
    char* imx = smem; char* imn = smem + 16 * 928;
#pragma unroll
    for (int i = 0; i < 7; ++i) {
      u32 a = pkbf(f[4 * i] - mb, f[4 * i + 1] - mb), c = pkbf(f[4 * i + 2] - mb, f[4 * i + 3] - mb);
      u32x2 n2; n2.x = pkbf(bflo(a) * rs, bfhi(a) * rs); n2.y = pkbf(bflo(c) * rs, bfhi(c) * rs);
      *(u32x2*)(imx + row * 928 + (p + 16 * i) * 8) = v[i];
      *(u32x2*)(imn + row * 928 + (p + 16 * i) * 8) = n2;
    }
    __syncthreads();
    const int g = lane >> 4, qq = (lane & 15) >> 2, pp = lane & 3;
    char* dst = ws + WS_XX + (size_t)b * (448 * 64);
#pragma unroll
    for (int i = 0; i < 7; ++i) {
      const int dg = w * 7 + i;
      const int off = (4 * g + qq) * 928 + dg * 32 + pp * 8;
      u32x2 a = trread(imx + off), c = trread(imn + off);
      u32x4 o = {a.x, a.y, c.x, c.y};
      *(u32x4*)(dst + (dg * 16 + (lane & 15)) * 64 + g * 16) = o;
    }
    STAMP(stamps, blockIdx.x, 1);
    return;
  }
  b -= K1_NA;
  if (b < K1_NB) {
    const int cg = b >> 2, kq = b & 3;
    const int r = tid >> 3, p = tid & 7;
    u32x4 v[4]; u16 cv[4];
#pragma unroll
    for (int i = 0; i < 4; ++i) {
      int rr = r + 32 * i; if (rr > 111) rr = 111;
      const int k = 112 * kq + rr;
      v[i] = *(const u32x4*)(wmod + (size_t)k * 2688 + cg * 64 + p * 8);
      cv[i] = cvec[k];
    }
    float acc[8] = {0, 0, 0, 0, 0, 0, 0, 0};
#pragma unroll
    for (int i = 0; i < 4; ++i) {
      float c = bf2f(cv[i]);
      float sl = rbf(c / (1.f + fexp(-c)));
      if (r + 32 * i > 111) sl = 0.f;
#pragma unroll
      for (int j = 0; j < 4; ++j) { acc[2 * j] += sl * bflo(v[i][j]); acc[2 * j + 1] += sl * bfhi(v[i][j]); }
    }
    float* red = (float*)smem;
#pragma unroll
    for (int j = 0; j < 8; ++j) { acc[j] += dppf<0x128>(acc[j]); acc[j] = add32(acc[j]); }
    if ((lane & 40) == 0) {
      const int slot = w * 2 + (lane >> 4);
      *(f32x4*)(red + slot * 64 + p * 8) = f32x4{acc[0], acc[1], acc[2], acc[3]};
      *(f32x4*)(red + slot * 64 + p * 8 + 4) = f32x4{acc[4], acc[5], acc[6], acc[7]};
    }
    __syncthreads();
    if (tid < 64) {
      float t = 0.f;
#pragma unroll
      for (int j = 0; j < 8; ++j) t += red[j * 64 + tid];
      ((float*)(ws + WS_MODP))[kq * 2688 + cg * 64 + tid] = t;
    }
    STAMP(stamps, blockIdx.x, 1);
    return;
  }
  b -= K1_NB;
  {
    const int d = b, n = tid;
    const float la = bf2f(logA[d * N_ + n]), aim = bf2f(Aim[d * N_ + n]);
    const float cr = bf2f(Cr[d * N_ + n]), ci = bf2f(Ci[d * N_ + n]);
    const float dt = rbf(fexp(bf2f(logdt[d])));
    const float ar = -rbf(fexp(la)) * dt * 1.4426950408889634f;
    const float ai = aim * dt;
    const float I2PH = 0.15915494f, I2PL = 6.4206382e-9f;
    const float th = ai * I2PH;
    const float tl = __builtin_fmaf(ai, I2PH, -th) + ai * I2PL;
    auto zpow = [&](float sg) {
      float e = __builtin_amdgcn_exp2f(ar * sg);
      float rev = __builtin_amdgcn_fractf(th * sg) + tl * sg;
      cplx z; z.r = e * __builtin_amdgcn_cosf(rev); z.i = e * __builtin_amdgcn_sinf(rev); return z;
    };
    const cplx z1 = zpow(1.f), z8 = zpow(8.f), z16 = zpow(16.f), z256 = zpow(256.f);
    float cm = max64(fmaxf(fabsf(cr), fabsf(ci)));
    u32 eb = (__float_as_uint(cm) >> 23) & 255u;
    eb = eb < 1u ? 1u : (eb > 252u ? 252u : eb);
    const float scl = __uint_as_float((253u - eb) << 23);
    const float iscl = __uint_as_float((eb + 1u) << 23);
    char* reg = smem + w * (32 * 272);
    {
      cplx ra = {cr * scl, ci * scl};
      cplx rb = cmul(ra, z8);
#pragma unroll
      for (int r = 0; r < 32; r += 2) {
        *(u32*)(reg + r * 272 + lane * 4) = pkh(ra.r, -ra.i);
        *(u32*)(reg + (r + 1) * 272 + lane * 4) = pkh(rb.r, -rb.i);
        if (r < 30) { ra = cmul(ra, z16); rb = cmul(rb, z16); }
      }
    }
    f16x8 af[8];
#pragma unroll
    for (int s = 0; s < 8; ++s) af[s] = *(const f16x8*)(reg + (lane & 31) * 272 + s * 32 + (lane >> 5) * 16);
    {
      cplx z2 = cmul(z1, z1), z3 = cmul(z2, z1), z4 = cmul(z2, z2), z5 = cmul(z4, z1), z6 = cmul(z4, z2), z7 = cmul(z4, z3);
      cplx z512 = cmul(z256, z256), z768 = cmul(z512, z256), z1024 = cmul(z512, z512);
      cplx z1280 = cmul(z1024, z256), z1536 = cmul(z1024, z512), z1792 = cmul(z1024, z768), z2048 = cmul(z1024, z1024);
      cplx col[21] = {{1.f, 0.f}, z1, z2, z3, z4, z5, z6, z7, z256, cmul(z256, z2), cmul(z256, z4), cmul(z256, z6),
                      z512, cmul(z512, z4), z768, cmul(z768, z4), z1024, z1280, z1536, z1792, z2048};
      asm volatile("s_waitcnt lgkmcnt(0)" : "+v"(af[0]), "+v"(af[1]), "+v"(af[2]), "+v"(af[3]), "+v"(af[4]), "+v"(af[5]), "+v"(af[6]), "+v"(af[7]) :: "memory");
#pragma unroll
      for (int s = 0; s < 21; ++s) *(u32*)(reg + s * 272 + lane * 4) = pkh(col[s].r, col[s].i);
    }
    f32x16 acc = {};
#pragma unroll
    for (int s = 0; s < 8; ++s) {
      f16x8 bfr = *(const f16x8*)(reg + (lane & 31) * 272 + s * 32 + (lane >> 5) * 16);
      acc = __builtin_amdgcn_mfma_f32_32x32x16_f16(af[s], bfr, acc, 0, 0, 0);
    }
    const float fs = 2.f * dt * iscl;
    asm volatile("s_waitcnt lgkmcnt(0)" ::: "memory");
#pragma unroll
    for (int qd = 0; qd < 4; ++qd)
      *(f32x4*)(reg + qd * 1024 + lane * 16) = f32x4{acc[4 * qd] * fs, acc[4 * qd + 1] * fs, acc[4 * qd + 2] * fs, acc[4 * qd + 3] * fs};
    __syncthreads();
    u16* kp = (u16*)(smem + 4 * 32 * 272);
    {
      f32x4 t = *(const f32x4*)(smem + w * 1024 + lane * 16);
#pragma unroll
      for (int ww = 1; ww < 4; ++ww) t += *(const f32x4*)(smem + ww * (32 * 272) + w * 1024 + lane * 16);
      const int s = lane & 31;
      int sig = s < 8 ? s : (s < 12 ? 256 + 2 * (s - 8) : (s < 16 ? 512 + 256 * ((s - 12) >> 1) + 4 * (s & 1) : 1024 + 256 * (s - 16)));
      const int r0 = 8 * w + 4 * (lane >> 5);
      if (s < 20) {
        u32 a = pkbf(t[0], t[1]), c = pkbf(t[2], t[3]);
        kp[sig + 8 * r0] = (u16)a; kp[sig + 8 * r0 + 8] = (u16)(a >> 16); kp[sig + 8 * r0 + 16] = (u16)c; kp[sig + 8 * r0 + 24] = (u16)(c >> 16);
      } else if (s == 20 && r0 == 0) {
        kp[2048] = (u16)pkbf(t[0], t[0]);
      }
    }
    __syncthreads();
    {
      u32 o[4];
#pragma unroll
      for (int e = 0; e < 4; ++e) {
        const int lag0 = 2047 - 8 * tid - 2 * e, lag1 = lag0 - 1;
        u32 pp = pkbf((float)lag0, (float)lag1);
        int p0 = (int)bflo(pp), p1 = (int)bfhi(pp);
        o[e] = (u32)kp[p0] | ((u32)kp[p1] << 16);
      }
      u16* dst = (u16*)(ws + WS_KREV) + (size_t)d * KREV_STRIDE;
      *(u32x4*)(dst + 8 * tid) = u32x4{o[0], o[1], o[2], o[3]};
      if (tid < 2) *(u32x4*)(dst + 2048 + 8 * tid) = u32x4{0, 0, 0, 0};
    }
    STAMP(stamps, blockIdx.x, 1);
  }
}

constexpr int K2_NCONV = 224, K2_NW13 = 112, K2_NW2 = 56;
constexpr int K2_DSTR = 4 * 4160 + 4608;
constexpr int K2_LDS = 2 * K2_DSTR + 16384;

__global__ __launch_bounds__(256) void k2_conv(const u16* __restrict__ Dsk, const u16* __restrict__ w1, const u16* __restrict__ w3,
                                               const u16* __restrict__ w2, char* __restrict__ ws, unsigned long long* stamps) {
  __shared__ __attribute__((aligned(16))) char smem[K2_LDS];
  const int tid = threadIdx.x, lane = tid & 63, w = tid >> 6;
  int b = blockIdx.x;
  const float* modp = (const float*)(ws + WS_MODP);
  STAMP(stamps, 1024 + b, 0);
  if (b < K2_NCONV) {
    const int wu = __builtin_amdgcn_readfirstlane(w);
    const int dl = wu >> 1, half = wu & 1;
    const int d = 2 * b + dl;
    const int a = lane & 15, g = lane >> 4;
    char* cp = smem + dl * K2_DSTR;
    char* hp = cp + 4 * 4160;
    u32x4 xx[4];
#pragma unroll
    for (int i = 0; i < 4; ++i) {
      const int t = 4 * half + i;
      xx[i] = *(const u32x4*)(ws + WS_XX + ((size_t)(16 * t + a) * 448 + d) * 64 + g * 16);
    }
    const int t128 = tid & 127;
    const u16* kr = (const u16*)(ws + WS_KREV) + (size_t)d * KREV_STRIDE + 16 * t128;
    u32x4 k0 = *(const u32x4*)kr, k1 = *(const u32x4*)(kr + 8);
    u32x2 k2 = *(const u32x2*)(kr + 16);
    const float sa = modsum(modp, d), beta = rbf(1.f + modsum(modp, D_ + d)), ga = modsum(modp, 2 * D_ + d);
    const float dsk = bf2f(Dsk[d]);
    if (t128 < 32) *(u32x4*)(hp + t128 * 16) = u32x4{0, 0, 0, 0};
    else if (t128 < 40) *(u32x4*)(cp + ((t128 - 32) >> 1) * 4160 + 4096 + (t128 & 1) * 16) = u32x4{0, 0, 0, 0};
    {
      u32 wv[10] = {k0.x, k0.y, k0.z, k0.w, k1.x, k1.y, k1.z, k1.w, k2.x, k2.y};
      u32 al[9];
#pragma unroll
      for (int i = 0; i < 9; ++i) al[i] = __builtin_amdgcn_alignbit(wv[i + 1], wv[i], 16);
      char* q = cp + t128 * 32;
      *(u32x4*)(q) = k0; *(u32x4*)(q + 16) = k1;
      *(u32x4*)(q + 4160) = u32x4{al[0], al[1], al[2], al[3]}; *(u32x4*)(q + 4160 + 16) = u32x4{al[4], al[5], al[6], al[7]};
      *(u32x4*)(q + 8320) = u32x4{wv[1], wv[2], wv[3], wv[4]}; *(u32x4*)(q + 8320 + 16) = u32x4{wv[5], wv[6], wv[7], wv[8]};
      *(u32x4*)(q + 12480) = u32x4{al[1], al[2], al[3], al[4]}; *(u32x4*)(q + 12480 + 16) = u32x4{al[5], al[6], al[7], al[8]};
    }
    u32x2 hD[4];
#pragma unroll
    for (int i = 0; i < 4; ++i) {
      const int t = 4 * half + i;
      u32 p0 = pkbf(bflo(xx[i].z) * beta, bfhi(xx[i].z) * beta), p1 = pkbf(bflo(xx[i].w) * beta, bfhi(xx[i].w) * beta);
      u32x2 h; h.x = pkbf(bflo(p0) + sa, bfhi(p0) + sa); h.y = pkbf(bflo(p1) + sa, bfhi(p1) + sa);
      *(u32x2*)(hp + 512 + (256 * t + 16 * a + 4 * g) * 2) = h;
      hD[i].x = pkbf(bflo(h.x) * dsk, bfhi(h.x) * dsk); hD[i].y = pkbf(bflo(h.y) * dsk, bfhi(h.y) * dsk);
    }
    __syncthreads();
    STAMP(stamps, 1024 + blockIdx.x, 2);
    f32x4 acc[8];
#pragma unroll
    for (int t = 0; t < 8; ++t) acc[t] = f32x4{0, 0, 0, 0};
    const int im = 15 - a;
    const char* abase = cp + (im & 3) * 4160 + (2016 + 8 * g + 4 * (im >> 2)) * 2 - half * 4 * 64;
    const char* bbase = hp + (256 - 16 + 16 * a + 8 * g) * 2 - half * 4 * 64;
#pragma unroll
    for (int cc = 0; cc < 4; ++cc) {
      bf16x8 af[8], bfr[8];
#pragma unroll
      for (int c1 = 0; c1 < 8; ++c1) {
        const char* pa = abase - (8 * c1 + cc) * 64;
        u32x2 lo = *(const u32x2*)pa, hi = *(const u32x2*)(pa + 8);
        af[c1] = __builtin_bit_cast(bf16x8, u32x4{lo.x, lo.y, hi.x, hi.y});
        bfr[c1] = *(const bf16x8*)(bbase + (8 * c1 - cc) * 64);
      }
#pragma unroll
      for (int c1 = 0; c1 < 8; ++c1)
#pragma unroll
        for (int s = 0; s + c1 < 8; ++s)
          acc[s + c1] = __builtin_amdgcn_mfma_f32_16x16x32_bf16(af[c1], bfr[s], acc[s + c1], 0, 0, 0);
    }
    STAMP(stamps, 1024 + blockIdx.x, 3);
    char* red = smem + 2 * K2_DSTR;
    if (half) {
#pragma unroll
      for (int i = 0; i < 4; ++i) { *(f32x4*)(red + w * 4096 + i * 1024 + lane * 16) = acc[i]; acc[i] = acc[4 + i]; }
    } else {
#pragma unroll
      for (int i = 0; i < 4; ++i) *(f32x4*)(red + w * 4096 + i * 1024 + lane * 16) = acc[4 + i];
    }
    __syncthreads();
    char* x1t = ws + WS_X1T;
#pragma unroll
    for (int i = 0; i < 4; ++i) {
      const int t = 4 * half + i;
      f32x4 y = acc[i] + *(const f32x4*)(red + (w ^ 1) * 4096 + i * 1024 + lane * 16);
      u32 y0 = pkbf(y[0], y[1]), y1 = pkbf(y[2], y[3]);
      u32 s0 = pkbf(bflo(y0) + bflo(hD[i].x), bfhi(y0) + bfhi(hD[i].x)), s1 = pkbf(bflo(y1) + bflo(hD[i].y), bfhi(y1) + bfhi(hD[i].y));
      u32 m0 = pkbf(bflo(s0) * ga, bfhi(s0) * ga), m1 = pkbf(bflo(s1) * ga, bfhi(s1) * ga);
      u32x2 o; o.x = pkbf(bflo(xx[i].x) + bflo(m0), bfhi(xx[i].x) + bfhi(m0)); o.y = pkbf(bflo(xx[i].y) + bflo(m1), bfhi(xx[i].y) + bfhi(m1));
      const int l = 256 * t + 16 * a + 4 * g;
      *(u32x2*)(x1t + ((size_t)(l >> 5) * 448 + d) * 64 + (l & 31) * 2) = o;
    }
    STAMP(stamps, 1024 + blockIdx.x, 1);
    return;
  }
  b -= K2_NCONV;
  const int G = lane >> 4, qq = (lane & 15) >> 2, pp = lane & 3, hi = lane >> 5;
  if (b < K2_NW13) {
    const int fg = b >> 2, kq = b & 3;
    u32x4 v[7];
#pragma unroll
    for (int i = 0; i < 7; ++i) {
      const int c = tid + 256 * i;
      const int mat = c >= 896, cr = c - mat * 896;
      v[i] = *(const u32x4*)((mat ? w3 : w1) + (size_t)(112 * kq + (cr >> 3)) * F_ + fg * 64 + (cr & 7) * 8);
    }
    float* betas = (float*)(smem + 2 * 112 * 144 + 64 + 64);
    u16* sbs = (u16*)(betas + 112);
    if (tid < 112) {
      betas[tid] = rbf(1.f + modsum(modp, 4 * D_ + 112 * kq + tid));
      sbs[tid] = (u16)(__float_as_uint(modsum(modp, 3 * D_ + 112 * kq + tid)) >> 16);
    }
    if (b < 21 && tid < 128) ((float*)(ws + WS_MODF))[b * 128 + tid] = modsum(modp, b * 128 + tid);
#pragma unroll
    for (int i = 0; i < 7; ++i) {
      const int c = tid + 256 * i;
      const int mat = c >= 896, cr = c - mat * 896;
      *(u32x4*)(smem + mat * (112 * 144 + 64) + (cr >> 3) * 144 + (cr & 7) * 16) = v[i];
    }
    __syncthreads();
    const int ft = 4 * fg + w;
    const char* rbase = smem + (G & 1) * (112 * 144 + 64) + (8 * hi + qq) * 144 + (16 * w + 8 * (pp & 1) + 4 * (pp >> 1)) * 2;
    char* dst = ws + WS_W13 + (size_t)ft * 29 * 1024;
    float us = 0.f, vs = 0.f;
#pragma unroll
    for (int s = 0; s < 7; ++s) {
      u32x2 lo = trread(rbase + s * 16 * 144), hh = trread(rbase + (s * 16 + 4) * 144);
      f32x4 b0 = *(const f32x4*)(betas + 16 * s + 8 * hi), b1 = *(const f32x4*)(betas + 16 * s + 8 * hi + 4);
      u32x4 sb4 = *(const u32x4*)(sbs + 16 * s + 8 * hi);
      u32x4 o;
      o.x = pkbf(bflo(lo.x) * b0[0], bfhi(lo.x) * b0[1]); o.y = pkbf(bflo(lo.y) * b0[2], bfhi(lo.y) * b0[3]);
      o.z = pkbf(bflo(hh.x) * b1[0], bfhi(hh.x) * b1[1]); o.w = pkbf(bflo(hh.y) * b1[2], bfhi(hh.y) * b1[3]);
      us = dot2(o.x, 0x3f803f80u, us); us = dot2(o.y, 0x3f803f80u, us); us = dot2(o.z, 0x3f803f80u, us); us = dot2(o.w, 0x3f803f80u, us);
      vs = dot2(lo.x, sb4.x, vs); vs = dot2(lo.y, sb4.y, vs); vs = dot2(hh.x, sb4.z, vs); vs = dot2(hh.y, sb4.w, vs);
      *(u32x4*)(dst + (7 * kq + s) * 1024 + lane * 16) = o;
    }
    us = add32(us); vs = add32(vs);
    if (lane < 32) {
      float uh = rbf(us), vh = rbf(vs);
      u32x2 o; o.x = pkbf(uh, us - uh); o.y = pkbf(vh, vs - vh);
      *(u32x2*)(dst + 28 * 1024 + (lane + 32 * (kq >> 1)) * 16 + (kq & 1) * 8) = o;
    }
    STAMP(stamps, 1024 + blockIdx.x, 1);
    return;
  }
  b -= K2_NW13;
  {
    const int dg = b >> 3, fr = b & 7;
    u32x4 v[7];
#pragma unroll
    for (int i = 0; i < 7; ++i) {
      const int c = tid + 256 * i;
      v[i] = *(const u32x4*)(w2 + (size_t)(224 * fr + (c >> 3)) * D_ + dg * 64 + (c & 7) * 8);
    }
#pragma unroll
    for (int i = 0; i < 7; ++i) {
      const int c = tid + 256 * i;
      *(u32x4*)(smem + (c >> 3) * 144 + (c & 7) * 16) = v[i];
    }
    __syncthreads();
    const char* rbase = smem + (112 * (w >> 1) + 8 * hi + qq) * 144 + (32 * (w & 1) + 16 * (G & 1) + 4 * pp) * 2;
    char* dst = ws + WS_W2F + ((size_t)(2 * dg + (w & 1)) * 112 + 14 * fr + 7 * (w >> 1)) * 1024;
#pragma unroll
    for (int s = 0; s < 7; ++s) {
      u32x2 lo = trread(rbase + s * 16 * 144), hh = trread(rbase + (s * 16 + 4) * 144);
      *(u32x4*)(dst + s * 1024 + lane * 16) = u32x4{lo.x, lo.y, hh.x, hh.y};
    }
    STAMP(stamps, 1024 + blockIdx.x, 1);
  }
}

#define GLOAD(dst, voff, sbase, imm) asm volatile("global_load_dwordx4 %0, %1, %2 offset:%3" : "=v"(dst) : "v"(voff), "s"(sbase), "i"(imm))
DI void glds16(u32 voff, const char* sbase, u32 ldsaddr) {
  u32 keep;
  asm volatile("s_mov_b32 %0, m0\n\ts_mov_b32 m0, %3\n\ts_nop 0\n\tglobal_load_lds_dwordx4 %1, %2\n\ts_mov_b32 m0, %0"
               : "=&s"(keep) : "v"(voff), "s"(sbase), "s"(ldsaddr) : "memory");
}
DI const char* uptr(const char* p) {
  size_t v = (size_t)p;
  u32 lo = __builtin_amdgcn_readfirstlane((u32)v), hi = __builtin_amdgcn_readfirstlane((u32)(v >> 32));
  return (const char*)(((size_t)hi << 32) | lo);
}

constexpr int K3_LDS = 4 * 28672 + 1024;
constexpr int K3_PD = 8;

__global__ __launch_bounds__(256) void k3_gemm1(char* __restrict__ ws, unsigned long long* stamps) {
  __shared__ __attribute__((aligned(16))) char smem[K3_LDS];
  const int tid = threadIdx.x, lane = tid & 63;
  const int w = __builtin_amdgcn_readfirstlane(tid >> 6);
  const int b = blockIdx.x, xcd = b & 7, idx = b >> 3;
  const int rt = 4 * (xcd >> 1) + idx / 7, ct = 7 * (xcd & 1) + idx % 7;
  STAMP(stamps, 2048 + b, 0);
  const u32 voff = lane * 16;
  const char* xsrc = uptr(ws + WS_X1T + (size_t)(4 * rt + w) * 28672);
  const char* wsrc = uptr(ws + WS_W13 + (size_t)(8 * ct + 2 * w) * 29 * 1024);
  const u32 lds0 = (u32)(size_t)LDSP(char, smem);
  const u32 xdst = lds0 + w * 28672;
  u32x4 wf[29][2];
#define K3_ISSUE(ks) do { if ((ks) < 28) glds16(voff, xsrc + (ks) * 1024, xdst + (ks) * 1024); \
    if ((ks) < 29) { GLOAD(wf[(ks) < 29 ? (ks) : 0][0], voff, wsrc + (ks) * 1024, 0); GLOAD(wf[(ks) < 29 ? (ks) : 0][1], voff, wsrc + (29 + (ks)) * 1024, 0); } } while (0)
#pragma unroll
  for (int ks = 0; ks < K3_PD; ++ks) K3_ISSUE(ks);
  f32x16 acc[2][4];
#pragma unroll
  for (int i = 0; i < 2; ++i)
#pragma unroll
    for (int j = 0; j < 4; ++j)
#pragma unroll
      for (int r = 0; r < 16; ++r) acc[i][j][r] = 0.f;
  const int G = lane >> 4, qq = (lane & 15) >> 2, pp = lane & 3, hi = lane >> 5;
  const char* rbase = smem + (8 * hi + qq) * 64 + (G & 1) * 32 + pp * 8;
  float ssum = 0.f, ssq = 0.f;
#define K3_CNT(k) ((k) < 28 ? 3 * ((k) + 1) : 86)
#pragma unroll
  for (int kg = 0; kg < 28; kg += 2) {
    {
      constexpr int dummy = 0; (void)dummy;
      const int issued = K3_CNT((kg + K3_PD - 1) < 28 ? (kg + K3_PD - 1) : 28);
      const int pend = issued - K3_CNT(kg + 1);
      asm volatile("s_waitcnt vmcnt(%4)\n\ts_barrier" : "+v"(wf[kg][0]), "+v"(wf[kg][1]), "+v"(wf[kg + 1][0]), "+v"(wf[kg + 1][1]) : "i"(pend) : "memory");
    }
    K3_ISSUE(kg + K3_PD);
    K3_ISSUE(kg + K3_PD + 1);
#pragma unroll
    for (int ks = kg; ks < kg + 2; ++ks) {
      bf16x8 xf[4];
#pragma unroll
      for (int j = 0; j < 4; ++j) {
        u32x2 lo = trread(rbase + j * 28672 + ks * 1024), hh = trread(rbase + j * 28672 + ks * 1024 + 256);
        xf[j] = __builtin_bit_cast(bf16x8, u32x4{lo.x, lo.y, hh.x, hh.y});
      }
      {
        u32x2 lo = trread(rbase + w * 28672 + ks * 1024), hh = trread(rbase + w * 28672 + ks * 1024 + 256);
        ssum = dot2(lo.x, 0x3f803f80u, ssum); ssum = dot2(lo.y, 0x3f803f80u, ssum); ssum = dot2(hh.x, 0x3f803f80u, ssum); ssum = dot2(hh.y, 0x3f803f80u, ssum);
        ssq = dot2(lo.x, lo.x, ssq); ssq = dot2(lo.y, lo.y, ssq); ssq = dot2(hh.x, hh.x, ssq); ssq = dot2(hh.y, hh.y, ssq);
      }
#pragma unroll
      for (int j = 0; j < 4; ++j)
#pragma unroll
        for (int i = 0; i < 2; ++i)
          acc[i][j] = __builtin_amdgcn_mfma_f32_32x32x16_bf16(__builtin_bit_cast(bf16x8, wf[ks][i]), xf[j], acc[i][j], 0, 0, 0);
    }
  }
  STAMP(stamps, 2048 + b, 2);
  ssum = add32(ssum); ssq = add32(ssq);
  {
    const float m = ssum * (1.f / D_);
    float var = ssq * (1.f / D_) - m * m; var = fmaxf(var, 0.f);
    const float sd = rbf(__builtin_amdgcn_sqrtf(rbf(rbf(var) + 1e-5f)));
    if (lane < 32) *(u32x2*)(smem + 4 * 28672 + (w * 32 + lane) * 8) = u32x2{pkbf(-m, sd), __float_as_uint(1.f / sd)};
  }
  asm volatile("s_waitcnt vmcnt(0) lgkmcnt(0)\n\ts_barrier" : "+v"(wf[28][0]), "+v"(wf[28][1]) :: "memory");
  char* hdst = ws + WS_HF + ((size_t)(4 * rt) * 112 + 8 * ct + 2 * w) * 1024 + lane * 16;
#pragma unroll
  for (int j = 0; j < 4; ++j) {
    const u32x2 st = *(const u32x2*)(smem + 4 * 28672 + (j * 32 + (lane & 31)) * 8);
    const u32 mm = (st.x & 0xffffu) | (st.x << 16), ss = (st.x >> 16) | (st.x & 0xffff0000u);
    const bf16x8 ex = __builtin_bit_cast(bf16x8, u32x4{mm, ss, mm, ss});
    const float al = __uint_as_float(st.y), nal = -al * 1.4426950408889634f, al2 = al * al;
#pragma unroll
    for (int i = 0; i < 2; ++i) {
      f32x16 c = __builtin_amdgcn_mfma_f32_32x32x16_bf16(__builtin_bit_cast(bf16x8, wf[28][i]), ex, acc[i][j], 0, 0, 0);
      float hv[8];
#pragma unroll
      for (int r = 0; r < 8; ++r) {
        float e = __builtin_amdgcn_exp2f(c[r] * nal);
        float rc = __builtin_amdgcn_rcpf(1.f + e);
        hv[r] = (c[r] * rc) * (c[8 + r] * al2);
      }
      u32x4 o = {pkbf(hv[0], hv[1]), pkbf(hv[2], hv[3]), pkbf(hv[4], hv[5]), pkbf(hv[6], hv[7])};
      *(u32x4*)(hdst + (size_t)j * 112 * 1024 + i * 1024) = o;
    }
  }
  STAMP(stamps, 2048 + b, 1);
}

constexpr int K4_PD = 8;
constexpr int K4_LDS = 4 * 3 * 4096;

__global__ __launch_bounds__(256) void k4_gemm2(char* __restrict__ ws, u16* __restrict__ out, unsigned long long* stamps) {
  __shared__ __attribute__((aligned(16))) char smem[K4_LDS];
  const int tid = threadIdx.x, lane = tid & 63;
  const int w = __builtin_amdgcn_readfirstlane(tid >> 6);
  const int b = blockIdx.x, xcd = b & 7, idx = b >> 3;
  const int rt = 4 * xcd + idx / 7, dg = idx % 7;
  STAMP(stamps, 3072 + b, 0);
  const int hi = lane >> 5, tok = lane & 31;
  const int fdt = w >> 1, ftt = w & 1;
  const int dbase = 64 * dg + 32 * fdt + 4 * hi;
  u16 xr[16]; f32x4 gbv[4];
  {
    const u16* x1 = (const u16*)(ws + WS_X1T) + ((size_t)(2 * rt + ftt) * 448 + dbase) * 32 + tok;
    const float* gb = (const float*)(ws + WS_MODF) + 5 * D_ + dbase;
#pragma unroll
    for (int q = 0; q < 4; ++q) {
      gbv[q] = *(const f32x4*)(gb + 8 * q);
#pragma unroll
      for (int e = 0; e < 4; ++e) xr[4 * q + e] = x1[(8 * q + e) * 32];
    }
  }
  const u32 voff = lane * 16;
  const char* hsrc = uptr(ws + WS_HF + ((size_t)(2 * rt) * 112 + 28 * w) * 1024);
  const char* wsrc = uptr(ws + WS_W2F + ((size_t)(2 * dg) * 112 + 28 * w) * 1024);
  u32x4 hf[28][2], wf[28][2];
#define K4_ISSUE(ks) do { if ((ks) < 28) { GLOAD(hf[(ks) < 28 ? (ks) : 0][0], voff, hsrc + (ks) * 1024, 0); GLOAD(wf[(ks) < 28 ? (ks) : 0][0], voff, wsrc + (ks) * 1024, 0); \
    GLOAD(hf[(ks) < 28 ? (ks) : 0][1], voff, hsrc + (112 + (ks)) * 1024, 0); GLOAD(wf[(ks) < 28 ? (ks) : 0][1], voff, wsrc + (112 + (ks)) * 1024, 0); } } while (0)
#pragma unroll
  for (int ks = 0; ks < K4_PD; ++ks) K4_ISSUE(ks);
  f32x16 acc[2][2];
#pragma unroll
  for (int i = 0; i < 2; ++i)
#pragma unroll
    for (int j = 0; j < 2; ++j)
#pragma unroll
      for (int r = 0; r < 16; ++r) acc[i][j][r] = 0.f;
#pragma unroll
  for (int ks = 0; ks < 28; ++ks) {
    const int issued = (ks + K4_PD < 28 ? ks + K4_PD : 28) * 4;
    const int pend = issued - 4 * (ks + 1);
    asm volatile("s_waitcnt vmcnt(%4)" : "+v"(hf[ks][0]), "+v"(hf[ks][1]), "+v"(wf[ks][0]), "+v"(wf[ks][1]) : "i"(pend) : "memory");
    K4_ISSUE(ks + K4_PD);
#pragma unroll
    for (int i = 0; i < 2; ++i)
#pragma unroll
      for (int j = 0; j < 2; ++j)
        acc[i][j] = __builtin_amdgcn_mfma_f32_32x32x16_bf16(__builtin_bit_cast(bf16x8, wf[ks][i]), __builtin_bit_cast(bf16x8, hf[ks][j]), acc[i][j], 0, 0, 0);
  }
  STAMP(stamps, 3072 + b, 2);
#pragma unroll
  for (int T = 0; T < 4; ++T) {
    if (T != w) {
      const int slot = w < T ? w : w - 1;
      char* p = smem + (T * 3 + slot) * 4096 + lane * 16;
#pragma unroll
      for (int q = 0; q < 4; ++q)
        *(f32x4*)(p + q * 1024) = f32x4{acc[T >> 1][T & 1][4 * q], acc[T >> 1][T & 1][4 * q + 1], acc[T >> 1][T & 1][4 * q + 2], acc[T >> 1][T & 1][4 * q + 3]};
    }
  }
  f32x16 mine = w == 0 ? acc[0][0] : (w == 1 ? acc[0][1] : (w == 2 ? acc[1][0] : acc[1][1]));
  __syncthreads();
  u16* orow = out + (size_t)(64 * rt + 32 * ftt + tok) * D_ + dbase;
#pragma unroll
  for (int q = 0; q < 4; ++q) {
    f32x4 s = {mine[4 * q], mine[4 * q + 1], mine[4 * q + 2], mine[4 * q + 3]};
#pragma unroll
    for (int sl = 0; sl < 3; ++sl) s += *(const f32x4*)(smem + (w * 3 + sl) * 4096 + q * 1024 + lane * 16);
    u32 o0 = pkbf(s[0], s[1]), o1 = pkbf(s[2], s[3]);
    u32 m0 = pkbf(bflo(o0) * gbv[q][0], bfhi(o0) * gbv[q][1]), m1 = pkbf(bflo(o1) * gbv[q][2], bfhi(o1) * gbv[q][3]);
    u32x2 o; o.x = pkbf(bf2f(xr[4 * q]) + bflo(m0), bf2f(xr[4 * q + 1]) + bfhi(m0)); o.y = pkbf(bf2f(xr[4 * q + 2]) + bflo(m1), bf2f(xr[4 * q + 3]) + bfhi(m1));
    *(u32x2*)(orow + 8 * q) = o;
  }
  STAMP(stamps, 3072 + b, 1);
}

extern "C" void kernel_launch(void* const* d_in, const int* in_sizes, int n_in, void* d_out, int out_size, void* d_ws, size_t ws_size,
                              hipStream_t stream) {
  const u16* x = (const u16*)d_in[0]; const u16* logA = (const u16*)d_in[1]; const u16* Aim = (const u16*)d_in[2];
  const u16* Cr = (const u16*)d_in[3]; const u16* Ci = (const u16*)d_in[4]; const u16* logdt = (const u16*)d_in[5]; const u16* Dsk = (const u16*)d_in[6];
  const u16* w1 = (const u16*)d_in[8]; const u16* w3 = (const u16*)d_in[9]; const u16* w2 = (const u16*)d_in[10];
  const u16* c = (const u16*)d_in[11]; const u16* wm = (const u16*)d_in[12];
  char* ws = (char*)d_ws;
  unsigned long long* stamps = (unsigned long long*)(ws + (64u << 20));
  k1_prep<<<K1_NA + K1_NB + K1_NC, 256, 0, stream>>>(x, logA, Aim, Cr, Ci, logdt, c, wm, ws, stamps);
  k2_conv<<<K2_NCONV + K2_NW13 + K2_NW2, 256, 0, stream>>>(Dsk, w1, w3, w2, ws, stamps);
  k3_gemm1<<<224, 256, 0, stream>>>(ws, stamps);
  k4_gemm2<<<224, 256, 0, stream>>>(ws, (u16*)d_out, stamps);
}
```

```cpp
#include <hip/hip_runtime.h>

typedef unsigned short u16;
typedef unsigned u32;
typedef u32 u32x4 __attribute__((ext_vector_type(4)));
typedef u32 u32x2 __attribute__((ext_vector_type(2)));
typedef short s16x4 __attribute__((ext_vector_type(4)));
typedef __bf16 bf16x8 __attribute__((ext_vector_type(8)));
typedef __bf16 bf16x2 __attribute__((ext_vector_type(2)));
typedef _Float16 f16x8 __attribute__((ext_vector_type(8)));
typedef _Float16 f16x2 __attribute__((ext_vector_type(2)));
typedef float f32x2 __attribute__((ext_vector_type(2)));
typedef float f32x4 __attribute__((ext_vector_type(4)));
typedef float f32x16 __attribute__((ext_vector_type(16)));
#define DI __device__ __forceinline__
#define U2F(x) __builtin_bit_cast(float, (u32)(x))
#define F2U(x) __builtin_bit_cast(u32, (float)(x))
#define I2F(x) __builtin_bit_cast(float, (int)(x))
#define F2I(x) __builtin_bit_cast(int, (float)(x))
#if defined(__HIP_DEVICE_COMPILE__)
#define NOPK __attribute__((target("no-packed-fp32-ops")))
#else
#define NOPK
#endif
#define LDSP(T, p) ((__attribute__((address_space(3))) T*)(p))
#define GAS __attribute__((address_space(1)))
#define RLX_AGENT __ATOMIC_RELAXED, __HIP_MEMORY_SCOPE_AGENT
#define SB() __builtin_amdgcn_sched_barrier(0)
template <int V> struct IC { static constexpr int value = V; };
template <int I, int N, class F> __device__ __forceinline__ void static_for(F&& f) { if constexpr (I < N) { f(IC<I>{}); static_for<I + 1, N>(f); } }

constexpr int L_ = 2048, D_ = 448, N_ = 256, F_ = 1792;

constexpr size_t WS_CTL = 0;
constexpr int C1_CNT = 0;
constexpr int C1_FLG = 1024;
constexpr int CW_CNT = 5120;
constexpr int CW_FLG = 5184;
constexpr int C2_CNT = 6144;
constexpr int ST2_OFF = 106496;
constexpr int CTL_BYTES = 122880;
constexpr size_t WS_MODP = 122880;
constexpr size_t WS_ST1 = 167936;
constexpr size_t WS_XT = WS_ST1 + 16384;
constexpr size_t WS_KREV = WS_XT + 64 * 448 * 64;
constexpr int KREV_STRIDE = 2064;
constexpr size_t WS_X1T = WS_KREV + 448 * KREV_STRIDE * 2;
constexpr size_t WS_XB = WS_X1T + 64 * 448 * 64;
constexpr size_t WS_W13 = WS_XB + 64 * 448 * 64;
constexpr size_t WS_W2F = WS_W13 + 112 * 28 * 1024;
constexpr size_t WS_HF = WS_W2F + 14 * 112 * 1024;
constexpr size_t WS_END = WS_HF + 64 * 112 * 1024;

DI float bflo(u32 v) { return U2F(v << 16); }
DI float bfhi(u32 v) { return U2F(v & 0xffff0000u); }
DI float bf2f(u16 v) { return U2F(((u32)v) << 16); }
DI u32 pkbf(float lo, float hi) { f32x2 p = {lo, hi}; return __builtin_bit_cast(u32, __builtin_convertvector(p, bf16x2)); }
DI u32 pkh(f32x2 p) { return __builtin_bit_cast(u32, __builtin_convertvector(p, f16x2)); }
DI float rbf(float f) { return bflo(pkbf(f, f)); }
DI float dot2(u32 a, u32 b, float c) { return __builtin_amdgcn_fdot2_f32_bf16(__builtin_bit_cast(bf16x2, a), __builtin_bit_cast(bf16x2, b), c, false); }
DI float fexp(float x) { return __builtin_amdgcn_exp2f(x * 1.4426950408889634f); }
DI float silu_bf(float c) { return rbf(c * __builtin_amdgcn_rcpf(1.f + fexp(-c))); }
template <int CTRL> DI float dppf(float x) { return I2F(__builtin_amdgcn_update_dpp(0, F2I(x), CTRL, 0xf, 0xf, false)); }
DI float sum16(float x) { x += dppf<0x128>(x); x += dppf<0x124>(x); x += dppf<0x122>(x); x += dppf<0x121>(x); return x; }
DI float max16(float x) { x = __builtin_fmaxf(x, dppf<0x128>(x)); x = __builtin_fmaxf(x, dppf<0x124>(x)); x = __builtin_fmaxf(x, dppf<0x122>(x)); x = __builtin_fmaxf(x, dppf<0x121>(x)); return x; }
DI float add32(float x) { auto r = __builtin_amdgcn_permlane32_swap(F2U(x), F2U(x), false, false); return U2F(r[0]) + U2F(r[1]); }
DI float add16(float x) { auto r = __builtin_amdgcn_permlane16_swap(F2U(x), F2U(x), false, false); return U2F(r[0]) + U2F(r[1]); }
DI float max64(float x) {
  x = max16(x);
  auto r = __builtin_amdgcn_permlane16_swap(F2U(x), F2U(x), false, false);
  x = __builtin_fmaxf(U2F(r[0]), U2F(r[1]));
  auto q = __builtin_amdgcn_permlane32_swap(F2U(x), F2U(x), false, false);
  return __builtin_fmaxf(U2F(q[0]), U2F(q[1]));
}
DI u32x2 trread(const char* p) { return __builtin_bit_cast(u32x2, __builtin_amdgcn_ds_read_tr16_b64_v4i16(LDSP(s16x4, p))); }
DI u32x2 trread3(const __attribute__((address_space(3))) char* p) { return __builtin_bit_cast(u32x2, __builtin_amdgcn_ds_read_tr16_b64_v4i16((__attribute__((address_space(3))) s16x4*)p)); }
DI void pinf(float& v) { asm volatile("" : "+v"(v)); }
DI void pin(u32x4& v) { asm volatile("" : "+v"(v)); }
DI void ostore(u32x2 v, u16* p) { *(u32x2*)p = v; }
DI float modsum(const float* modp, int j) { return rbf((modp[j] + modp[2688 + j]) + (modp[2 * 2688 + j] + modp[3 * 2688 + j])); }
DI f32x2 cmul(f32x2 a, f32x2 z) {
  f32x2 t, o;
  asm("v_pk_mul_f32 %0, %1, %2 op_sel_hi:[1,0]" : "=v"(t) : "v"(a), "v"(z));
  asm("v_pk_fma_f32 %0, %1, %2, %3 op_sel:[1,1,0] op_sel_hi:[0,1,1] neg_lo:[1,0,0]" : "=v"(o) : "v"(a), "v"(z), "v"(t));
  return o;
}

#define STAMP_DECL
#define STAMP(i)
#define CSTAMP(i)
#define STAMP_OUT(buf, slot)
#define STAMP_OUT2(buf, slot)

constexpr int K1_NC = 448, K1_NE = 112, K1_NA = 64, K1_NB = 168;
constexpr int K1_LDS = 4 * 32 * 272 + 4160;
#define K1_AUX 16
#define MFMA32(a, b, c) __builtin_amdgcn_mfma_f32_32x32x16_bf16(__builtin_bit_cast(bf16x8, a), __builtin_bit_cast(bf16x8, b), c, 0, 0, 0)
#define ST16(v, off) __builtin_amdgcn_raw_buffer_store_b128(v, rs, (int)(off), 0, K1_AUX)

__global__ __launch_bounds__(256, 4) void k1_prep(const u16* __restrict__ x, const u16* __restrict__ logA, const u16* __restrict__ Aim,
                                                  const u16* __restrict__ Cr, const u16* __restrict__ Ci, const u16* __restrict__ logdt,
                                                  const u16* __restrict__ cvec, const u16* __restrict__ wmod, const u16* __restrict__ w1,
                                                  const u16* __restrict__ w3, char* __restrict__ ws,
                                                  unsigned long long* stamps) {
  __shared__ __attribute__((aligned(16))) char smem[K1_LDS];
  const int tid = threadIdx.x, lane = tid & 63, w = __builtin_amdgcn_readfirstlane(tid >> 6);
  const int G = lane >> 4, qq = (lane & 15) >> 2, pp = lane & 3, hi = lane >> 5;
  auto rs = __builtin_amdgcn_make_buffer_rsrc(ws, 0, (int)WS_END, 0x00020000);
  int b = blockIdx.x;
  STAMP_DECL;
  STAMP(0);
  if (b < K1_NC) {
    const int d = b, n = tid;
    const float la = bf2f(logA[d * N_ + n]), aim = bf2f(Aim[d * N_ + n]);
    const float cr = bf2f(Cr[d * N_ + n]), ci = bf2f(Ci[d * N_ + n]);
    const float dt = rbf(fexp(bf2f(logdt[d])));
    const float ar = -rbf(fexp(la)) * dt * 1.4426950408889634f;
    const float ai = aim * dt;
    const float I2PH = 0.15915494f, I2PL = 6.4206382e-9f;
    const float th = ai * I2PH;
    const float tl = __builtin_fmaf(ai, I2PH, -th) + ai * I2PL;
    auto zpow = [&](float sg, float sgn) {
      float e = __builtin_amdgcn_exp2f(ar * sg);
      float rev = __builtin_amdgcn_fractf(th * sg) + tl * sg;
      return f32x2{e * __builtin_amdgcn_cosf(rev), sgn * e * __builtin_amdgcn_sinf(rev)};
    };
    const f32x2 z8c = zpow(8.f, -1.f), z16c = zpow(16.f, -1.f), z1 = zpow(1.f, 1.f), z256 = zpow(256.f, 1.f);
    float cm = max64(__builtin_fmaxf(__builtin_fabsf(cr), __builtin_fabsf(ci)));
    STAMP(2);
    u32 eb = (F2U(cm) >> 23) & 255u;
    eb = eb < 1u ? 1u : (eb > 252u ? 252u : eb);
    const float scl = U2F((253u - eb) << 23);
    const float iscl = U2F((eb + 1u) << 23);
    char* reg = smem + w * (32 * 272);
    {
      f32x2 ra = {cr * scl, -ci * scl};
      f32x2 rb = cmul(ra, z8c);
#pragma unroll
      for (int r = 0; r < 32; r += 2) {
        *(u32*)(reg + r * 272 + lane * 4) = pkh(ra);
        *(u32*)(reg + (r + 1) * 272 + lane * 4) = pkh(rb);
        if (r < 30) { ra = cmul(ra, z16c); rb = cmul(rb, z16c); }
      }
    }
    f16x8 af[8];
#pragma unroll
    for (int s = 0; s < 8; ++s) af[s] = *(const f16x8*)(reg + (lane & 31) * 272 + s * 32 + hi * 16);
    {
      f32x2 z2 = cmul(z1, z1), z512 = cmul(z256, z256), z3 = cmul(z2, z1), z4 = cmul(z2, z2), z768 = cmul(z512, z256), z1024 = cmul(z512, z512);
      f32x2 z6 = cmul(z4, z2);
      u32 col[21] = {0x00003c00u, pkh(z1), pkh(z2), pkh(z3), pkh(z4), pkh(cmul(z4, z1)), pkh(z6), pkh(cmul(z4, z3)),
                     pkh(z256), pkh(cmul(z256, z2)), pkh(cmul(z256, z4)), pkh(cmul(z256, z6)),
                     pkh(z512), pkh(cmul(z512, z4)), pkh(z768), pkh(cmul(z768, z4)),
                     pkh(z1024), pkh(cmul(z1024, z256)), pkh(cmul(z1024, z512)), pkh(cmul(z1024, z768)), pkh(cmul(z1024, z1024))};
      asm volatile("s_waitcnt lgkmcnt(0)" : "+v"(af[0]), "+v"(af[1]), "+v"(af[2]), "+v"(af[3]), "+v"(af[4]), "+v"(af[5]), "+v"(af[6]), "+v"(af[7]) :: "memory");
#pragma unroll
      for (int s = 0; s < 21; ++s) *(u32*)(reg + s * 272 + lane * 4) = col[s];
    }
    STAMP(3);
    f32x16 acc = {};
#pragma unroll
    for (int s = 0; s < 8; ++s) {
      f16x8 bfr = *(const f16x8*)(reg + (lane & 31) * 272 + s * 32 + hi * 16);
      acc = __builtin_amdgcn_mfma_f32_32x32x16_f16(af[s], bfr, acc, 0, 0, 0);
    }
    const float fs = 2.f * dt * iscl;
    asm volatile("s_waitcnt lgkmcnt(0)" ::: "memory");
#pragma unroll
    for (int qd = 0; qd < 4; ++qd)
      *(f32x4*)(reg + qd * 1024 + lane * 16) = f32x4{acc[4 * qd] * fs, acc[4 * qd + 1] * fs, acc[4 * qd + 2] * fs, acc[4 * qd + 3] * fs};
    __syncthreads();
    STAMP(4);
    u16* kp = (u16*)(smem + 4 * 32 * 272);
    {
      f32x4 t = *(const f32x4*)(smem + w * 1024 + lane * 16);
#pragma unroll
      for (int ww = 1; ww < 4; ++ww) t += *(const f32x4*)(smem + ww * (32 * 272) + w * 1024 + lane * 16);
      const int s = lane & 31;
      const int s12 = s - 12;
      int sig = s < 8 ? s : (s < 12 ? 240 + 2 * s : (s < 16 ? 512 + ((s12 >> 1) << 8) + 4 * (s & 1) : (s12 << 8)));
      const int r0 = 8 * w + 4 * hi;
      u32 a = pkbf(t[0], t[1]), c = pkbf(t[2], t[3]);
      u16* q = kp + sig + 8 * r0;
      if (s < 20 || (s == 20 && r0 == 0)) { q[0] = (u16)a; q[8] = (u16)(a >> 16); q[16] = (u16)c; q[24] = (u16)(c >> 16); }
    }
    __syncthreads();
    {
      u32 o[4];
#pragma unroll
      for (int e = 0; e < 4; ++e) {
        const int lag0 = 2047 - 8 * tid - 2 * e, lag1 = lag0 - 1;
        u32 pz = pkbf((float)lag0, (float)lag1);
        int p0 = (int)bflo(pz), p1 = (int)bfhi(pz);
        o[e] = (u32)kp[p0] | ((u32)kp[p1] << 16);
      }
      ST16((u32x4{o[0], o[1], o[2], o[3]}), WS_KREV + d * (KREV_STRIDE * 2) + 16 * tid);
      if (tid < 2) ST16((u32x4{0, 0, 0, 0}), WS_KREV + d * (KREV_STRIDE * 2) + 4096 + 16 * tid);
    }
    STAMP(1); STAMP_OUT(stamps, blockIdx.x);
    return;
  }
  b -= K1_NC;
  if (b < K1_NE) {
    const int fg = b >> 2, kq = b & 3;
    u32x4 v[7];
#pragma unroll
    for (int i = 0; i < 7; ++i) v[i] = *(const u32x4*)(((tid & 8) ? w3 : w1) + (size_t)(112 * kq + (tid >> 4) + 16 * i) * F_ + fg * 64 + (tid & 7) * 8);
#pragma unroll
    for (int i = 0; i < 7; ++i) *(u32x4*)(smem + ((tid >> 4) + 16 * i) * 272 + (tid & 15) * 16) = v[i];
    __syncthreads();
    const char* rbase = smem + (8 * hi + qq) * 272 + (G & 1) * 128 + (16 * w + 8 * (pp & 1) + 4 * (pp >> 1)) * 2;
    const int dst = (int)WS_W13 + ((4 * fg + w) * 28 + 7 * kq) * 1024 + lane * 16;
#pragma unroll
    for (int s = 0; s < 7; ++s) {
      u32x2 lo = trread(rbase + s * 16 * 272), hh = trread(rbase + (s * 16 + 4) * 272);
      ST16((u32x4{lo.x, lo.y, hh.x, hh.y}), dst + s * 1024);
    }
    STAMP(1); STAMP_OUT(stamps, blockIdx.x);
    return;
  }
  b -= K1_NE;
  if (b < K1_NA) {
    const int row = tid >> 3, p = tid & 7;
    const u32x4* src = (const u32x4*)(x + (size_t)(b * 32 + row) * D_);
    u32x4 v[7];
#pragma unroll
    for (int i = 0; i < 7; ++i) v[i] = src[p + 8 * i];
    if (b < CTL_BYTES / 4096) ST16((u32x4{0, 0, 0, 0}), WS_CTL + b * 4096 + tid * 16);
    float s = 0.f, q = 0.f;
#pragma unroll
    for (int i = 0; i < 7; ++i) {
      *(u32x4*)(smem + row * 928 + (p + 8 * i) * 16) = v[i];
#pragma unroll
      for (int j = 0; j < 4; ++j) { s = dot2(v[i][j], 0x3f803f80u, s); q = dot2(v[i][j], v[i][j], q); }
    }
    s += dppf<0xB1>(s); q += dppf<0xB1>(q); s += dppf<0x4E>(s); q += dppf<0x4E>(q); s += dppf<0x141>(s); q += dppf<0x141>(q);
    const float m = s * (1.f / D_);
    const float var = __builtin_fmaxf(q * (1.f / D_) - m * m, 0.f);
    const float sd = rbf(__builtin_amdgcn_sqrtf(rbf(rbf(var) + 1e-5f)));
    const float rsd = __builtin_amdgcn_rcpf(sd);
    if (p == 0) __builtin_amdgcn_raw_buffer_store_b64(u32x2{F2U(rsd), F2U(-rbf(m) * rsd)}, rs, (int)WS_ST1 + (b * 32 + row) * 8, 0, K1_AUX);
    __syncthreads();
#pragma unroll
    for (int i = 0; i < 7; ++i) {
      const int dg = w * 7 + i;
      const int off = (8 * G + qq) * 928 + dg * 32 + pp * 8;
      u32x2 a = trread(smem + off), c = trread(smem + off + 4 * 928);
      ST16((u32x4{a.x, a.y, c.x, c.y}), WS_XT + b * 28672 + (dg * 16 + (lane & 15)) * 64 + G * 16);
    }
    STAMP(1); STAMP_OUT(stamps, blockIdx.x);
    return;
  }
  b -= K1_NA;
  {
    const int cg = b >> 2, kq = b & 3;
    const int col0 = 64 * cg;
    const int r = tid >> 3, p = tid & 7;
    u32x4 v[4]; u16 cv[4];
#pragma unroll
    for (int i = 0; i < 4; ++i) {
      int rr = r + 32 * i; if (rr > 111) rr = 111;
      const int k = 112 * kq + rr;
      v[i] = *(const u32x4*)(wmod + (size_t)k * 2688 + col0 + p * 8);
      cv[i] = cvec[k];
    }
    float acc[8] = {0, 0, 0, 0, 0, 0, 0, 0};
#pragma unroll
    for (int i = 0; i < 4; ++i) {
      u32 sh = F2U(silu_bf(bf2f(cv[i])));
      if (r + 32 * i > 111) sh = 0u;
      const u32 sl = sh >> 16;
#pragma unroll
      for (int j = 0; j < 4; ++j) { acc[2 * j] = dot2(v[i][j], sl, acc[2 * j]); acc[2 * j + 1] = dot2(v[i][j], sh, acc[2 * j + 1]); }
    }
    float* red = (float*)smem;
#pragma unroll
    for (int j = 0; j < 8; ++j) { acc[j] += dppf<0x128>(acc[j]); acc[j] = add32(acc[j]); }
    if ((lane & 40) == 0) {
      const int slot = w * 2 + (lane >> 4);
      *(f32x4*)(red + slot * 64 + p * 8) = f32x4{acc[0], acc[1], acc[2], acc[3]};
      *(f32x4*)(red + slot * 64 + p * 8 + 4) = f32x4{acc[4], acc[5], acc[6], acc[7]};
    }
    __syncthreads();
    if (tid < 64) {
      float t = 0.f;
#pragma unroll
      for (int j = 0; j < 8; ++j) t += red[j * 64 + tid];
      __builtin_amdgcn_raw_buffer_store_b32(F2U(t), rs, (int)WS_MODP + (kq * 2688 + col0 + tid) * 4, 0, K1_AUX);
    }
    STAMP(1); STAMP_OUT(stamps, blockIdx.x);
  }
}

template <int IMM> DI u32x4 gload(u32 voff, const char* sbase) {
  u32x4 r; asm volatile("global_load_dwordx4 %0, %1, %2 offset:%3 sc1" : "=v"(r) : "v"(voff), "s"(sbase), "i"(IMM)); return r;
}
DI void glds16(u32 voff, const char* sbase, u32 ldsaddr) {
  asm volatile("s_mov_b32 m0, %2\n\ts_nop 0\n\tglobal_load_lds_dwordx4 %0, %1 sc1" :: "v"(voff), "s"(sbase), "s"(ldsaddr) : "memory");
}
constexpr unsigned SPIN_CAP = 1u << 16;
DI void sgload(unsigned long long& g, GAS unsigned long long* p) { asm volatile("global_load_dwordx2 %0, %1, off sc1" : "+v"(g) : "v"(p) : "memory"); }
DI void sgwait(unsigned long long& g) { asm volatile("s_waitcnt vmcnt(1)" : "+v"(g) :: "memory"); }
DI void sgreload(unsigned long long& g, GAS unsigned long long* p) { asm volatile("global_load_dwordx2 %0, %1, off sc1\n\ts_waitcnt vmcnt(0)" : "+v"(g) : "v"(p) : "memory"); }
DI void arrive1(GAS u32* ctl, int b, int lane) {
  u32 old = 0;
  if (lane == 0) old = __hip_atomic_fetch_add(ctl + C1_CNT + (b & 15) * 64, 1u, RLX_AGENT);
  old = __builtin_amdgcn_readfirstlane(old);
  if (old == 13u && lane < 8) __hip_atomic_store(ctl + C1_FLG + (lane * 16 + (b & 15)) * 32, 1u, RLX_AGENT);
}
DI void wait1(GAS u32* ctl, int b, int lane) {
#pragma unroll 1
  for (unsigned spins = 0; spins < SPIN_CAP; ++spins) {
    const u32 pv = __hip_atomic_load(ctl + C1_FLG + ((b & 7) * 16 + (lane & 15)) * 32, RLX_AGENT);
    if (__builtin_amdgcn_ballot_w64(pv == 0u) == 0) break;
  }
}
DI void wait2(GAS u32* p, u32 want) {
#pragma unroll 1
  for (unsigned spins = 0; spins < SPIN_CAP; ++spins) {
    if (__builtin_amdgcn_ballot_w64(__hip_atomic_load(p, RLX_AGENT) < want) == 0) break;
  }
}
DI f32x16 mfma_to(u32x4 a, u32x4 b, f32x16 c) { f32x16 d; asm volatile("v_mfma_f32_32x32x16_bf16 %0, %1, %2, %3" : "=&v"(d) : "v"(a), "v"(b), "v"(c)); return d; }
#define WAIT_VM0() __builtin_amdgcn_s_waitcnt(0x0F70)
#define BAR() __builtin_amdgcn_s_barrier()

constexpr int CV_DSTR = 4 * 4160 + 4608;
constexpr int CV_RED = 2 * CV_DSTR;
constexpr int G1_ST = 4 * 28672;
constexpr int G2_RED = 4 * 28672;
constexpr int G2_X1 = 18 * 8192;
constexpr int TAB_GB = G2_X1 + 8192;
constexpr int TAB_SB = TAB_GB + 256;
constexpr int KF_LDS = TAB_SB + 1792;
#define W2_N0 13
#define EPI0 7
#define W_N0 4
#define W_N2 (W_N0 + 10)
#define SC_SIG 6
#define SC_POLL 21
#define LAS __attribute__((address_space(3)))
#define PIN14(a, o) asm volatile("" : "+v"(a[o]), "+v"(a[o + 1]), "+v"(a[o + 2]), "+v"(a[o + 3]), "+v"(a[o + 4]), "+v"(a[o + 5]), "+v"(a[o + 6]), "+v"(a[o + 7]), \
    "+v"(a[o + 8]), "+v"(a[o + 9]), "+v"(a[o + 10]), "+v"(a[o + 11]), "+v"(a[o + 12]), "+v"(a[o + 13]) :: "memory")

constexpr int g1_w2(int n) {
  if (n >= 27 && n < 27 + 13 * 4 && (n - 27) % 4 == 0) return (n - 27) / 4;
  if (n >= 84 && n < 84 + 23) { const int q = n - 84; if (q % 3 != 2) return 13 + q - q / 3 < 28 ? 13 + q - q / 3 : -1; }
  return -1;
}
constexpr int g1_sg(int t) { return t == 0 ? 24 : 28 * t + 10; }
constexpr int g1_vm(int a, int b) {
  int c = 0;
  for (int n = a; n < b; ++n) {
    const int t = n / 28, q = n % 28;
    if (n > a) { if (t >= 2 && q == 22) ++c; if (n == g1_sg(t)) ++c; }
    if (t >= 1 && q == 26 && n != a) ++c;
    if (g1_w2(n) >= 0) ++c;
  }
  return c;
}
NOPK __global__ __launch_bounds__(512) void kf_main(const u16* __restrict__ Dsk, const u16* __restrict__ w2, char* __restrict__ ws, u16* __restrict__ out, unsigned long long* stamps) {
  __shared__ __attribute__((aligned(16))) char smem[KF_LDS];
  const int tid = __builtin_amdgcn_workitem_id_x(), lane = tid & 63;
  const int w = __builtin_amdgcn_readfirstlane(tid >> 6);
  const int b = __builtin_amdgcn_workgroup_id_x(), xcd = b & 7, idx = b >> 3;
  const int hi = lane >> 5;
  const int G = lane >> 4, qq = (lane & 15) >> 2, pp = lane & 3;
  const u32 voff = lane * 16;
  const float* modp = (const float*)(ws + WS_MODP);
  GAS u32* ctl = (GAS u32*)(ws + WS_CTL);
  auto rs = __builtin_amdgcn_make_buffer_rsrc(ws, 0, (int)WS_END, 0x00020000);
  const u32 lds0 = (u32)(size_t)LDSP(char, smem);
  STAMP_DECL;
  STAMP(0);
  if (b >= 224) {
    const int fr = b - 224;
    if (fr < 28) {
      u32x4 v[7];
#pragma unroll
      for (int i = 0; i < 7; ++i) v[i] = *(const u32x4*)(w2 + (size_t)(64 * fr + (tid >> 3)) * D_ + ((tid & 7) + 8 * i) * 8);
#pragma unroll
      for (int i = 0; i < 7; ++i) *(u32x4*)(smem + (tid >> 3) * 912 + ((tid & 7) + 8 * i) * 16) = v[i];
      asm volatile("s_waitcnt lgkmcnt(0)\n\ts_barrier" ::: "memory");
      const char* rbase = smem + (16 * (w & 3) + 8 * hi + qq) * 912 + (w >> 2) * 448 + (16 * (G & 1) + 4 * pp) * 2;
      const int dst = (int)WS_W2F + ((7 * (w >> 2)) * 112 + 4 * fr + (w & 3)) * 1024 + lane * 16;
#pragma unroll
      for (int s = 0; s < 7; ++s) {
        u32x2 lo = trread(rbase + s * 64), hh = trread(rbase + s * 64 + 4 * 912);
        __builtin_amdgcn_raw_buffer_store_b128(u32x4{lo.x, lo.y, hh.x, hh.y}, rs, dst + s * 112 * 1024, 0, 16);
      }
    }
    asm volatile("s_waitcnt vmcnt(0)\n\ts_barrier" ::: "memory");
    if (w == 0) {
      u32 old = 0;
      if (lane == 0) old = __hip_atomic_fetch_add(ctl + CW_CNT, 1u, RLX_AGENT);
      old = __builtin_amdgcn_readfirstlane(old);
      if (old == 31u && lane < 8) __hip_atomic_store(ctl + CW_FLG + lane * 32, 1u, RLX_AGENT);
      wait1(ctl, b, lane);
    }
    asm volatile("s_barrier" ::: "memory");
    {
      const char* xsrc = ws + WS_X1T + (size_t)(2 * fr + (w >> 2)) * 28672 + (w & 3) * 1024;
#pragma unroll
      for (int i = 0; i < 7; ++i) glds16(voff, xsrc + i * 4096, lds0 + w * 7168 + i * 1024);
      const char* sp = smem + w * 7168 + (8 * hi + qq) * 64 + (G & 1) * 32 + pp * 8;
      float s0 = 0.f, q0 = 0.f, s1 = 0.f, q1 = 0.f;
      static_for<0, 7>([&](auto ic) __attribute__((always_inline)) {
        constexpr int i = decltype(ic)::value;
        asm volatile("s_waitcnt vmcnt(%0)" :: "i"(6 - i) : "memory");
        const u32x2 lo = trread(sp + i * 1024), hh = trread(sp + i * 1024 + 256);
        s0 = dot2(lo.x, 0x3f803f80u, s0); q0 = dot2(lo.x, lo.x, q0); s1 = dot2(lo.y, 0x3f803f80u, s1); q1 = dot2(lo.y, lo.y, q1);
        s0 = dot2(hh.x, 0x3f803f80u, s0); q0 = dot2(hh.x, hh.x, q0); s1 = dot2(hh.y, 0x3f803f80u, s1); q1 = dot2(hh.y, hh.y, q1);
      });
      auto r = __builtin_amdgcn_permlane32_swap(F2U(s0 + s1), F2U(q0 + q1), false, false);
      *(float*)(smem + 65536 + w * 256 + (lane & 31) * 8 + hi * 4) = U2F(r[0]) + U2F(r[1]);
      asm volatile("s_waitcnt lgkmcnt(0)\n\ts_barrier" ::: "memory");
      if (w == 0) {
        const char* pt = smem + 65536 + hi * 1024 + (lane & 31) * 8;
        const f32x2 ts = (*(const f32x2*)pt + *(const f32x2*)(pt + 256)) + (*(const f32x2*)(pt + 512) + *(const f32x2*)(pt + 768));
        const float m = ts[0] * (1.f / D_);
        const float var = __builtin_fmaxf(ts[1] * (1.f / D_) - m * m, 0.f);
        const float sd = __builtin_amdgcn_sqrtf(rbf(rbf(var) + 1e-5f));
        __hip_atomic_store((GAS unsigned long long*)(ws + ST2_OFF) + 64 * fr + lane, (1ull << 32) | pkbf(-m, sd), RLX_AGENT);
      }
    }
    return;
  }
  const int rt = 4 * (xcd >> 1) + idx / 7, ct = 7 * (xcd & 1) + idx % 7;
  const int ft = 8 * ct + w;
  const int r2 = 4 * xcd + idx / 7, dg = idx % 7, kq = w >> 1, dt = w & 1;
  const int te = 4 * (r2 >> 1) + (r2 & 1);
  const int wv = (int)WS_W13 + ft * 28672 + lane * 16;
#define WLOAD(p) wf[p] = __builtin_amdgcn_raw_buffer_load_b128(rs, wv + ((p) & 3) * 1024, ((p) & ~3) * 1024, 0)

  u32x4 wf[28];
  u32x4 wex;

  {
    const int dl = w >> 2, q4 = w & 3;
    const int a = lane & 15;
    const int t256 = tid & 255;
    const int l0 = 256 * w + 16 * a + 4 * G;
    const int xoff = ((l0 >> 5) * 448 + 2 * b) * 64 + (l0 & 31) * 2;
    f32x2 ms[16];
#pragma unroll
    for (int i = 0; i < 16; ++i) ms[i] = *(const f32x2*)(modp + (i & 3) * 2688 + ((i >> 2) + (i >= 12)) * D_ + 2 * b);
    const u32 dskr = *(const u32*)(Dsk + 2 * b);
    u32x4 k0 = __builtin_amdgcn_raw_buffer_load_b128(rs, (int)WS_KREV + (2 * b + dl) * (KREV_STRIDE * 2) + 16 * t256, 0, 0);
    u32x2 k1 = __builtin_amdgcn_raw_buffer_load_b64(rs, (int)WS_KREV + (2 * b + dl) * (KREV_STRIDE * 2) + 16 * t256 + 16, 0, 0);
    u32x2 xx[2]; u32x4 st[2];
#pragma unroll
    for (int i = 0; i < 2; ++i) {
      xx[i] = __builtin_amdgcn_raw_buffer_load_b64(rs, (int)WS_XT + xoff + i * 64, 0, 0);
      st[i] = __builtin_amdgcn_raw_buffer_load_b128(rs, (int)WS_ST1 + l0 * 8 + i * 16, 0, 0);
    }
    SB();
    asm volatile("s_barrier" ::: "memory");
    SB();
    float tb[8];
#pragma unroll
    for (int i = 0; i < 8; ++i)
      tb[i] = U2F(__builtin_amdgcn_raw_buffer_load_b32(rs, (int)WS_MODP + (tid < 448 ? 3 * D_ + tid : 4 * D_ + 64 * dg + tid) * 4, ((i & 3) * 2688 + (i >> 2) * D_) * 4, 0));
#pragma unroll
    for (int i = 0; i < W_N0; ++i) WLOAD(i);
    SB();
    STAMP(8);
    {
      char* cp = smem + dl * CV_DSTR;
      if (t256 < 32) *(u32x4*)(cp + 4 * 4160 + t256 * 16) = u32x4{0, 0, 0, 0};
      else if (t256 < 40) *(u32x4*)(cp + ((t256 - 32) >> 1) * 4160 + 4096 + (t256 & 1) * 16) = u32x4{0, 0, 0, 0};
      u32 al0 = __builtin_amdgcn_alignbit(k0.y, k0.x, 16), al1 = __builtin_amdgcn_alignbit(k0.z, k0.y, 16), al2 = __builtin_amdgcn_alignbit(k0.w, k0.z, 16),
          al3 = __builtin_amdgcn_alignbit(k1.x, k0.w, 16), al4 = __builtin_amdgcn_alignbit(k1.y, k1.x, 16);
      char* q = cp + t256 * 16;
      *(u32x4*)(q) = k0;
      *(u32x4*)(q + 4160) = u32x4{al0, al1, al2, al3};
      *(u32x4*)(q + 8320) = u32x4{k0.y, k0.z, k0.w, k1.x};
      *(u32x4*)(q + 12480) = u32x4{al1, al2, al3, al4};
    }
    f32x4 base[2]; float ga[2];
#pragma unroll
    for (int i = 0; i < 2; ++i) {
      const float sa = rbf((ms[0][i] + ms[1][i]) + (ms[2][i] + ms[3][i])), beta = rbf(1.f + rbf((ms[4][i] + ms[5][i]) + (ms[6][i] + ms[7][i])));
      ga[i] = rbf((ms[8][i] + ms[9][i]) + (ms[10][i] + ms[11][i]));
      const float gd = ga[i] * (i ? bfhi(dskr) : bflo(dskr));
      const float xv[4] = {bflo(xx[i].x), bfhi(xx[i].x), bflo(xx[i].y), bfhi(xx[i].y)};
      float hv[4];
#pragma unroll
      for (int e = 0; e < 4; ++e)
        hv[e] = __builtin_fmaf(__builtin_fmaf(xv[e], U2F(st[e >> 1][2 * (e & 1)]), U2F(st[e >> 1][2 * (e & 1) + 1])), beta, sa);
      *(u32x2*)(smem + i * CV_DSTR + 4 * 4160 + 512 + l0 * 2) = u32x2{pkbf(hv[0], hv[1]), pkbf(hv[2], hv[3])};
#pragma unroll
      for (int e = 0; e < 4; ++e) base[i][e] = __builtin_fmaf(hv[e], gd, xv[e]);
    }
    asm volatile("s_waitcnt lgkmcnt(0)\n\ts_barrier" ::: "memory");
    STAMP(1);
    f32x4 acc[8];
#pragma unroll
    for (int t = 0; t < 8; ++t) acc[t] = f32x4{0, 0, 0, 0};
    const int im = 15 - a;
    const u32 abase = lds0 + dl * CV_DSTR + (im & 3) * 4160 + (2016 + 8 * G + 4 * (im >> 2)) * 2 - q4 * 128 - 3648;
    const u32 bbase = lds0 + dl * CV_DSTR + 4 * 4160 + (256 - 16 + 16 * a + 8 * G) * 2 - q4 * 128 - 64;
    char* dump = smem + CV_RED + (dl * 32 + q4) * 1024 + lane * 16;
#pragma unroll
    for (int cc = 0; cc < 2; ++cc) {
      u32x2 alo[8], ahi[8]; u32x4 bfr[8];
#pragma unroll
      for (int c1 = 0; c1 < 8; ++c1)
        asm volatile("ds_read_b64 %0, %3 offset:%5\n\tds_read_b64 %1, %3 offset:%6\n\tds_read_b128 %2, %4 offset:%7"
                     : "=&v"(alo[c1]), "=&v"(ahi[c1]), "=&v"(bfr[c1])
                     : "v"(abase), "v"(bbase), "i"(3648 - (8 * c1 + cc) * 64), "i"(3656 - (8 * c1 + cc) * 64), "i"(64 + (8 * c1 - cc) * 64) : "memory");
      SB();
#pragma unroll
      for (int t = 0; t < 8; ++t) {
        if (t == 0) asm volatile("s_waitcnt lgkmcnt(15)" : "+v"(alo[0]), "+v"(ahi[0]), "+v"(bfr[0]), "+v"(alo[1]), "+v"(ahi[1]), "+v"(bfr[1]), "+v"(alo[2]), "+v"(ahi[2]), "+v"(bfr[2]));
        if (t == 3) asm volatile("s_waitcnt lgkmcnt(9)" : "+v"(alo[3]), "+v"(ahi[3]), "+v"(bfr[3]), "+v"(alo[4]), "+v"(ahi[4]), "+v"(bfr[4]));
        if (t == 5) asm volatile("s_waitcnt lgkmcnt(0)" : "+v"(alo[5]), "+v"(ahi[5]), "+v"(bfr[5]), "+v"(alo[6]), "+v"(ahi[6]), "+v"(bfr[6]), "+v"(alo[7]), "+v"(ahi[7]), "+v"(bfr[7]));
#pragma unroll
        for (int c1 = 0; c1 <= t; ++c1) {
          acc[t] = __builtin_amdgcn_mfma_f32_16x16x32_bf16(__builtin_bit_cast(bf16x8, u32x4{alo[c1].x, alo[c1].y, ahi[c1].x, ahi[c1].y}),
                                                           __builtin_bit_cast(bf16x8, bfr[t - c1]), acc[t], 0, 0, 0);
          const int n = t * (t + 1) / 2 + c1;
          if ((n % 6) == 5 && W_N0 + 6 * cc + n / 6 < W_N2) { SB(); WLOAD(W_N0 + 6 * cc + n / 6); SB(); }
          if (cc == 1 && t >= 5 && c1 >= 1) {
            const int u = (t == 5 ? c1 - 1 : (t == 6 ? (c1 == 1 ? 5 : -1) : (c1 == 1 ? 6 : -1)));
            if (u >= 0) { SB(); *(f32x4*)(dump + u * 4096) = acc[u]; SB(); }
          }
        }
        SB();
      }
    }
    *(f32x4*)(dump + 7 * 4096) = acc[7];
    STAMP(2);
    if (tid < 448) {
      const float be = rbf(1.f + rbf((tb[4] + tb[5]) + (tb[6] + tb[7])));
      *(u16*)(smem + TAB_SB + tid * 2) = (u16)pkbf((tb[0] + tb[1]) + (tb[2] + tb[3]), 0.f);
      *(u16*)(smem + TAB_SB + 896 + tid * 2) = (u16)(F2U(be) >> 16);
    } else {
      *(float*)(smem + TAB_GB + (tid - 448) * 4) = rbf((tb[0] + tb[1]) + (tb[2] + tb[3]));
    }
    asm volatile("s_waitcnt lgkmcnt(0)\n\ts_barrier" ::: "memory");
#pragma unroll
    for (int i = 0; i < 2; ++i) {
      const char* rp = smem + CV_RED + (i * 32 + w * 4) * 1024 + lane * 16;
      const f32x4 y = (*(const f32x4*)rp + *(const f32x4*)(rp + 1024)) + (*(const f32x4*)(rp + 2048) + *(const f32x4*)(rp + 3072));
      u32x2 o; o.x = pkbf(__builtin_fmaf(y[0], ga[i], base[i][0]), __builtin_fmaf(y[1], ga[i], base[i][1]));
      o.y = pkbf(__builtin_fmaf(y[2], ga[i], base[i][2]), __builtin_fmaf(y[3], ga[i], base[i][3]));
      const float b2 = rbf(1.f + rbf((ms[12][i] + ms[13][i]) + (ms[14][i] + ms[15][i])));
      u32x2 ob; ob.x = pkbf(bflo(o.x) * b2, bfhi(o.x) * b2); ob.y = pkbf(bflo(o.y) * b2, bfhi(o.y) * b2);
      __builtin_amdgcn_raw_buffer_store_b64(ob, rs, (int)WS_XB + xoff + i * 64, 0, 16);
      __builtin_amdgcn_raw_buffer_store_b64(o, rs, (int)WS_X1T + xoff + i * 64, 0, 16);
    }
    SB();
    STAMP(10);
  }
  u32x4 w2f[28];
  const char* w2src = ws + WS_W2F + ((size_t)(2 * dg + dt) * 112 + 28 * kq) * 1024;
#define W2LOAD(lo, hi_) static_for<lo, hi_>([&](auto ic) __attribute__((always_inline)) { constexpr int i = decltype(ic)::value; w2f[i] = gload<(i & 3) * 1024>(voff, w2src + (i & ~3) * 1024); })
  WAIT_VM0();
  asm volatile("s_waitcnt vmcnt(0)\n\ts_barrier" ::: "memory");
  STAMP(3);
  if (w == 7) arrive1(ctl, b, lane);
  SB();
#pragma unroll
  for (int i = W_N2; i < 28; ++i) WLOAD(i);
  SB();
  {
    f32x16 accw;
#pragma unroll
    for (int r = 0; r < 16; ++r) accw[r] = 0.f;
    const char* tsb = smem + TAB_SB + (G & 1) * 896 + hi * 16;
#pragma unroll
    for (int p = 0; p < 28; ++p) accw = MFMA32(*(const u32x4*)(tsb + p * 32), wf[p], accw);
    const float us = accw[8], vs = accw[0];
    const float uh = rbf(us), vh = rbf(vs);
    wex = u32x4{hi ? 0u : pkbf(uh, us - uh), hi ? 0u : pkbf(vh, vs - vh), 0u, 0u};
    pin(wex);
  }
  WAIT_VM0();
  STAMP(11);
  if (w == 0) wait1(ctl, b, lane);
  if (w == 1) wait2(ctl + CW_FLG + xcd * 32, 1u);
  asm volatile("s_barrier" ::: "memory");
  STAMP(4);
  {
    const char* xsrc = ws + WS_XB + (size_t)(4 * rt) * 28672 + w * 1024;
#pragma unroll
    for (int i = 0; i < 14; ++i) glds16(voff, xsrc + i * 8192, lds0 + w * 1024 + i * 8192);
    glds16(voff, ws + WS_X1T + ((size_t)(te + 2 * (w >> 2)) * 448 + 64 * dg) * 64 + (w & 3) * 1024, lds0 + G2_X1 + w * 1024);
  }
  {
    u32 rb = lds0 + (8 * hi + qq) * 64 + (G & 1) * 32 + pp * 8;
    const int hbase = (int)WS_HF + (4 * rt * 112 + ft) * 1024 + lane * 16;
    GAS unsigned long long* sgp = (GAS unsigned long long*)(ws + ST2_OFF) + 128 * rt + (lane & 31);
    GAS u32* c2p = ctl + C2_CNT + rt * 1280 + w * 32;
    u32x4 xf[4];
    f32x16 acc[2];
    float nal, s2, ex[8], pr[8];
    unsigned long long sg = 0;
#define G1_READ(q) do { const LAS char* p_ = (const LAS char*)(size_t)rb + (q) * 1024; u32x2 lo = trread3(p_), hh = trread3(p_ + 256); xf[(q) & 3] = u32x4{lo.x, lo.y, hh.x, hh.y}; } while (0)
#define SG_WAIT(g, ptr) do { _Pragma("unroll 1") for (unsigned spins = 0; spins < SPIN_CAP; ++spins) { if (__builtin_amdgcn_ballot_w64((u32)((g) >> 32) != 1u) == 0) break; sgreload(g, ptr); } } while (0)
#define SG_APPLY(g, A) do { const u32 st_ = (u32)(g); const u32 mm_ = __builtin_amdgcn_perm(st_, st_, 0x01000100u), ss_ = __builtin_amdgcn_perm(st_, st_, 0x03020302u); \
      const float sd_ = bfhi(st_); nal = -1.4426950408889634f * __builtin_amdgcn_rcpf(sd_); s2 = sd_ * sd_; \
      pinf(nal); pinf(s2); A = MFMA32(wex, (u32x4{mm_, ss_, mm_, ss_}), A); } while (0)
#define EPI_STAGE(A, st, v) do { if constexpr ((st) == 0) { ex[v] = A[v] * nal; pr[v] = A[v] * A[8 + (v)]; pinf(pr[v]); } \
      if constexpr ((st) == 1) ex[v] = __builtin_amdgcn_exp2f(ex[v]); if constexpr ((st) == 2) ex[v] = __builtin_fmaf(ex[v], s2, s2); \
      if constexpr ((st) == 3) ex[v] = __builtin_amdgcn_rcpf(ex[v]); if constexpr ((st) == 4) { pr[v] *= ex[v]; pinf(pr[v]); } else pinf(ex[v]); } while (0)
#define EPI_STORE(tt) __builtin_amdgcn_raw_buffer_store_b128(u32x4{pkbf(pr[0], pr[1]), pkbf(pr[2], pr[3]), pkbf(pr[4], pr[5]), pkbf(pr[6], pr[7])}, rs, hbase, (tt) * 112 * 1024, 16)
    asm volatile("s_waitcnt vmcnt(11)\n\ts_barrier" ::: "memory");
    STAMP(12);
    G1_READ(0); G1_READ(1);
    static_for<0, 112>([&](auto nc) __attribute__((always_inline)) {
      constexpr int n = decltype(nc)::value, t = n / 28, q = n % 28;
      constexpr int wl = g1_w2(n);
      if constexpr (t >= 1 && q == EPI0 - 2) {
        asm volatile("s_waitcnt vmcnt(%1)" : "+v"(sg) : "i"(g1_vm(g1_sg(t - 1), n)) : "memory");
        SG_WAIT(sg, sgp + 32 * (t - 1));
      }
      if constexpr (n == 24) asm volatile("s_waitcnt vmcnt(0)\n\ts_barrier" ::: "memory");
      if constexpr (t >= 2 && q == 22) {
        asm volatile("s_waitcnt vmcnt(%3)\n\ts_mov_b64 exec, 1\n\tglobal_atomic_add %0, %1, %2\n\ts_mov_b64 exec, -1"
                     :: "v"(0), "v"(1), "s"(c2p + (t - 2) * 256), "i"(g1_vm(28 * (t - 1) + 26, n)) : "memory");
      }
      if constexpr (n == g1_sg(t)) sgload(sg, sgp + 32 * t);
      if constexpr (q == 26 && (t & 1)) { rb += 57344; asm volatile("" : "+v"(rb)); }
      SB();
      if constexpr (n < 110) G1_READ((n + 2) % 56);
      SB();
      if constexpr (q == 0) { _Pragma("unroll") for (int r = 0; r < 16; ++r) acc[t & 1][r] = 0.f; }
      acc[t & 1] = MFMA32(wf[q], xf[q & 3], acc[t & 1]);
      if constexpr (t >= 1) {
        if constexpr (q == EPI0 - 2) SG_APPLY(sg, acc[(t - 1) & 1]);
        static_for<0, 5>([&](auto sc) __attribute__((always_inline)) {
          constexpr int st = decltype(sc)::value, g = q - EPI0 - st, v = g / 2;
          if constexpr (g >= 0 && (g & 1) == 0 && v < 8) EPI_STAGE(acc[(t - 1) & 1], st, v);
        });
        if constexpr (q == 26) { SB(); EPI_STORE(t - 1); }
      }
      if constexpr (wl >= 0) { SB(); w2f[wl] = gload<(wl & 3) * 1024>(voff, w2src + (wl & ~3) * 1024); }
    });
    SB();
    STAMP(5);
    asm volatile("s_waitcnt vmcnt(%1)" : "+v"(sg) : "i"(g1_vm(g1_sg(3), 112)) : "memory");
    SG_WAIT(sg, sgp + 96);
    SG_APPLY(sg, acc[1]);
    static_for<0, 5>([&](auto sc) __attribute__((always_inline)) {
      static_for<0, 8>([&](auto vc) __attribute__((always_inline)) { EPI_STAGE(acc[1], decltype(sc)::value, decltype(vc)::value); });
    });
    EPI_STORE(3);
    SB();
    WAIT_VM0();
    asm volatile("s_waitcnt vmcnt(0)" ::: "memory");
    if (lane < 2) __hip_atomic_fetch_add(c2p + 512 + lane * 256, 1u, RLX_AGENT);
    SB();
  }
  GAS u32* c2w = ctl + C2_CNT + (r2 >> 1) * 1280 + (r2 & 1) * 256 + (lane & 7) * 32;
  if (w == 0) wait2(c2w, 14u);
  asm volatile("s_waitcnt lgkmcnt(0)\n\ts_barrier" ::: "memory");
  STAMP(6);
  {
    const char* hsrc = ws + WS_HF + ((size_t)te * 112 + 28 * kq + dt) * 1024;
    const u32 hdst = lds0 + (dt * 4 + kq) * 1024;
#pragma unroll
    for (int i = 0; i < 14; ++i) glds16(voff, hsrc + i * 2048, hdst + i * 8192);
    const f32x4 gbv = *(const f32x4*)(smem + TAB_GB + (32 * dt + 8 * kq + 4 * hi) * 4);
    u32 pv = 0;
    const char* hb = smem + kq * 1024 + lane * 16;
    char* red = smem + G2_RED + dt * 12288;
#pragma unroll 1
    for (int j = 0; j < 2; ++j) {
      f32x16 acc;
#pragma unroll
      for (int r = 0; r < 16; ++r) acc[r] = 0.f;
      u32x4 hfr[4];
      static_for<0, 28>([&](auto sc) __attribute__((always_inline)) {
        constexpr int s = decltype(sc)::value;
        if constexpr (s == 0) { asm volatile("s_waitcnt vmcnt(10)\n\ts_barrier" ::: "memory"); PIN14(w2f, 0); PIN14(w2f, 14);
          hfr[0] = *(const u32x4*)(hb); hfr[1] = *(const u32x4*)(hb + 4096); hfr[2] = *(const u32x4*)(hb + 8192); }
        if constexpr (s == 5) asm volatile("s_waitcnt vmcnt(6)\n\ts_barrier" ::: "memory");
        if constexpr (s == 13) asm volatile("s_waitcnt vmcnt(3)\n\ts_barrier\n\tglobal_load_dword %0, %1, off offset:2048 sc1" : "+v"(pv) : "v"(c2w) : "memory");
        if constexpr (s == 19) asm volatile("s_waitcnt vmcnt(1)\n\ts_barrier" ::: "memory");
        SB();
        if constexpr (s + 3 < 28) hfr[(s + 3) & 3] = *(const u32x4*)(hb + (s + 3) * 4096);
        SB();
        acc = MFMA32(w2f[s], hfr[s & 3], acc);
      });
      SB();
      if (j == 0) {
        STAMP(14);
        asm volatile("s_waitcnt vmcnt(0)" : "+v"(pv) :: "memory");
        if (__builtin_amdgcn_ballot_w64(pv < 14u) != 0) wait2(c2w + 512, 14u);
        asm volatile("s_waitcnt vmcnt(0) lgkmcnt(0)\n\ts_barrier" ::: "memory");
        STAMP(9);
#pragma unroll
        for (int i = 0; i < 14; ++i) glds16(voff, hsrc + 2 * 112 * 1024 + i * 2048, hdst + i * 8192);
      } else {
        STAMP(7);
      }
      f32x4 y;
#pragma unroll
      for (int P = 0; P < 4; ++P) {
        f32x4 a0 = {acc[4 * P], acc[4 * P + 1], acc[4 * P + 2], acc[4 * P + 3]};
        if (P != kq) *(f32x4*)(red + (P * 3 + (kq < P ? kq : kq - 1)) * 1024 + lane * 16) = a0;
        else y = a0;
      }
      asm volatile("s_waitcnt lgkmcnt(0)\n\ts_barrier" ::: "memory");
#pragma unroll
      for (int s = 0; s < 3; ++s) y += *(const f32x4*)(red + (kq * 3 + s) * 1024 + lane * 16);
      const u32x2 xr = trread(smem + G2_X1 + j * 4096 + (32 * dt + 8 * kq + 4 * hi + qq) * 64 + (G & 1) * 32 + pp * 8);
      u32x2 o; o.x = pkbf(__builtin_fmaf(y[0], gbv[0], bflo(xr.x)), __builtin_fmaf(y[1], gbv[1], bfhi(xr.x)));
      o.y = pkbf(__builtin_fmaf(y[2], gbv[2], bflo(xr.y)), __builtin_fmaf(y[3], gbv[3], bfhi(xr.y)));
      ostore(o, out + (size_t)(32 * (te + 2 * j) + (lane & 31)) * D_ + 64 * dg + 32 * dt + 8 * kq + 4 * hi);
    }
  }
  STAMP(15);
  STAMP_OUT(stamps, 1024 + b);
  STAMP_OUT2(stamps, 2048 + b);
}

extern "C" void kernel_launch(void* const* d_in, const int* in_sizes, int n_in, void* d_out, int out_size, void* d_ws, size_t ws_size,
                              hipStream_t stream) {
  const u16* x = (const u16*)d_in[0]; const u16* logA = (const u16*)d_in[1]; const u16* Aim = (const u16*)d_in[2];
  const u16* Cr = (const u16*)d_in[3]; const u16* Ci = (const u16*)d_in[4]; const u16* logdt = (const u16*)d_in[5]; const u16* Dsk = (const u16*)d_in[6];
  const u16* w1 = (const u16*)d_in[8]; const u16* w3 = (const u16*)d_in[9]; const u16* w2 = (const u16*)d_in[10];
  const u16* c = (const u16*)d_in[11]; const u16* wm = (const u16*)d_in[12];
  char* ws = (char*)d_ws;
  unsigned long long* stamps = (unsigned long long*)(ws + (64u << 20));
  k1_prep<<<K1_NE + K1_NC + K1_NA + K1_NB, 256, 0, stream>>>(x, logA, Aim, Cr, Ci, logdt, c, wm, w1, w3, ws, stamps);
  kf_main<<<256, 512, 0, stream>>>(Dsk, w2, ws, (u16*)d_out, stamps);
}
```

```cpp
#include <hip/hip_runtime.h>

typedef unsigned short u16;
typedef unsigned u32;
typedef u32 u32x4 __attribute__((ext_vector_type(4)));
typedef u32 u32x2 __attribute__((ext_vector_type(2)));
typedef short s16x4 __attribute__((ext_vector_type(4)));
typedef __bf16 bf16x8 __attribute__((ext_vector_type(8)));
typedef __bf16 bf16x2 __attribute__((ext_vector_type(2)));
typedef _Float16 f16x8 __attribute__((ext_vector_type(8)));
typedef _Float16 f16x2 __attribute__((ext_vector_type(2)));
typedef float f32x2 __attribute__((ext_vector_type(2)));
typedef float f32x4 __attribute__((ext_vector_type(4)));
typedef float f32x16 __attribute__((ext_vector_type(16)));
#define DI __device__ __forceinline__
#define U2F(x) __builtin_bit_cast(float, (u32)(x))
#define F2U(x) __builtin_bit_cast(u32, (float)(x))
#define I2F(x) __builtin_bit_cast(float, (int)(x))
#define F2I(x) __builtin_bit_cast(int, (float)(x))
#if defined(__HIP_DEVICE_COMPILE__)
#define NOPK __attribute__((target("no-packed-fp32-ops")))
#else
#define NOPK
#endif
#define LDSP(T, p) ((__attribute__((address_space(3))) T*)(p))
#define GAS __attribute__((address_space(1)))
#define RLX_AGENT __ATOMIC_RELAXED, __HIP_MEMORY_SCOPE_AGENT
#define SB() __builtin_amdgcn_sched_barrier(0)
#define LDIN(p) __builtin_nontemporal_load(p)
template <int V> struct IC { static constexpr int value = V; };
template <int I, int N, class F> __device__ __forceinline__ void static_for(F&& f) { if constexpr (I < N) { f(IC<I>{}); static_for<I + 1, N>(f); } }

constexpr int L_ = 2048, D_ = 448, N_ = 256, F_ = 1792;

constexpr size_t WS_CTL = 0;
constexpr int C1_CNT = 0;
constexpr int C2_CNT = 5120;
constexpr int ST2_OFF = 86016;
constexpr int CTL_BYTES = 102400;
constexpr size_t WS_MODA = 102400;
constexpr size_t WS_MODB = WS_MODA + 448 * 64;
constexpr size_t WS_MODC = WS_MODB + 448 * 32;
constexpr size_t WS_ST1 = 155648;
constexpr size_t WS_XT = WS_ST1 + 16384;
constexpr size_t WS_KREV = WS_XT + 64 * 448 * 64;
constexpr int KREV_STRIDE = 2064;
constexpr size_t WS_X1T = WS_KREV + 448 * KREV_STRIDE * 2;
constexpr size_t WS_XB = WS_X1T + 64 * 448 * 64;
constexpr size_t WS_W13 = WS_XB + 64 * 448 * 64;
constexpr size_t WS_W2F = WS_W13 + 112 * 28 * 1024;
constexpr size_t WS_HF = WS_W2F + 14 * 112 * 1024;
constexpr size_t WS_END = WS_HF + 64 * 112 * 1024;

DI float bflo(u32 v) { return U2F(v << 16); }
DI float bfhi(u32 v) { return U2F(v & 0xffff0000u); }
DI float bf2f(u16 v) { return U2F(((u32)v) << 16); }
DI u32 pkbf(float lo, float hi) { f32x2 p = {lo, hi}; return __builtin_bit_cast(u32, __builtin_convertvector(p, bf16x2)); }
DI u32 pkh(f32x2 p) { return __builtin_bit_cast(u32, __builtin_convertvector(p, f16x2)); }
DI float rbf(float f) { return bflo(pkbf(f, f)); }
DI float dot2(u32 a, u32 b, float c) { return __builtin_amdgcn_fdot2_f32_bf16(__builtin_bit_cast(bf16x2, a), __builtin_bit_cast(bf16x2, b), c, false); }
DI float fexp(float x) { return __builtin_amdgcn_exp2f(x * 1.4426950408889634f); }
DI float silu_bf(float c) { return rbf(c * __builtin_amdgcn_rcpf(1.f + fexp(-c))); }
template <int CTRL> DI float dppf(float x) { return I2F(__builtin_amdgcn_update_dpp(0, F2I(x), CTRL, 0xf, 0xf, false)); }
DI float sum16(float x) { x += dppf<0x128>(x); x += dppf<0x124>(x); x += dppf<0x122>(x); x += dppf<0x121>(x); return x; }
DI float max16(float x) { x = __builtin_fmaxf(x, dppf<0x128>(x)); x = __builtin_fmaxf(x, dppf<0x124>(x)); x = __builtin_fmaxf(x, dppf<0x122>(x)); x = __builtin_fmaxf(x, dppf<0x121>(x)); return x; }
DI float add32(float x) { auto r = __builtin_amdgcn_permlane32_swap(F2U(x), F2U(x), false, false); return U2F(r[0]) + U2F(r[1]); }
DI float add16(float x) { auto r = __builtin_amdgcn_permlane16_swap(F2U(x), F2U(x), false, false); return U2F(r[0]) + U2F(r[1]); }
DI float max64(float x) {
  x = max16(x);
  auto r = __builtin_amdgcn_permlane16_swap(F2U(x), F2U(x), false, false);
  x = __builtin_fmaxf(U2F(r[0]), U2F(r[1]));
  auto q = __builtin_amdgcn_permlane32_swap(F2U(x), F2U(x), false, false);
  return __builtin_fmaxf(U2F(q[0]), U2F(q[1]));
}
DI u32x2 trread(const char* p) { return __builtin_bit_cast(u32x2, __builtin_amdgcn_ds_read_tr16_b64_v4i16(LDSP(s16x4, p))); }
DI u32x2 trread3(const __attribute__((address_space(3))) char* p) { return __builtin_bit_cast(u32x2, __builtin_amdgcn_ds_read_tr16_b64_v4i16((__attribute__((address_space(3))) s16x4*)p)); }
DI void pinf(float& v) { asm volatile("" : "+v"(v)); }
DI void pin(u32x4& v) { asm volatile("" : "+v"(v)); }
DI void ostore(u32x2 v, u16* p) { *(u32x2*)p = v; }
DI f32x2 cmulp(f32x2 a, f32x2 z, f32x2 zs) { return __builtin_elementwise_fma(a.yx, zs, a * z.xx); }

#define STAMP_DECL
#define STAMP(i)
#define CSTAMP(i)
#define STAMP_OUT(buf, slot)
#define STAMP_OUT2(buf, slot)

constexpr int K1_NC = 448, K1_NE = 112, K1_NA = 64, K1_NB = 168;
constexpr int K1_LDS = 4 * 32 * 272 + 1536;
#define K1_AUX 16
#define MFMA32(a, b, c) __builtin_amdgcn_mfma_f32_32x32x16_bf16(__builtin_bit_cast(bf16x8, a), __builtin_bit_cast(bf16x8, b), c, 0, 0, 0)
#define ST16(v, off) __builtin_amdgcn_raw_buffer_store_b128(v, rs, (int)(off), 0, K1_AUX)

__global__ __launch_bounds__(256, 4) void k1_prep(const u16* __restrict__ x, const u16* __restrict__ logA, const u16* __restrict__ Aim,
                                                  const u16* __restrict__ Cr, const u16* __restrict__ Ci, const u16* __restrict__ logdt,
                                                  const u16* __restrict__ cvec, const u16* __restrict__ wmod, const u16* __restrict__ w1,
                                                  const u16* __restrict__ w3, char* __restrict__ ws,
                                                  unsigned long long* stamps) {
  __shared__ __attribute__((aligned(16))) char smem[K1_LDS];
  const int tid = threadIdx.x, lane = tid & 63, w = __builtin_amdgcn_readfirstlane(tid >> 6);
  const int G = lane >> 4, qq = (lane & 15) >> 2, pp = lane & 3, hi = lane >> 5;
  auto rs = __builtin_amdgcn_make_buffer_rsrc(ws, 0, (int)WS_END, 0x00020000);
  int b = blockIdx.x;
  STAMP_DECL;
  STAMP(0);
  if (__builtin_expect(b < K1_NC, 1)) {
    const int d = b;
    u32 lar, air, crr, cir, ldt;
    asm volatile("global_load_ushort %0, %5, %6 nt\n\tglobal_load_ushort %1, %5, %7 nt\n\tglobal_load_ushort %4, %10, %11\n\tglobal_load_ushort %2, %5, %8 nt\n\tglobal_load_ushort %3, %5, %9 nt"
                 : "=&v"(lar), "=&v"(air), "=&v"(crr), "=&v"(cir), "=&v"(ldt) : "v"((d * N_ + tid) * 2), "s"(logA), "s"(Aim), "s"(Cr), "s"(Ci), "v"(d * 2), "s"(logdt) : "memory");
    SB();
    const int lb = 2040 - 8 * tid;
    const int kr = lb < 256 ? 0 : 24 - __builtin_clz(lb);
    const float mB = U2F((u32)(150 + kr) << 23) + (float)lb;
    const u32 kpa = (u32)(4 * 32 * 272) + 256u * kr - ((u32)(150 + kr) << 24);
    const int s = lane & 31;
    const int kw = s < 8 ? 0 : (s < 20 ? (s >> 2) - 1 : 3);
    int wb = s < 12 ? (s < 8 ? s : 248 + s) : (s < 16 ? 384 + ((s & 2) << 5) + (s & 1) : 32 * s);
    wb = (wb > 672 ? 672 : wb) * 2 + 4 * 32 * 272;
    const int wst = 16 >> kw;
    SB();
    asm volatile("s_waitcnt vmcnt(0)" : "+v"(lar), "+v"(air), "+v"(crr), "+v"(cir), "+v"(ldt) :: "memory");
    const float dt = rbf(fexp(bflo(ldt)));
    const float ar = -rbf(fexp(bflo(lar))) * dt * 1.4426950408889634f;
    const float ai = bflo(air) * dt;
    const float I2PH = 0.15915494f, I2PL = 6.4206382e-9f;
    const float th = ai * I2PH;
    const float tl = __builtin_fmaf(ai, I2PL, __builtin_fmaf(ai, I2PH, -th));
    auto zpow = [&](float sg) {
      float e = __builtin_amdgcn_exp2f(ar * sg);
      float rev = __builtin_fmaf(tl, sg, __builtin_amdgcn_fractf(th * sg));
      return f32x2{e * __builtin_amdgcn_cosf(rev), e * __builtin_amdgcn_sinf(rev)};
    };
    const f32x2 z1 = zpow(1.f), z16 = zpow(16.f), z256 = zpow(256.f);
    const f32x2 s1 = {-z1.y, z1.y}, s16c = {z16.y, -z16.y}, s256 = {-z256.y, z256.y};
    u32 eb = ((crr & 0x7f80u) > (cir & 0x7f80u) ? (crr & 0x7f80u) : (cir & 0x7f80u));
    asm("s_nop 1\n\tv_max_u32_dpp %0, %0, %0 row_ror:8 row_mask:0xf bank_mask:0xf\n\ts_nop 1\n\tv_max_u32_dpp %0, %0, %0 row_ror:4 row_mask:0xf bank_mask:0xf\n\ts_nop 1\n\t"
        "v_max_u32_dpp %0, %0, %0 row_ror:2 row_mask:0xf bank_mask:0xf\n\ts_nop 1\n\tv_max_u32_dpp %0, %0, %0 row_ror:1 row_mask:0xf bank_mask:0xf\n\ts_nop 1" : "+v"(eb));
    {
      const u32 e0 = __builtin_amdgcn_readlane(eb, 0), e1 = __builtin_amdgcn_readlane(eb, 16), e2 = __builtin_amdgcn_readlane(eb, 32), e3 = __builtin_amdgcn_readlane(eb, 48);
      const u32 m0 = e0 > e1 ? e0 : e1, m1 = e2 > e3 ? e2 : e3;
      eb = (m0 > m1 ? m0 : m1) >> 7;
    }
    eb = eb < 1u ? 1u : (eb > 252u ? 252u : eb);
    const float scl = U2F((253u - eb) << 23);
    const float iscl = U2F((eb + 1u) << 23);
    STAMP(2);
    char* reg = smem + w * (32 * 272);
    f32x2 z8;
    {
      f32x2 lo[9], hp[9];
      lo[1] = z1; hp[1] = z256;
#pragma unroll
      for (int i = 2; i <= 8; ++i) { lo[i] = cmulp(lo[i - 1], z1, s1); hp[i] = cmulp(hp[i - 1], z256, s256); }
      const f32x2 a2 = cmulp(lo[2], z256, s256), a4 = cmulp(lo[4], z256, s256), a6 = cmulp(lo[6], z256, s256);
      const f32x2 b4 = cmulp(a4, z256, s256), c4 = cmulp(b4, z256, s256);
      const u32 col[21] = {0x00003c00u, pkh(lo[1]), pkh(lo[2]), pkh(lo[3]), pkh(lo[4]), pkh(lo[5]), pkh(lo[6]), pkh(lo[7]),
                           pkh(hp[1]), pkh(a2), pkh(a4), pkh(a6), pkh(hp[2]), pkh(b4), pkh(hp[3]), pkh(c4),
                           pkh(hp[4]), pkh(hp[5]), pkh(hp[6]), pkh(hp[7]), pkh(hp[8])};
#pragma unroll
      for (int i = 0; i < 21; ++i) *(u32*)(reg + i * 272 + lane * 4) = col[i];
      z8 = lo[8];
    }
    f16x8 bfr[8];
#pragma unroll
    for (int i = 0; i < 8; ++i) bfr[i] = *(const f16x8*)(reg + s * 272 + i * 32 + hi * 16);
    {
      f32x2 ra = {bflo(crr) * scl, -bflo(cir) * scl};
      f32x2 rb = cmulp(ra, z8, f32x2{z8.y, -z8.y});
      u32 row[32];
#pragma unroll
      for (int r = 0; r < 32; r += 2) {
        row[r] = pkh(ra); row[r + 1] = pkh(rb);
        if (r < 30) { ra = cmulp(ra, z16, s16c); rb = cmulp(rb, z16, s16c); }
        if (r == 4) asm volatile("s_waitcnt lgkmcnt(0)" : "+v"(bfr[0]), "+v"(bfr[1]), "+v"(bfr[2]), "+v"(bfr[3]), "+v"(bfr[4]), "+v"(bfr[5]), "+v"(bfr[6]), "+v"(bfr[7]) :: "memory");
        if (r == 4) { _Pragma("unroll") for (int q = 0; q < 4; ++q) *(u32*)(reg + q * 272 + lane * 4) = row[q]; }
        if (r >= 4) { *(u32*)(reg + r * 272 + lane * 4) = row[r]; *(u32*)(reg + (r + 1) * 272 + lane * 4) = row[r + 1]; }
      }
    }
    STAMP(3);
    f32x16 acc = {};
#pragma unroll
    for (int i = 0; i < 8; ++i) {
      const f16x8 af = *(const f16x8*)(reg + s * 272 + i * 32 + hi * 16);
      acc = __builtin_amdgcn_mfma_f32_32x32x16_f16(af, bfr[i], acc, 0, 0, 0);
    }
    const float fs = 2.f * dt * iscl;
#pragma unroll
    for (int qd = 0; qd < 4; ++qd)
      *(f32x4*)(reg + qd * 1024 + lane * 16) = f32x4{acc[4 * qd] * fs, acc[4 * qd + 1] * fs, acc[4 * qd + 2] * fs, acc[4 * qd + 3] * fs};
    __syncthreads();
    STAMP(4);
    {
      const char* rp = smem + w * 1024 + lane * 16;
      const f32x4 t = (*(const f32x4*)rp + *(const f32x4*)(rp + 32 * 272)) + (*(const f32x4*)(rp + 64 * 272) + *(const f32x4*)(rp + 96 * 272));
      const u32 a = pkbf(t[0], t[1]), c = pkbf(t[2], t[3]);
      char* q = smem + wb + (8 * w + 4 * hi) * wst;
      *(u16*)q = (u16)a; *(u16*)(q + wst) = (u16)(a >> 16); *(u16*)(q + 2 * wst) = (u16)c; *(u16*)(q + 3 * wst) = (u16)(c >> 16);
    }
    __syncthreads();
    {
      u32 o[4];
#pragma unroll
      for (int e = 0; e < 4; ++e) {
        const u32 i0 = (F2U(mB + (float)(7 - 2 * e)) << 1) + kpa, i1 = (F2U(mB + (float)(6 - 2 * e)) << 1) + kpa;
        o[e] = (u32)*(const u16*)(smem + i0) | ((u32)*(const u16*)(smem + i1) << 16);
      }
      ST16((u32x4{o[0], o[1], o[2], o[3]}), WS_KREV + d * (KREV_STRIDE * 2) + 16 * tid);
      if (tid < 2) ST16((u32x4{0, 0, 0, 0}), WS_KREV + d * (KREV_STRIDE * 2) + 4096 + 16 * tid);
    }
    STAMP(1); STAMP_OUT(stamps, blockIdx.x);
    return;
  }
  __builtin_amdgcn_s_sleep(6);
  b -= K1_NC;
  if (b < K1_NE) {
    const int fg = b >> 2, kq = b & 3;
    u32x4 v[7];
#pragma unroll
    for (int i = 0; i < 7; ++i) v[i] = LDIN((const u32x4*)(((tid & 8) ? w3 : w1) + (size_t)(112 * kq + (tid >> 4) + 16 * i) * F_ + fg * 64 + (tid & 7) * 8));
#pragma unroll
    for (int i = 0; i < 7; ++i) *(u32x4*)(smem + ((tid >> 4) + 16 * i) * 272 + (tid & 15) * 16) = v[i];
    __syncthreads();
    const char* rbase = smem + (8 * hi + qq) * 272 + (G & 1) * 128 + (16 * w + 8 * (pp & 1) + 4 * (pp >> 1)) * 2;
    const int dst = (int)WS_W13 + ((4 * fg + w) * 28 + 7 * kq) * 1024 + lane * 16;
#pragma unroll
    for (int s = 0; s < 7; ++s) {
      u32x2 lo = trread(rbase + s * 16 * 272), hh = trread(rbase + (s * 16 + 4) * 272);
      ST16((u32x4{lo.x, lo.y, hh.x, hh.y}), dst + s * 1024);
    }
    STAMP(1); STAMP_OUT(stamps, blockIdx.x);
    return;
  }
  b -= K1_NE;
  if (b < K1_NA) {
    const int row = tid >> 3, p = tid & 7;
    const u32x4* src = (const u32x4*)(x + (size_t)(b * 32 + row) * D_);
    u32x4 v[7];
#pragma unroll
    for (int i = 0; i < 7; ++i) v[i] = LDIN(src + p + 8 * i);
    if (b < CTL_BYTES / 4096) ST16((u32x4{0, 0, 0, 0}), WS_CTL + b * 4096 + tid * 16);
    float s = 0.f, q = 0.f;
#pragma unroll
    for (int i = 0; i < 7; ++i) {
      *(u32x4*)(smem + row * 928 + (p + 8 * i) * 16) = v[i];
#pragma unroll
      for (int j = 0; j < 4; ++j) { s = dot2(v[i][j], 0x3f803f80u, s); q = dot2(v[i][j], v[i][j], q); }
    }
    s += dppf<0xB1>(s); q += dppf<0xB1>(q); s += dppf<0x4E>(s); q += dppf<0x4E>(q); s += dppf<0x141>(s); q += dppf<0x141>(q);
    const float m = s * (1.f / D_);
    const float var = __builtin_fmaxf(q * (1.f / D_) - m * m, 0.f);
    const float sd = rbf(__builtin_amdgcn_sqrtf(rbf(rbf(var) + 1e-5f)));
    const float rsd = __builtin_amdgcn_rcpf(sd);
    if (p < 2) __builtin_amdgcn_raw_buffer_store_b32(F2U(p ? -rbf(m) * rsd : rsd), rs, (int)WS_ST1 + p * 8192 + (b * 32 + row) * 4, 0, K1_AUX);
    __syncthreads();
#pragma unroll
    for (int i = 0; i < 7; ++i) {
      const int dg = w * 7 + i;
      const int off = (8 * G + qq) * 928 + dg * 32 + pp * 8;
      u32x2 a = trread(smem + off), c = trread(smem + off + 4 * 928);
      ST16((u32x4{a.x, a.y, c.x, c.y}), WS_XT + b * 28672 + (dg * 16 + (lane & 15)) * 64 + G * 16);
    }
    STAMP(1); STAMP_OUT(stamps, blockIdx.x);
    return;
  }
  b -= K1_NA;
  {
    const int cg = b >> 2, kq = b & 3;
    const int col0 = 64 * cg;
    const int r = tid >> 3, p = tid & 7;
    u32x4 v[4]; u16 cv[4];
#pragma unroll
    for (int i = 0; i < 4; ++i) {
      int rr = r + 32 * i; if (rr > 111) rr = 111;
      const int k = 112 * kq + rr;
      v[i] = LDIN((const u32x4*)(wmod + (size_t)k * 2688 + col0 + p * 8));
      cv[i] = cvec[k];
    }
    float acc[8] = {0, 0, 0, 0, 0, 0, 0, 0};
#pragma unroll
    for (int i = 0; i < 4; ++i) {
      u32 sh = F2U(silu_bf(bf2f(cv[i])));
      if (r + 32 * i > 111) sh = 0u;
      const u32 sl = sh >> 16;
#pragma unroll
      for (int j = 0; j < 4; ++j) { acc[2 * j] = dot2(v[i][j], sl, acc[2 * j]); acc[2 * j + 1] = dot2(v[i][j], sh, acc[2 * j + 1]); }
    }
    float* red = (float*)smem;
#pragma unroll
    for (int j = 0; j < 8; ++j) { acc[j] += dppf<0x128>(acc[j]); acc[j] = add32(acc[j]); }
    if ((lane & 40) == 0) {
      const int slot = w * 2 + (lane >> 4);
      *(f32x4*)(red + slot * 64 + p * 8) = f32x4{acc[0], acc[1], acc[2], acc[3]};
      *(f32x4*)(red + slot * 64 + p * 8 + 4) = f32x4{acc[4], acc[5], acc[6], acc[7]};
    }
    __syncthreads();
    if (tid < 64) {
      float t = 0.f;
#pragma unroll
      for (int j = 0; j < 8; ++j) t += red[j * 64 + tid];
      const int sec = cg / 7, dd = (cg % 7) * 64 + tid;
      const int o1 = sec == 3 ? (int)WS_MODB + dd * 32 : (sec == 5 ? (int)WS_MODC + dd * 16 : (int)WS_MODA + dd * 64 + (sec == 4 ? 3 : sec) * 16);
      __builtin_amdgcn_raw_buffer_store_b32(F2U(t), rs, o1 + kq * 4, 0, K1_AUX);
      if (sec == 4) __builtin_amdgcn_raw_buffer_store_b32(F2U(t), rs, (int)WS_MODB + dd * 32 + 16 + kq * 4, 0, K1_AUX);
    }
    STAMP(1); STAMP_OUT(stamps, blockIdx.x);
  }
}

template <int IMM> DI u32x4 gload(u32 voff, const char* sbase) {
  u32x4 r; asm volatile("global_load_dwordx4 %0, %1, %2 offset:%3 sc1" : "=v"(r) : "v"(voff), "s"(sbase), "i"(IMM)); return r;
}
DI void glds16(u32 voff, const char* sbase, u32 ldsaddr) {
  asm volatile("s_mov_b32 m0, %2\n\ts_nop 0\n\tglobal_load_lds_dwordx4 %0, %1 sc1" :: "v"(voff), "s"(sbase), "s"(ldsaddr) : "memory");
}
constexpr unsigned SPIN_CAP = 1u << 16;
DI void sgload(unsigned long long& g, GAS unsigned long long* p) { asm volatile("global_load_dwordx2 %0, %1, off sc1" : "+v"(g) : "v"(p) : "memory"); }
DI void sgwait(unsigned long long& g) { asm volatile("s_waitcnt vmcnt(1)" : "+v"(g) :: "memory"); }
DI void sgreload(unsigned long long& g, GAS unsigned long long* p) { asm volatile("global_load_dwordx2 %0, %1, off sc1\n\ts_waitcnt vmcnt(0)" : "+v"(g) : "v"(p) : "memory"); }
#define POLL_ISSUE(v, p, m) asm volatile("s_mov_b64 exec, %3\n\tglobal_atomic_or %0, %1, %2, off sc0\n\ts_mov_b64 exec, -1" : "+v"(v) : "v"(p), "v"(0), "i"(m) : "memory")
template <int NL, int SLP> DI void wait_cnt(GAS u32* p, u32 want, u32& v0, u32& v1, u32& v2) {
  v0 = want; v1 = want; v2 = want;
  POLL_ISSUE(v0, p, (1 << NL) - 1); __builtin_amdgcn_s_sleep(SLP);
  POLL_ISSUE(v1, p, (1 << NL) - 1); __builtin_amdgcn_s_sleep(SLP);
#pragma unroll 1
  for (unsigned spins = 0; spins < SPIN_CAP; ++spins) {
    POLL_ISSUE(v2, p, (1 << NL) - 1);
    asm volatile("s_waitcnt vmcnt(2)" : "+v"(v0) :: "memory");
    if (__builtin_amdgcn_ballot_w64(v0 < want) == 0) break;
    POLL_ISSUE(v0, p, (1 << NL) - 1);
    asm volatile("s_waitcnt vmcnt(2)" : "+v"(v1) :: "memory");
    if (__builtin_amdgcn_ballot_w64(v1 < want) == 0) break;
    POLL_ISSUE(v1, p, (1 << NL) - 1);
    asm volatile("s_waitcnt vmcnt(2)" : "+v"(v2) :: "memory");
    if (__builtin_amdgcn_ballot_w64(v2 < want) == 0) break;
  }
}
#define POLL_KEEP() asm volatile("" :: "v"(pl0), "v"(pl1), "v"(pl2) : "memory")
DI float sum4(f32x4 v) { return rbf((v[0] + v[1]) + (v[2] + v[3])); }
#define WAIT_VM0() __builtin_amdgcn_s_waitcnt(0x0F70)
#define BAR() __builtin_amdgcn_s_barrier()

constexpr int CV_DSTR = 4 * 4160 + 4608;
constexpr int CV_RED = 2 * CV_DSTR;
constexpr int G1_ST = 4 * 28672;
constexpr int G2_RED = 4 * 28672;
constexpr int G2_X1 = 18 * 8192;
constexpr int TAB_GB = G2_X1 + 8192;
constexpr int TAB_SB = TAB_GB + 256;
constexpr int KF_LDS = TAB_SB + 1792;
#define WSLOT stv[0]
#define W2_N0 13
#define G2_B1 6
#define G2_PL 8
#define EPI0 7
#define W_N0 4
#define W_NM 18
#define W_ST 4
#define W_NB 2
#define W_N1 (W_N0 + W_NB)
#define W_N2 (W_N1 + W_NM)
#define SC_SIG 6
#define SC_POLL 21
#define LAS __attribute__((address_space(3)))
#define PIN14(a, o) asm volatile("" : "+v"(a[o]), "+v"(a[o + 1]), "+v"(a[o + 2]), "+v"(a[o + 3]), "+v"(a[o + 4]), "+v"(a[o + 5]), "+v"(a[o + 6]), "+v"(a[o + 7]), \
    "+v"(a[o + 8]), "+v"(a[o + 9]), "+v"(a[o + 10]), "+v"(a[o + 11]), "+v"(a[o + 12]), "+v"(a[o + 13]) :: "memory")

constexpr int g1_e0(int t) { return t == 1 ? EPI0 : 3; }
constexpr int g1_st(int t) { return g1_e0(t) + 19; }
constexpr int g1_an(int t) { return t == 2 ? 18 : 14; }
constexpr int g1_w2(int n) {
  if (n >= 25 && n < 25 + 14 * 4 && (n - 25) % 4 == 0) return (n - 25) / 4;
  if (n >= 84 && n < 84 + 23) { const int q = n - 84; if (q % 3 != 2) return 14 + q - q / 3 < 28 ? 14 + q - q / 3 : -1; }
  return -1;
}
constexpr int g1_sg(int t) { return t == 0 ? 24 : 28 * t + 10; }
constexpr int g1_vm(int a, int b) {
  int c = 0;
  for (int n = a; n < b; ++n) {
    const int t = n / 28, q = n % 28;
    if (n > a) { if (t >= 2 && q == g1_an(t)) ++c; if (n == 108) ++c; if (n == g1_sg(t)) ++c; }
    if (t >= 1 && q == g1_st(t) && n != a) ++c;
    if (g1_w2(n) >= 0) ++c;
  }
  return c;
}
NOPK __global__ __launch_bounds__(512) void kf_main(const u16* __restrict__ Dsk, const u16* __restrict__ w2, char* __restrict__ ws, u16* __restrict__ out, unsigned long long* stamps) {
  __shared__ __attribute__((aligned(16))) char smem[KF_LDS];
  const int tid = __builtin_amdgcn_workitem_id_x(), lane = tid & 63;
  const int w = __builtin_amdgcn_readfirstlane(tid >> 6);
  const int b = __builtin_amdgcn_workgroup_id_x(), xcd = b & 7, idx = b >> 3;
  const int hi = lane >> 5;
  const int G = lane >> 4, qq = (lane & 15) >> 2, pp = lane & 3;
  const u32 voff = lane * 16;
  GAS u32* ctl = (GAS u32*)(ws + WS_CTL);
  auto rs = __builtin_amdgcn_make_buffer_rsrc(ws, 0, (int)WS_END, 0x00020000);
  const u32 lds0 = (u32)(size_t)LDSP(char, smem);
  STAMP_DECL;
  STAMP(0);
  u32 pl0 = 0, pl1 = 0, pl2 = 0;
  if (b >= 224) {
    const int fr = b - 224;
    if (fr < 28) {
      u32x4 v[7];
#pragma unroll
      for (int i = 0; i < 7; ++i) v[i] = LDIN((const u32x4*)(w2 + (size_t)(64 * fr + (tid >> 3)) * D_ + ((tid & 7) + 8 * i) * 8));
#pragma unroll
      for (int i = 0; i < 7; ++i) *(u32x4*)(smem + (tid >> 3) * 912 + ((tid & 7) + 8 * i) * 16) = v[i];
      asm volatile("s_waitcnt lgkmcnt(0)\n\ts_barrier" ::: "memory");
      const char* rbase = smem + (16 * (w & 3) + 8 * hi + qq) * 912 + (w >> 2) * 448 + (16 * (G & 1) + 4 * pp) * 2;
      const int dst = (int)WS_W2F + ((7 * (w >> 2)) * 112 + 4 * fr + (w & 3)) * 1024 + lane * 16;
#pragma unroll
      for (int s = 0; s < 7; ++s) {
        u32x2 lo = trread(rbase + s * 64), hh = trread(rbase + s * 64 + 4 * 912);
        __builtin_amdgcn_raw_buffer_store_b128(u32x4{lo.x, lo.y, hh.x, hh.y}, rs, dst + s * 112 * 1024, 0, 16);
      }
    }
    asm volatile("s_waitcnt vmcnt(0)\n\ts_barrier" ::: "memory");
    if (w == 0) {
      if (lane < 16 && fr < 28) __hip_atomic_fetch_add(ctl + C1_CNT + (lane * 9 + 8) * 32, 1u, RLX_AGENT);
      wait_cnt<8, 6>(ctl + C1_CNT + ((b & 15) * 9 + lane) * 32, 28u, pl0, pl1, pl2);
      asm volatile("s_waitcnt vmcnt(0)" ::: "memory");
      POLL_KEEP();
    }
    asm volatile("s_barrier" ::: "memory");
    {
      const char* xsrc = ws + WS_X1T + (size_t)(2 * fr + (w >> 2)) * 28672 + (w & 3) * 1024;
#pragma unroll
      for (int i = 0; i < 7; ++i) glds16(voff, xsrc + i * 4096, lds0 + w * 7168 + i * 1024);
      const char* sp = smem + w * 7168 + (8 * hi + qq) * 64 + (G & 1) * 32 + pp * 8;
      float s0 = 0.f, q0 = 0.f, s1 = 0.f, q1 = 0.f;
      static_for<0, 7>([&](auto ic) __attribute__((always_inline)) {
        constexpr int i = decltype(ic)::value;
        asm volatile("s_waitcnt vmcnt(%0)" :: "i"(6 - i) : "memory");
        const u32x2 lo = trread(sp + i * 1024), hh = trread(sp + i * 1024 + 256);
        s0 = dot2(lo.x, 0x3f803f80u, s0); q0 = dot2(lo.x, lo.x, q0); s1 = dot2(lo.y, 0x3f803f80u, s1); q1 = dot2(lo.y, lo.y, q1);
        s0 = dot2(hh.x, 0x3f803f80u, s0); q0 = dot2(hh.x, hh.x, q0); s1 = dot2(hh.y, 0x3f803f80u, s1); q1 = dot2(hh.y, hh.y, q1);
      });
      auto r = __builtin_amdgcn_permlane32_swap(F2U(s0 + s1), F2U(q0 + q1), false, false);
      *(float*)(smem + 65536 + w * 256 + (lane & 31) * 8 + hi * 4) = U2F(r[0]) + U2F(r[1]);
      asm volatile("s_waitcnt lgkmcnt(0)\n\ts_barrier" ::: "memory");
      if (w == 0) {
        const char* pt = smem + 65536 + hi * 1024 + (lane & 31) * 8;
        const f32x2 ts = (*(const f32x2*)pt + *(const f32x2*)(pt + 256)) + (*(const f32x2*)(pt + 512) + *(const f32x2*)(pt + 768));
        const float m = ts[0] * (1.f / D_);
        const float var = __builtin_fmaxf(ts[1] * (1.f / D_) - m * m, 0.f);
        const float sd = __builtin_amdgcn_sqrtf(rbf(rbf(var) + 1e-5f));
        __hip_atomic_store((GAS unsigned long long*)(ws + ST2_OFF) + 64 * fr + lane, (1ull << 32) | pkbf(-m, sd), RLX_AGENT);
      }
    }
    return;
  }
  const int rt = 4 * (xcd >> 1) + idx / 7, ct = 7 * (xcd & 1) + idx % 7;
  const int ft = 8 * ct + w;
  const int r2 = 4 * xcd + idx / 7, dg = idx % 7, kq = w >> 1, dt = w & 1;
  const int te = 4 * (r2 >> 1) + (r2 & 1), tl = 4 * (r2 >> 1) + 3 - (r2 & 1);
  const int wv = (int)WS_W13 + ft * 28672 + lane * 16;
#define WLOAD(p) wf[p] = __builtin_amdgcn_raw_buffer_load_b128(rs, wv + ((p) & 3) * 1024, ((p) & ~3) * 1024, 0)

  u32x4 wf[28];
  u32x4 wex;
  const int hbase = (int)WS_HF + (4 * rt * 112 + ft) * 1024 + lane * 16;

  {
    const int dl = w >> 2, q4 = w & 3;
    const int a = lane & 15;
    const int t256 = tid & 255;
    const int l0 = 256 * w + 16 * a + 4 * G;
    const int xoff = ((l0 >> 5) * 448 + 2 * b) * 64 + (l0 & 31) * 2;
    u32x4 k0 = __builtin_amdgcn_raw_buffer_load_b128(rs, (int)WS_KREV + (2 * b + dl) * (KREV_STRIDE * 2) + 16 * t256, 0, 0);
    u32x2 k1 = __builtin_amdgcn_raw_buffer_load_b64(rs, (int)WS_KREV + (2 * b + dl) * (KREV_STRIDE * 2) + 16 * t256 + 16, 0, 0);
    u32x2 xx[2]; u32x4 st[2];
#pragma unroll
    for (int i = 0; i < 2; ++i) {
      xx[i] = __builtin_amdgcn_raw_buffer_load_b64(rs, (int)WS_XT + xoff + i * 64, 0, 0);
      st[i] = __builtin_amdgcn_raw_buffer_load_b128(rs, (int)WS_ST1 + l0 * 4, i * 8192, 0);
    }
    SB();
    f32x4 ms[8];
#pragma unroll
    for (int i = 0; i < 8; ++i) ms[i] = *(const f32x4*)(ws + WS_MODA + b * 128 + i * 16);
    const u32 dskr = *(const u32*)(Dsk + 2 * b);
    SB();
    STAMP(13);
    asm volatile("s_barrier" ::: "memory");
    SB();

    f32x4 tb[2];
#pragma unroll
    for (int i = 0; i < 2; ++i)
      tb[i] = __builtin_bit_cast(f32x4, __builtin_amdgcn_raw_buffer_load_b128(rs, tid < 448 ? (int)WS_MODB + tid * 32 + i * 16 : (int)WS_MODC + (64 * dg + tid - 448) * 16, 0, 0));
#pragma unroll
    for (int i = 0; i < W_N0; ++i) WLOAD(i);
    SB();
    STAMP(8);
    {
      char* cp = smem + dl * CV_DSTR;
      if (t256 < 32) *(u32x4*)(cp + 4 * 4160 + t256 * 16) = u32x4{0, 0, 0, 0};
      else if (t256 < 40) *(u32x4*)(cp + ((t256 - 32) >> 1) * 4160 + 4096 + (t256 & 1) * 16) = u32x4{0, 0, 0, 0};
      u32 al0 = __builtin_amdgcn_alignbit(k0.y, k0.x, 16), al1 = __builtin_amdgcn_alignbit(k0.z, k0.y, 16), al2 = __builtin_amdgcn_alignbit(k0.w, k0.z, 16),
          al3 = __builtin_amdgcn_alignbit(k1.x, k0.w, 16), al4 = __builtin_amdgcn_alignbit(k1.y, k1.x, 16);
      char* q = cp + t256 * 16;
      *(u32x4*)(q) = k0;
      *(u32x4*)(q + 4160) = u32x4{al0, al1, al2, al3};
      *(u32x4*)(q + 8320) = u32x4{k0.y, k0.z, k0.w, k1.x};
      *(u32x4*)(q + 12480) = u32x4{al1, al2, al3, al4};
    }
    float ga[2], b2v[2];
    const int swl = (lane ^ ((lane >> 5) << 3)) * 16;
#pragma unroll
    for (int i = 0; i < 2; ++i) {
      const float sa = sum4(ms[4 * i]), beta = rbf(1.f + sum4(ms[4 * i + 1]));
      ga[i] = sum4(ms[4 * i + 2]); b2v[i] = rbf(1.f + sum4(ms[4 * i + 3]));
      const float gd = ga[i] * (i ? bfhi(dskr) : bflo(dskr));
      const float xv[4] = {bflo(xx[i].x), bfhi(xx[i].x), bflo(xx[i].y), bfhi(xx[i].y)};
      float hv[4];
#pragma unroll
      for (int e = 0; e < 4; ++e)
        hv[e] = __builtin_fmaf(__builtin_fmaf(xv[e], U2F(st[0][e]), U2F(st[1][e])), beta, sa);
      *(u32x2*)(smem + i * CV_DSTR + 4 * 4160 + 512 + l0 * 2) = u32x2{pkbf(hv[0], hv[1]), pkbf(hv[2], hv[3])};
      f32x4 base;
#pragma unroll
      for (int e = 0; e < 4; ++e) base[e] = __builtin_fmaf(hv[e], gd, xv[e]);
      *(f32x4*)(smem + CV_RED + (i * 40 + w * 5 + 4) * 1024 + swl) = base;
    }
    SB();
#pragma unroll
    for (int i = W_N0; i < W_N1; ++i) WLOAD(i);
    SB();
    asm volatile("s_waitcnt lgkmcnt(0)\n\ts_barrier" ::: "memory");
    STAMP(1);
    f32x4 acc[8];
#pragma unroll
    for (int t = 0; t < 8; ++t) acc[t] = f32x4{0, 0, 0, 0};
    const int im = 15 - a;
    const u32 abase = lds0 + dl * CV_DSTR + (im & 3) * 4160 + (2016 + 8 * G + 4 * (im >> 2)) * 2 - q4 * 128 - 3648;
    const u32 bbase = lds0 + dl * CV_DSTR + 4 * 4160 + (256 - 16 + 16 * a + 8 * G) * 2 - q4 * 128 - 64;
    char* dump = smem + CV_RED + (dl * 40 + q4) * 1024 + swl;
#pragma unroll
    for (int cc = 0; cc < 2; ++cc) {
      u32x2 alo[8], ahi[8]; u32x4 bfr[8];
#pragma unroll
      for (int c1 = 0; c1 < 8; ++c1)
        asm volatile("ds_read_b64 %0, %3 offset:%5\n\tds_read_b64 %1, %3 offset:%6\n\tds_read_b128 %2, %4 offset:%7"
                     : "=&v"(alo[c1]), "=&v"(ahi[c1]), "=&v"(bfr[c1])
                     : "v"(abase), "v"(bbase), "i"(3648 - (8 * c1 + cc) * 64), "i"(3656 - (8 * c1 + cc) * 64), "i"(64 + (8 * c1 - cc) * 64) : "memory");
      SB();
      if (cc == 0) __builtin_amdgcn_s_setprio(1); else __builtin_amdgcn_s_setprio(0);
#pragma unroll
      for (int t = 0; t < 8; ++t) {
        if (t == 0) asm volatile("s_waitcnt lgkmcnt(15)" : "+v"(alo[0]), "+v"(ahi[0]), "+v"(bfr[0]), "+v"(alo[1]), "+v"(ahi[1]), "+v"(bfr[1]), "+v"(alo[2]), "+v"(ahi[2]), "+v"(bfr[2]));
        if (t == 3) asm volatile("s_waitcnt lgkmcnt(9)" : "+v"(alo[3]), "+v"(ahi[3]), "+v"(bfr[3]), "+v"(alo[4]), "+v"(ahi[4]), "+v"(bfr[4]));
        if (t == 5) asm volatile("s_waitcnt lgkmcnt(0)" : "+v"(alo[5]), "+v"(ahi[5]), "+v"(bfr[5]), "+v"(alo[6]), "+v"(ahi[6]), "+v"(bfr[6]), "+v"(alo[7]), "+v"(ahi[7]), "+v"(bfr[7]));
#pragma unroll
        for (int c1 = 0; c1 <= t; ++c1) {
          acc[t] = __builtin_amdgcn_mfma_f32_16x16x32_bf16(__builtin_bit_cast(bf16x8, u32x4{alo[c1].x, alo[c1].y, ahi[c1].x, ahi[c1].y}),
                                                           __builtin_bit_cast(bf16x8, bfr[t - c1]), acc[t], 0, 0, 0);
          const int n = t * (t + 1) / 2 + c1;
          if ((n % W_ST) == W_ST - 1 && W_N1 + (36 / W_ST) * cc + n / W_ST < W_N2) { SB(); WLOAD(W_N1 + (36 / W_ST) * cc + n / W_ST); SB(); }
          if (cc == 1 && t >= 5 && c1 >= 1) {
            const int u = (t == 5 ? c1 - 1 : (t == 6 ? (c1 == 1 ? 5 : -1) : (c1 == 1 ? 6 : -1)));
            if (u >= 0) { SB(); *(f32x4*)(dump + u * 5120) = acc[u]; SB(); }
          }
        }
        SB();
      }
    }
    *(f32x4*)(dump + 7 * 5120) = acc[7];
    STAMP(2);
    SB();
#pragma unroll
    for (int i = W_N2; i < 28; ++i) WLOAD(i);
    SB();
    if (tid < 448) {
      const float be = rbf(1.f + sum4(tb[1]));
      *(u16*)(smem + TAB_SB + tid * 2) = (u16)(F2U(sum4(tb[0])) >> 16);
      *(u16*)(smem + TAB_SB + 896 + tid * 2) = (u16)(F2U(be) >> 16);
    } else {
      *(float*)(smem + TAB_GB + (tid - 448) * 4) = sum4(tb[0]);
    }
    asm volatile("s_waitcnt lgkmcnt(0)\n\ts_barrier" ::: "memory");
    {
      const int ch = (lane >> 2) & 1;
      const float gac = ch ? ga[1] : ga[0];
      const float b2 = ch ? b2v[1] : b2v[0];
      const char* rp = smem + CV_RED + (ch * 40 + w * 5) * 1024 + (lane & 1) * 512 + ((2 * (lane >> 3) + ((lane >> 1) & 1)) ^ ((lane & 1) << 3)) * 16;
      u32x4 o, ob;
#pragma unroll
      for (int i = 0; i < 2; ++i) {
        const char* q = rp + i * 256;
        const f32x4 y = (*(const f32x4*)q + *(const f32x4*)(q + 1024)) + (*(const f32x4*)(q + 2048) + *(const f32x4*)(q + 3072));
        const f32x4 bs = *(const f32x4*)(q + 4096);
        o[2 * i] = pkbf(__builtin_fmaf(y[0], gac, bs[0]), __builtin_fmaf(y[1], gac, bs[1]));
        o[2 * i + 1] = pkbf(__builtin_fmaf(y[2], gac, bs[2]), __builtin_fmaf(y[3], gac, bs[3]));
      }
#pragma unroll
      for (int i = 0; i < 4; ++i) ob[i] = pkbf(bflo(o[i]) * b2, bfhi(o[i]) * b2);
      const int so = ((8 * w + (lane >> 3)) * 448 + 2 * b) * 64 + (lane & 7) * 16;
      __builtin_amdgcn_raw_buffer_store_b128(ob, rs, (int)WS_XB + so, 0, 16);
      __builtin_amdgcn_raw_buffer_store_b128(o, rs, (int)WS_X1T + so, 0, 16);
    }
    SB();
    STAMP(10);
  }
  u32x4 w2f[28];
  const int rot = (r2 & 1) * 7168;
  const char* w2src = ws + WS_W2F + ((size_t)(2 * dg) * 112 + 14 * w) * 1024;
  WAIT_VM0();
  asm volatile("s_waitcnt vmcnt(0)\n\ts_barrier" ::: "memory");
  STAMP(3);
  if (w == 0 && lane < 16) __hip_atomic_fetch_add(ctl + C1_CNT + (lane * 9 + xcd) * 32, 1u, RLX_AGENT);
  SB();
  STAMP(11);
  auto uvcalc = [&]() __attribute__((always_inline)) {
    f32x16 accw;
#pragma unroll
    for (int r = 0; r < 16; ++r) accw[r] = 0.f;
    const char* tsb = smem + TAB_SB + (G & 1) * 896 + hi * 16;
#pragma unroll
    for (int p = 0; p < 28; ++p) accw = MFMA32(*(const u32x4*)(tsb + p * 32), wf[p], accw);
    const float us = accw[8], vs = accw[0];
    const float uh = rbf(us), vh = rbf(vs);
    wex = u32x4{hi ? 0u : pkbf(uh, us - uh), hi ? 0u : pkbf(vh, vs - vh), 0u, 0u};
    pin(wex);
  };
  if (w == 0) wait_cnt<9, 6>(ctl + C1_CNT + ((b & 15) * 9 + lane) * 32, 28u, pl0, pl1, pl2); else uvcalc();
  asm volatile("s_barrier" ::: "memory");
  STAMP(4);
  {
    const char* xsrc = ws + WS_XB + (size_t)(4 * rt) * 28672 + w * 1024;
#pragma unroll
    for (int i = 0; i < 14; ++i) glds16(voff, xsrc + i * 8192, lds0 + w * 1024 + i * 8192);
    glds16(voff, ws + WS_X1T + ((size_t)(w >> 2 ? tl : te) * 448 + 64 * dg) * 64 + (w & 3) * 1024, lds0 + G2_X1 + w * 1024);
  }
  if (w == 0) uvcalc();
  GAS u32* c2p = ctl + C2_CNT + rt * 1024 + (ft / 14) * 32;
  GAS u32* c2e = ctl + C2_CNT + te * 256 + w * 32;
  GAS u32* c2l = ctl + C2_CNT + tl * 256 + w * 32;
  u32 pv = 14;
#define ANNOUNCE(n, tt) asm volatile("s_waitcnt vmcnt(%3)\n\ts_mov_b64 exec, 1\n\tglobal_atomic_add %0, %1, %2 offset:%4\n\ts_mov_b64 exec, -1" :: "v"(0), "v"(1), "s"(c2p), "i"(n), "i"((tt) * 1024) : "memory")
  {
    u32 rb = lds0 + (8 * hi + qq) * 64 + (G & 1) * 32 + pp * 8;
    GAS unsigned long long* sgp = (GAS unsigned long long*)(ws + ST2_OFF) + 128 * rt + (lane & 31);

    u32x4 xf[4];
    f32x16 acc[2];
    float nal, s2, ex[8], pr[8];
    unsigned long long sg = 0;
#define G1_READ(q) do { const LAS char* p_ = (const LAS char*)(size_t)rb + (q) * 1024; u32x2 lo = trread3(p_), hh = trread3(p_ + 256); xf[(q) & 3] = u32x4{lo.x, lo.y, hh.x, hh.y}; } while (0)
#define SG_WAIT(g, ptr) do { _Pragma("unroll 1") for (unsigned spins = 0; spins < SPIN_CAP; ++spins) { if (__builtin_amdgcn_ballot_w64((u32)((g) >> 32) != 1u) == 0) break; sgreload(g, ptr); } } while (0)
#define SG_APPLY(g, A) do { const u32 st_ = (u32)(g); const u32 mm_ = __builtin_amdgcn_perm(st_, st_, 0x01000100u), ss_ = __builtin_amdgcn_perm(st_, st_, 0x03020302u); \
      const float sd_ = bfhi(st_); nal = -1.4426950408889634f * __builtin_amdgcn_rcpf(sd_); s2 = sd_ * sd_; \
      pinf(nal); pinf(s2); A = MFMA32(wex, (u32x4{mm_, ss_, mm_, ss_}), A); } while (0)
#define EPI_STAGE(A, st, v) do { if constexpr ((st) == 0) { ex[v] = A[v] * nal; pr[v] = A[v] * A[8 + (v)]; pinf(pr[v]); } \
      if constexpr ((st) == 1) ex[v] = __builtin_amdgcn_exp2f(ex[v]); if constexpr ((st) == 2) ex[v] = __builtin_fmaf(ex[v], s2, s2); \
      if constexpr ((st) == 3) ex[v] = __builtin_amdgcn_rcpf(ex[v]); if constexpr ((st) == 4) { pr[v] *= ex[v]; pinf(pr[v]); } else pinf(ex[v]); } while (0)
#define EPI_STORE(tt) __builtin_amdgcn_raw_buffer_store_b128(u32x4{pkbf(pr[0], pr[1]), pkbf(pr[2], pr[3]), pkbf(pr[4], pr[5]), pkbf(pr[6], pr[7])}, rs, hbase, (tt) * 112 * 1024, 16)
    asm volatile("s_waitcnt vmcnt(11)\n\ts_barrier" ::: "memory");
    POLL_KEEP();
    STAMP(12);
    G1_READ(0); G1_READ(1);
    static_for<0, 112>([&](auto nc) __attribute__((always_inline)) {
      constexpr int n = decltype(nc)::value, t = n / 28, q = n % 28;
      constexpr int wl = g1_w2(n);
      if constexpr (t >= 1 && q == g1_e0(t) - 2) {
        asm volatile("s_waitcnt vmcnt(%1)" : "+v"(sg) : "i"(g1_vm(g1_sg(t - 1), n)) : "memory");
        SG_WAIT(sg, sgp + 32 * (t - 1));
      }
      if constexpr (n == 24) asm volatile("s_waitcnt vmcnt(0)\n\ts_barrier" ::: "memory");
      if constexpr (t >= 2 && q == g1_an(t)) ANNOUNCE(g1_vm(28 * (t - 1) + g1_st(t - 1), n), t - 2);
      if constexpr (n == 108) POLL_ISSUE(pv, c2e, 1);
      if constexpr (n == g1_sg(t)) sgload(sg, sgp + 32 * t);
      if constexpr (q == 26 && (t & 1)) { rb += 57344; asm volatile("" : "+v"(rb)); }
      SB();
      if constexpr (n < 110) G1_READ((n + 2) % 56);
      SB();
      if constexpr (q == 0) { _Pragma("unroll") for (int r = 0; r < 16; ++r) acc[t & 1][r] = 0.f; }
      acc[t & 1] = MFMA32(wf[q], xf[q & 3], acc[t & 1]);
      if constexpr (t >= 1) {
        if constexpr (q == g1_e0(t) - 2) SG_APPLY(sg, acc[(t - 1) & 1]);
        static_for<0, 5>([&](auto sc) __attribute__((always_inline)) {
          constexpr int st = decltype(sc)::value, g = q - g1_e0(t) - st, v = g / 2;
          if constexpr (g >= 0 && (g & 1) == 0 && v < 8) EPI_STAGE(acc[(t - 1) & 1], st, v);
        });
        if constexpr (q == g1_st(t)) { SB(); EPI_STORE(t - 1); }
      }
      if constexpr (wl >= 0) { SB(); w2f[wl] = gload<(((wl >> 1) % 7) & 3) * 1024>(voff, w2src + (wl & 1) * 114688 + (wl >= 14 ? 7168 - rot : rot) + (((wl >> 1) % 7) >> 2) * 4096); }
    });
    SB();
    STAMP(5);
    asm volatile("s_waitcnt vmcnt(%1)" : "+v"(sg) : "i"(g1_vm(g1_sg(3), 112)) : "memory");
    SG_WAIT(sg, sgp + 96);
    SG_APPLY(sg, acc[1]);
    static_for<0, 5>([&](auto sc) __attribute__((always_inline)) {
      static_for<0, 8>([&](auto vc) __attribute__((always_inline)) { EPI_STAGE(acc[1], decltype(sc)::value, decltype(vc)::value); });
    });
    EPI_STORE(3);
    SB();
  }
  {
    const f32x4 gbv = *(const f32x4*)(smem + TAB_GB + (32 * dt + 8 * kq + 4 * hi) * 4);
    u32x4 hf[2][14];
    const char* hsrc[2] = {ws + WS_HF + ((size_t)te * 112 + 14 * w) * 1024, ws + WS_HF + ((size_t)tl * 112 + 14 * w) * 1024};
#define H_LOAD(j, lo, hi_) static_for<lo, hi_>([&](auto ic) __attribute__((always_inline)) { constexpr int i = decltype(ic)::value; hf[j][i] = gload<((i % 7) & 3) * 1024>(voff, hsrc[j] + (i >= 7 ? 7168 - rot : rot) + ((i % 7) >> 2) * 4096); })
    pl0 = pl1 = pl2 = 0;
#define ANNOUNCE23(n) asm volatile("s_waitcnt vmcnt(%3)\n\ts_mov_b64 exec, 3\n\tglobal_atomic_add %0, %1, %2 offset:2048\n\ts_mov_b64 exec, -1" :: "v"(lane << 10), "v"(1), "s"(c2p), "i"(n) : "memory")
    asm volatile("s_waitcnt vmcnt(1)\n\ts_barrier" : "+v"(pv) :: "memory");
    const bool slow = __builtin_amdgcn_readfirstlane(pv) < 14u;
    if (slow) {
      ANNOUNCE23(0);
      wait_cnt<1, 6>(c2e, 14u, pl0, pl1, pl2);
    }
    STAMP(6);
    H_LOAD(0, 0, G2_B1);
    if (!slow) ANNOUNCE23(G2_B1);
    H_LOAD(0, G2_B1, 14);
    static_for<0, 2>([&](auto jc) __attribute__((always_inline)) {
      constexpr int j = decltype(jc)::value;
      f32x16 acc[2];
#pragma unroll
      for (int c = 0; c < 2; ++c)
#pragma unroll
        for (int r = 0; r < 16; ++r) acc[c][r] = 0.f;
      static_for<0, 14>([&](auto sc) __attribute__((always_inline)) {
        constexpr int s = decltype(sc)::value;
        asm volatile("s_waitcnt vmcnt(%1)" : "+v"(hf[j][s]) : "i"(13 - s + (j == 0 && s > G2_PL ? 1 : 0)) : "memory");
        if constexpr (s == 0) POLL_KEEP();
        if constexpr (j == 0 && s == 0) { PIN14(w2f, 0); PIN14(w2f, 14); }
        if constexpr (j == 0 && s == G2_PL) POLL_ISSUE(pv, c2l, 1);
        acc[0] = MFMA32(w2f[2 * s], hf[j][s], acc[0]);
        acc[1] = MFMA32(w2f[2 * s + 1], hf[j][s], acc[1]);
        SB();
      });
      if constexpr (j == 0) {
        STAMP(14);
        asm volatile("s_waitcnt vmcnt(0)" : "+v"(pv) :: "memory");
        if (__builtin_amdgcn_readfirstlane(pv) < 14u) wait_cnt<1, 6>(c2l, 14u, pl0, pl1, pl2);
        STAMP(9);
        H_LOAD(1, 0, 14);
      } else {
        STAMP(7);
      }
      char* red = smem + j * 65536;
#pragma unroll
      for (int P = 0; P < 8; ++P)
        *(f32x4*)(red + (P * 8 + w) * 1024 + lane * 16) = f32x4{acc[P & 1][4 * (P >> 1)], acc[P & 1][4 * (P >> 1) + 1], acc[P & 1][4 * (P >> 1) + 2], acc[P & 1][4 * (P >> 1) + 3]};
      if constexpr (j == 1) {
        asm volatile("s_waitcnt lgkmcnt(0)\n\ts_barrier" ::: "memory");
#pragma unroll
        for (int jj = 0; jj < 2; ++jj) {
          const char* rp = smem + jj * 65536 + w * 8192 + lane * 16;
          const f32x4 y = ((*(const f32x4*)rp + *(const f32x4*)(rp + 1024)) + (*(const f32x4*)(rp + 2048) + *(const f32x4*)(rp + 3072))) +
                          ((*(const f32x4*)(rp + 4096) + *(const f32x4*)(rp + 5120)) + (*(const f32x4*)(rp + 6144) + *(const f32x4*)(rp + 7168)));
          const u32x2 xr = trread(smem + G2_X1 + jj * 4096 + (32 * dt + 8 * kq + 4 * hi + qq) * 64 + (G & 1) * 32 + pp * 8);
          u32x2 o; o.x = pkbf(__builtin_fmaf(y[0], gbv[0], bflo(xr.x)), __builtin_fmaf(y[1], gbv[1], bfhi(xr.x)));
          o.y = pkbf(__builtin_fmaf(y[2], gbv[2], bflo(xr.y)), __builtin_fmaf(y[3], gbv[3], bfhi(xr.y)));
          ostore(o, out + (size_t)(32 * (jj ? tl : te) + (lane & 31)) * D_ + 64 * dg + 32 * dt + 8 * kq + 4 * hi);
        }
      }
      SB();
    });
  }
  STAMP(15);
  STAMP_OUT(stamps, 1024 + b);
  STAMP_OUT2(stamps, 2048 + b);
}

extern "C" void kernel_launch(void* const* d_in, const int* in_sizes, int n_in, void* d_out, int out_size, void* d_ws, size_t ws_size,
                              hipStream_t stream) {
  const u16* x = (const u16*)d_in[0]; const u16* logA = (const u16*)d_in[1]; const u16* Aim = (const u16*)d_in[2];
  const u16* Cr = (const u16*)d_in[3]; const u16* Ci = (const u16*)d_in[4]; const u16* logdt = (const u16*)d_in[5]; const u16* Dsk = (const u16*)d_in[6];
  const u16* w1 = (const u16*)d_in[8]; const u16* w3 = (const u16*)d_in[9]; const u16* w2 = (const u16*)d_in[10];
  const u16* c = (const u16*)d_in[11]; const u16* wm = (const u16*)d_in[12];
  char* ws = (char*)d_ws;
  unsigned long long* stamps = (unsigned long long*)(ws + (64u << 20));
  k1_prep<<<K1_NE + K1_NC + K1_NA + K1_NB, 256, 0, stream>>>(x, logA, Aim, Cr, Ci, logdt, c, wm, w1, w3, ws, stamps);
  kf_main<<<256, 512, 0, stream>>>(Dsk, w2, ws, (u16*)d_out, stamps);
}
```

```cpp
#define DIAG_A 0
#define DIAG_B 0
#include <hip/hip_runtime.h>

typedef unsigned short u16;
typedef unsigned u32;
typedef u32 u32x4 __attribute__((ext_vector_type(4)));
typedef u32 u32x2 __attribute__((ext_vector_type(2)));
typedef short s16x4 __attribute__((ext_vector_type(4)));
typedef __bf16 bf16x8 __attribute__((ext_vector_type(8)));
typedef __bf16 bf16x2 __attribute__((ext_vector_type(2)));
typedef _Float16 f16x8 __attribute__((ext_vector_type(8)));
typedef _Float16 f16x2 __attribute__((ext_vector_type(2)));
typedef float f32x2 __attribute__((ext_vector_type(2)));
typedef float f32x4 __attribute__((ext_vector_type(4)));
typedef float f32x16 __attribute__((ext_vector_type(16)));
#define DI __device__ __forceinline__
#define U2F(x) __builtin_bit_cast(float, (u32)(x))
#define F2U(x) __builtin_bit_cast(u32, (float)(x))
#define I2F(x) __builtin_bit_cast(float, (int)(x))
#define F2I(x) __builtin_bit_cast(int, (float)(x))
#if defined(__HIP_DEVICE_COMPILE__)
#define NOPK __attribute__((target("no-packed-fp32-ops")))
#else
#define NOPK
#endif
#define LDSP(T, p) ((__attribute__((address_space(3))) T*)(p))
#define GAS __attribute__((address_space(1)))
#define RLX_AGENT __ATOMIC_RELAXED, __HIP_MEMORY_SCOPE_AGENT
#define SB() __builtin_amdgcn_sched_barrier(0)
#define LDIN(p) __builtin_nontemporal_load(p)
template <int V> struct IC { static constexpr int value = V; };
template <int I, int N, class F> __device__ __forceinline__ void static_for(F&& f) { if constexpr (I < N) { f(IC<I>{}); static_for<I + 1, N>(f); } }

constexpr int L_ = 2048, D_ = 448, N_ = 256, F_ = 1792;

constexpr int EP_E = 0;
constexpr int EP_R = 64;
constexpr int C0_CNT = 1024;
constexpr int C1_CNT = 5120;
constexpr int C2_CNT = 10240;
constexpr int ST2_OFF = 106496;
constexpr int ZERO_LO = 20480, ZERO_HI = 122880;
constexpr size_t WS_MODA = 122880;
constexpr size_t WS_MODB = WS_MODA + 448 * 32;
constexpr size_t WS_MODC = WS_MODB + 448 * 16;
constexpr size_t WS_ST1 = 151552;
constexpr size_t WS_XT = WS_ST1 + 16384;
constexpr size_t WS_X1T = WS_XT + 64 * 448 * 64;
constexpr size_t WS_XB = WS_X1T + 64 * 448 * 64;
constexpr size_t WS_W13 = WS_XB + 64 * 448 * 64;
constexpr size_t WS_W2F = WS_W13 + 112 * 28 * 1024;
constexpr size_t WS_HF = WS_W2F + 14 * 112 * 1024;
constexpr size_t WS_END = WS_HF + 64 * 112 * 1024;

DI float bflo(u32 v) { return U2F(v << 16); }
DI float bfhi(u32 v) { return U2F(v & 0xffff0000u); }
DI float bf2f(u16 v) { return U2F(((u32)v) << 16); }
DI u32 pkbf(float lo, float hi) { f32x2 p = {lo, hi}; return __builtin_bit_cast(u32, __builtin_convertvector(p, bf16x2)); }
DI u32 pkh(f32x2 p) { return __builtin_bit_cast(u32, __builtin_convertvector(p, f16x2)); }
DI float rbf(float f) { return bflo(pkbf(f, f)); }
DI float dot2(u32 a, u32 b, float c) { return __builtin_amdgcn_fdot2_f32_bf16(__builtin_bit_cast(bf16x2, a), __builtin_bit_cast(bf16x2, b), c, false); }
DI float fexp(float x) { return __builtin_amdgcn_exp2f(x * 1.4426950408889634f); }
DI float silu_bf(float c) { return rbf(c * __builtin_amdgcn_rcpf(1.f + fexp(-c))); }
template <int CTRL> DI float dppf(float x) { return I2F(__builtin_amdgcn_update_dpp(0, F2I(x), CTRL, 0xf, 0xf, false)); }
DI float sum16(float x) { x += dppf<0x128>(x); x += dppf<0x124>(x); x += dppf<0x122>(x); x += dppf<0x121>(x); return x; }
DI float max16(float x) { x = __builtin_fmaxf(x, dppf<0x128>(x)); x = __builtin_fmaxf(x, dppf<0x124>(x)); x = __builtin_fmaxf(x, dppf<0x122>(x)); x = __builtin_fmaxf(x, dppf<0x121>(x)); return x; }
DI float add32(float x) { auto r = __builtin_amdgcn_permlane32_swap(F2U(x), F2U(x), false, false); return U2F(r[0]) + U2F(r[1]); }
DI float add16(float x) { auto r = __builtin_amdgcn_permlane16_swap(F2U(x), F2U(x), false, false); return U2F(r[0]) + U2F(r[1]); }
DI float max64(float x) {
  x = max16(x);
  auto r = __builtin_amdgcn_permlane16_swap(F2U(x), F2U(x), false, false);
  x = __builtin_fmaxf(U2F(r[0]), U2F(r[1]));
  auto q = __builtin_amdgcn_permlane32_swap(F2U(x), F2U(x), false, false);
  return __builtin_fmaxf(U2F(q[0]), U2F(q[1]));
}
DI u32x2 trread(const char* p) { return __builtin_bit_cast(u32x2, __builtin_amdgcn_ds_read_tr16_b64_v4i16(LDSP(s16x4, p))); }
DI u32x2 trread3(const __attribute__((address_space(3))) char* p) { return __builtin_bit_cast(u32x2, __builtin_amdgcn_ds_read_tr16_b64_v4i16((__attribute__((address_space(3))) s16x4*)p)); }
DI void pinf(float& v) { asm volatile("" : "+v"(v)); }
DI void pin(u32x4& v) { asm volatile("" : "+v"(v)); }
DI void ostore(u32x2 v, u16* p) { *(u32x2*)p = v; }
DI f32x2 cmulp(f32x2 a, f32x2 z, f32x2 zs) { return __builtin_elementwise_fma(a.yx, zs, a * z.xx); }

#define STAMPS
DI unsigned long long RT() { unsigned long long t; SB(); asm volatile("s_memrealtime %0\n\ts_waitcnt lgkmcnt(0)" : "=s"(t) :: "memory"); SB(); return t; }
#define STAMP_DECL unsigned long long stv[16] = {0, 0, 0, 0, 0, 0, 0, 0, 0, 0, 0, 0, 0, 0, 0, 0}
#define STAMP(i) stv[i] = RT()
DI unsigned long long CT() { unsigned long long t; SB(); asm volatile("s_memtime %0\n\ts_waitcnt lgkmcnt(0)" : "=s"(t) :: "memory"); SB(); return t; }
#define CSTAMP(i) ctv[i] = CT()
#define STAMP_OUT(buf, slot) do { if (__builtin_amdgcn_workitem_id_x() == 0) { _Pragma("unroll") for (int i_ = 0; i_ < 8; ++i_) (buf)[(slot) * 8 + i_] = stv[i_]; } } while (0)
#define STAMP_OUT2(buf, slot) do { if (__builtin_amdgcn_workitem_id_x() == 0) { _Pragma("unroll") for (int i_ = 0; i_ < 8; ++i_) (buf)[(slot) * 8 + i_] = stv[8 + i_]; } } while (0)

#define MFMA32(a, b, c) __builtin_amdgcn_mfma_f32_32x32x16_bf16(__builtin_bit_cast(bf16x8, a), __builtin_bit_cast(bf16x8, b), c, 0, 0, 0)
#define ST16(v, off) __builtin_amdgcn_raw_buffer_store_b128(v, rs, (int)(off), 0, 16)
template <int IMM> DI u32x4 gload(u32 voff, const char* sbase) {
  u32x4 r; asm volatile("global_load_dwordx4 %0, %1, %2 offset:%3 sc1" : "=v"(r) : "v"(voff), "s"(sbase), "i"(IMM)); return r;
}
DI void glds16(u32 voff, const char* sbase, u32 ldsaddr) {
  asm volatile("s_mov_b32 m0, %2\n\ts_nop 0\n\tglobal_load_lds_dwordx4 %0, %1 sc1" :: "v"(voff), "s"(sbase), "s"(ldsaddr) : "memory");
}
constexpr unsigned SPIN_CAP = 1u << 16;
DI void sgload(unsigned long long& g, GAS unsigned long long* p) { asm volatile("global_load_dwordx2 %0, %1, off sc1" : "+v"(g) : "v"(p) : "memory"); }
DI void sgwait(unsigned long long& g) { asm volatile("s_waitcnt vmcnt(1)" : "+v"(g) :: "memory"); }
DI void sgreload(unsigned long long& g, GAS unsigned long long* p) { asm volatile("global_load_dwordx2 %0, %1, off sc1\n\ts_waitcnt vmcnt(0)" : "+v"(g) : "v"(p) : "memory"); }
#define POLL_ISSUE(v, p, m) asm volatile("s_mov_b64 exec, %3\n\tglobal_atomic_or %0, %1, %2, off sc0\n\ts_mov_b64 exec, -1" : "+v"(v) : "v"(p), "v"(0), "i"(m) : "memory")
template <int NL, int SLP> DI void wait_cnt(GAS u32* p, u32 want, u32& v0, u32& v1, u32& v2) {
  v0 = want; v1 = want; v2 = want;
  POLL_ISSUE(v0, p, (1 << NL) - 1); __builtin_amdgcn_s_sleep(SLP);
  POLL_ISSUE(v1, p, (1 << NL) - 1); __builtin_amdgcn_s_sleep(SLP);
#pragma unroll 1
  for (unsigned spins = 0; spins < SPIN_CAP; ++spins) {
    POLL_ISSUE(v2, p, (1 << NL) - 1);
    asm volatile("s_waitcnt vmcnt(2)" : "+v"(v0) :: "memory");
    if (__builtin_amdgcn_ballot_w64(v0 < want) == 0) break;
    POLL_ISSUE(v0, p, (1 << NL) - 1);
    asm volatile("s_waitcnt vmcnt(2)" : "+v"(v1) :: "memory");
    if (__builtin_amdgcn_ballot_w64(v1 < want) == 0) break;
    POLL_ISSUE(v1, p, (1 << NL) - 1);
    asm volatile("s_waitcnt vmcnt(2)" : "+v"(v2) :: "memory");
    if (__builtin_amdgcn_ballot_w64(v2 < want) == 0) break;
  }
}
#define POLL_KEEP() asm volatile("" :: "v"(pl0), "v"(pl1), "v"(pl2) : "memory")
#define WAIT_VM0() __builtin_amdgcn_s_waitcnt(0x0F70)
#define BAR() __builtin_amdgcn_s_barrier()

constexpr int CV_DSTR = 4 * 4160 + 4608;
constexpr int CV_RED = 2 * CV_DSTR;
constexpr int G1_ST = 4 * 28672;
constexpr int G2_RED = 4 * 28672;
constexpr int G2_X1 = 18 * 8192;
constexpr int TAB_GB = G2_X1 + 8192;
constexpr int TAB_SB = TAB_GB + 256;
constexpr int ARR_CNT = TAB_SB + 1792;
constexpr int KF_LDS = ARR_CNT + 16;
constexpr int KG_REG = 32 * 272;
constexpr int TASK_LDS = 8 * KG_REG;
constexpr int KG_TAB = 126976;
#define WSLOT stv[0]
#define W2_N0 13
#define G2_B1 6
#define G2_PL 8
#define EPI0 7
#define W_N0 4
#define W_NM 18
#define W_ST 4
#define W_NB 2
#define W_N1 (W_N0 + W_NB)
#define W_N2 (W_N1 + W_NM)
#define SC_SIG 6
#define SC_POLL 21
#define LAS __attribute__((address_space(3)))
#define PIN14(a, o) asm volatile("" : "+v"(a[o]), "+v"(a[o + 1]), "+v"(a[o + 2]), "+v"(a[o + 3]), "+v"(a[o + 4]), "+v"(a[o + 5]), "+v"(a[o + 6]), "+v"(a[o + 7]), \
    "+v"(a[o + 8]), "+v"(a[o + 9]), "+v"(a[o + 10]), "+v"(a[o + 11]), "+v"(a[o + 12]), "+v"(a[o + 13]) :: "memory")

constexpr int g1_e0(int t) { return t == 1 ? EPI0 : 3; }
constexpr int g1_st(int t) { return g1_e0(t) + 19; }
constexpr int g1_an(int t) { return t == 2 ? 18 : 14; }
constexpr int g1_w2(int n) {
  if (n >= 25 && n < 25 + 14 * 4 && (n - 25) % 4 == 0) return (n - 25) / 4;
  if (n >= 84 && n < 84 + 23) { const int q = n - 84; if (q % 3 != 2) return 14 + q - q / 3 < 28 ? 14 + q - q / 3 : -1; }
  return -1;
}
constexpr int g1_sg(int t) { return t == 0 ? 24 : 28 * t + 10; }
constexpr int g1_vm(int a, int b) {
  int c = 0;
  for (int n = a; n < b; ++n) {
    const int t = n / 28, q = n % 28;
    if (n > a) { if (t >= 2 && q == g1_an(t)) ++c; if (n == 108) ++c; if (n == g1_sg(t)) ++c; }
    if (t >= 1 && q == g1_st(t) && n != a) ++c;
    if (g1_w2(n) >= 0) ++c;
  }
  return c;
}
__global__ __launch_bounds__(512) void kf_main(const u16* __restrict__ x, const u16* __restrict__ logA, const u16* __restrict__ Aim,
                                               const u16* __restrict__ Cr, const u16* __restrict__ Ci, const u16* __restrict__ logdt,
                                               const u16* __restrict__ Dsk, const u16* __restrict__ w1, const u16* __restrict__ w3,
                                               const u16* __restrict__ w2, const u16* __restrict__ cvec, const u16* __restrict__ wmod,
                                               char* __restrict__ ws, u16* __restrict__ out, unsigned long long* stamps) {
  __shared__ __attribute__((aligned(16))) char smem[KF_LDS];
  const int tid = __builtin_amdgcn_workitem_id_x(), lane = tid & 63;
  const int w = __builtin_amdgcn_readfirstlane(tid >> 6);
  const int b = __builtin_amdgcn_workgroup_id_x(), xcd = b & 7, idx = b >> 3;
  const int hi = lane >> 5;
  const int G = lane >> 4, qq = (lane & 15) >> 2, pp = lane & 3;
  const u32 voff = lane * 16;
  GAS u32* ctl = (GAS u32*)ws;
  auto rs = __builtin_amdgcn_make_buffer_rsrc(ws, 0, (int)WS_END, 0x00020000);
  const u32 lds0 = (u32)(size_t)LDSP(char, smem);
  STAMP_DECL;
  STAMP(0);
  u32 pl0 = 0, pl1 = 0, pl2 = 0;
  const bool mainwg = b < 224;
  const int dl = w >> 2, q4 = w & 3, t256 = tid & 255;
  int kg_wb, kg_wst;
  unsigned long long epoch;
  {
    u32 lar = 0, air = 0, crr = 0, cir = 0, ldt = 0;
    if (mainwg) {
      const int d = 2 * b + dl;
      asm volatile("global_load_ushort %0, %5, %6 nt\n\tglobal_load_ushort %1, %5, %7 nt\n\tglobal_load_ushort %4, %10, %11\n\tglobal_load_ushort %2, %5, %8 nt\n\tglobal_load_ushort %3, %5, %9 nt"
                   : "+v"(lar), "+v"(air), "+v"(crr), "+v"(cir), "+v"(ldt) : "v"((d * N_ + t256) * 2), "s"(logA), "s"(Aim), "s"(Cr), "s"(Ci), "v"(d * 2), "s"(logdt) : "memory");
    }
    unsigned long long ep = 0;
    const int task = dl == 0 ? b : (b >= 252 ? b + 4 : -1);
    char* stg = smem + TASK_LDS + dl * 30464;
    u32x4 v[7]; u32 cv[7];
    if (task >= 0) {
      const u16* src; size_t step;
      if (task < 64) { src = x + (size_t)(task * 32 + (t256 >> 3)) * D_ + (t256 & 7) * 8; step = 64; }
      else if (task < 148) { const int m = task - 64; src = wmod + (size_t)(224 * (m & 1) + (t256 >> 3)) * 2688 + (m >> 1) * 64 + (t256 & 7) * 8; step = 32 * 2688; }
      else { const int m = task - 148; src = ((t256 & 8) ? w3 : w1) + (size_t)(112 * (m & 3) + (t256 >> 4)) * F_ + (m >> 2) * 64 + (t256 & 7) * 8; step = 16 * F_; }
#pragma unroll
      for (int i = 0; i < 7; ++i) v[i] = LDIN((const u32x4*)(src + i * step));
#pragma unroll
      for (int i = 0; i < 7; ++i) cv[i] = cvec[224 * (task & 1) + (t256 >> 3) + 32 * i];
    } else if (!mainwg) {
      const int zo = ZERO_LO + (b - 224) * 4096 + t256 * 16;
      if (zo < ZERO_HI) ST16((u32x4{0, 0, 0, 0}), zo);
      if (b == 251 && w == 5) {
        const unsigned long long e0 = __hip_atomic_load((GAS unsigned long long*)(ctl + EP_E), RLX_AGENT);
        __hip_atomic_store(ctl + C0_CNT + lane * 32, 0u, RLX_AGENT);
        __hip_atomic_store(ctl + C0_CNT + 2048 + lane * 32, 0u, RLX_AGENT);
        asm volatile("s_waitcnt vmcnt(0)" ::: "memory");
        if (lane < 16) __hip_atomic_store((GAS unsigned long long*)(ctl + EP_R + lane * 32), e0 + 1, RLX_AGENT);
      }
    }
    if (tid == 0) *(u32*)(smem + ARR_CNT) = 0;
    SB();
    const int s = lane & 31;
    {
      const int kw = s < 8 ? 0 : (s < 20 ? (s >> 2) - 1 : 3);
      int wb = s < 12 ? (s < 8 ? s : 248 + s) : (s < 16 ? 384 + ((s & 2) << 5) + (s & 1) : 32 * s);
      kg_wst = 16 >> kw;
      kg_wb = (wb > 672 ? 672 : wb) * 2 + KG_TAB + dl * 1536 + (8 * q4 + 4 * hi) * kg_wst;
    }
    char* reg = smem + w * KG_REG;
    f16x8 bfr[8];
    f32x2 z8, z16; float scl = 0.f, fs = 0.f;
    if (mainwg) {
      asm volatile("s_waitcnt vmcnt(14)" : "+v"(lar), "+v"(air), "+v"(crr), "+v"(cir), "+v"(ldt) :: "memory");
      if (dl) asm volatile("s_waitcnt vmcnt(0)" : "+v"(lar), "+v"(air), "+v"(crr), "+v"(cir), "+v"(ldt) :: "memory");
      const float dt = rbf(fexp(bflo(ldt)));
      const float ar = -rbf(fexp(bflo(lar))) * dt * 1.4426950408889634f;
      const float ai = bflo(air) * dt;
      const float I2PH = 0.15915494f, I2PL = 6.4206382e-9f;
      const float th = ai * I2PH;
      const float tl = __builtin_fmaf(ai, I2PL, __builtin_fmaf(ai, I2PH, -th));
      auto zpow = [&](float sg) __attribute__((always_inline)) {
        float e = __builtin_amdgcn_exp2f(ar * sg);
        float rev = __builtin_fmaf(tl, sg, __builtin_amdgcn_fractf(th * sg));
        return f32x2{e * __builtin_amdgcn_cosf(rev), e * __builtin_amdgcn_sinf(rev)};
      };
      const f32x2 z1 = zpow(1.f), z256 = zpow(256.f);
      z16 = zpow(16.f);
      const f32x2 s1 = {-z1.y, z1.y}, s256 = {-z256.y, z256.y};
      u32 eb = ((crr & 0x7f80u) > (cir & 0x7f80u) ? (crr & 0x7f80u) : (cir & 0x7f80u));
      asm("s_nop 1\n\tv_max_u32_dpp %0, %0, %0 row_ror:8 row_mask:0xf bank_mask:0xf\n\ts_nop 1\n\tv_max_u32_dpp %0, %0, %0 row_ror:4 row_mask:0xf bank_mask:0xf\n\ts_nop 1\n\t"
          "v_max_u32_dpp %0, %0, %0 row_ror:2 row_mask:0xf bank_mask:0xf\n\ts_nop 1\n\tv_max_u32_dpp %0, %0, %0 row_ror:1 row_mask:0xf bank_mask:0xf\n\ts_nop 1" : "+v"(eb));
      {
        const u32 e0 = __builtin_amdgcn_readlane(eb, 0), e1 = __builtin_amdgcn_readlane(eb, 16), e2 = __builtin_amdgcn_readlane(eb, 32), e3 = __builtin_amdgcn_readlane(eb, 48);
        const u32 m0 = e0 > e1 ? e0 : e1, m1 = e2 > e3 ? e2 : e3;
        eb = (m0 > m1 ? m0 : m1) >> 7;
      }
      eb = eb < 1u ? 1u : (eb > 252u ? 252u : eb);
      scl = U2F((253u - eb) << 23);
      fs = 2.f * dt * U2F((eb + 1u) << 23);
      f32x2 lo[9], hp[9];
      lo[1] = z1; hp[1] = z256;
#pragma unroll
      for (int i = 2; i <= 8; ++i) { lo[i] = cmulp(lo[i - 1], z1, s1); hp[i] = cmulp(hp[i - 1], z256, s256); }
      const f32x2 a2 = cmulp(lo[2], z256, s256), a4 = cmulp(lo[4], z256, s256), a6 = cmulp(lo[6], z256, s256);
      const f32x2 b4 = cmulp(a4, z256, s256), c4 = cmulp(b4, z256, s256);
      const u32 col[21] = {0x00003c00u, pkh(lo[1]), pkh(lo[2]), pkh(lo[3]), pkh(lo[4]), pkh(lo[5]), pkh(lo[6]), pkh(lo[7]),
                           pkh(hp[1]), pkh(a2), pkh(a4), pkh(a6), pkh(hp[2]), pkh(b4), pkh(hp[3]), pkh(c4),
                           pkh(hp[4]), pkh(hp[5]), pkh(hp[6]), pkh(hp[7]), pkh(hp[8])};
#pragma unroll
      for (int i = 0; i < 21; ++i) *(u32*)(reg + i * 272 + lane * 4) = col[i];
      z8 = lo[8];
#pragma unroll
      for (int i = 0; i < 8; ++i) bfr[i] = *(const f16x8*)(reg + s * 272 + i * 32 + hi * 16);
      asm volatile("s_waitcnt lgkmcnt(0)" : "+v"(bfr[0]), "+v"(bfr[1]), "+v"(bfr[2]), "+v"(bfr[3]), "+v"(bfr[4]), "+v"(bfr[5]), "+v"(bfr[6]), "+v"(bfr[7]) :: "memory");
    }
    STAMP(8);
    int pass;
    asm("v_mov_b32 %0, 0" : "=v"(pass));
    pass = __builtin_amdgcn_readfirstlane(pass);
    for (; pass < 2; ++pass) {
      if (task >= 0) {
        if (pass == 0) {
          if (task < 64) {
            const int row = t256 >> 3, p = t256 & 7;
            float sm = 0.f, sq = 0.f;
#pragma unroll
            for (int i = 0; i < 7; ++i) {
              pin(v[i]);
              *(u32x4*)(stg + row * 928 + (p + 8 * i) * 16) = v[i];
#pragma unroll
              for (int j = 0; j < 4; ++j) { sm = dot2(v[i][j], 0x3f803f80u, sm); sq = dot2(v[i][j], v[i][j], sq); }
            }
            sm += dppf<0xB1>(sm); sq += dppf<0xB1>(sq); sm += dppf<0x4E>(sm); sq += dppf<0x4E>(sq); sm += dppf<0x141>(sm); sq += dppf<0x141>(sq);
            const float m = sm * (1.f / D_);
            const float var = __builtin_fmaxf(sq * (1.f / D_) - m * m, 0.f);
            const float sd = rbf(__builtin_amdgcn_sqrtf(rbf(rbf(var) + 1e-5f)));
            const float rsd = __builtin_amdgcn_rcpf(sd);
            if (p < 2) __builtin_amdgcn_raw_buffer_store_b32(F2U(p ? -rbf(m) * rsd : rsd), rs, (int)WS_ST1 + p * 8192 + (task * 32 + row) * 4, 0, 16);
          } else if (task < 148) {
            float acc[8] = {0, 0, 0, 0, 0, 0, 0, 0};
#pragma unroll
            for (int i = 0; i < 7; ++i) {
              asm volatile("" : "+v"(cv[i]));
              const u32 sh = F2U(silu_bf(bflo(cv[i]))), sl = sh >> 16;
#pragma unroll
              for (int j = 0; j < 4; ++j) { acc[2 * j] = dot2(v[i][j], sl, acc[2 * j]); acc[2 * j + 1] = dot2(v[i][j], sh, acc[2 * j + 1]); }
            }
#pragma unroll
            for (int j = 0; j < 8; ++j) { acc[j] += dppf<0x128>(acc[j]); acc[j] = add32(acc[j]); }
            if ((lane & 40) == 0) {
              float* red = (float*)stg + (q4 * 2 + (lane >> 4)) * 64 + (lane & 7) * 8;
              *(f32x4*)red = f32x4{acc[0], acc[1], acc[2], acc[3]};
              *(f32x4*)(red + 4) = f32x4{acc[4], acc[5], acc[6], acc[7]};
            }
          } else {
#pragma unroll
            for (int i = 0; i < 7; ++i) *(u32x4*)(stg + ((t256 >> 4) + 16 * i) * 272 + (t256 & 15) * 16) = v[i];
          }
        } else {
          if (task >= 64 && task < 148) {
            if (q4 == 0) {
              const int m = task - 64, cg = m >> 1;
              float t = 0.f;
#pragma unroll
              for (int j = 0; j < 8; ++j) t += ((const float*)stg)[j * 64 + lane];
              const int sec = cg / 7, dd = (cg % 7) * 64 + lane;
              const int o1 = sec == 3 ? (int)WS_MODB + dd * 16 : (sec == 5 ? (int)WS_MODC + dd * 8 : (int)WS_MODA + dd * 32 + (sec == 4 ? 3 : sec) * 8);
              __builtin_amdgcn_raw_buffer_store_b32(F2U(t), rs, o1 + (m & 1) * 4, 0, 16);
              if (sec == 4) __builtin_amdgcn_raw_buffer_store_b32(F2U(t), rs, (int)WS_MODB + dd * 16 + 8 + (m & 1) * 4, 0, 16);
            }
          } else {
            const char* rb; int rstep, r4, dst;
            if (task < 64) { rb = stg + (8 * G + qq) * 928 + q4 * 224 + pp * 8; rstep = 32; r4 = 4 * 928; dst = (int)WS_XT + task * 28672 + q4 * 7168 + (lane & 15) * 64 + G * 16; }
            else { const int m = task - 148; rb = stg + (8 * hi + qq) * 272 + (G & 1) * 128 + (16 * q4 + 8 * (pp & 1) + 4 * (pp >> 1)) * 2; rstep = 16 * 272; r4 = 4 * 272;
                   dst = (int)WS_W13 + ((4 * (m >> 2) + q4) * 28 + 7 * (m & 3)) * 1024 + lane * 16; }
#pragma unroll
            for (int i = 0; i < 7; ++i) {
              const u32x2 lo = trread(rb + i * rstep), hh = trread(rb + i * rstep + r4);
              ST16((u32x4{lo.x, lo.y, hh.x, hh.y}), dst + i * 1024);
            }
          }
        }
      }
      if (mainwg && pass != dl) {
        pinf(scl);
        f32x2 ra = {bflo(crr) * scl, -bflo(cir) * scl};
        f32x2 rb = cmulp(ra, z8, f32x2{z8.y, -z8.y});
        const f32x2 s16c = {z16.y, -z16.y};
#pragma unroll
        for (int r = 0; r < 32; r += 2) {
          *(u32*)(reg + r * 272 + lane * 4) = pkh(ra); *(u32*)(reg + (r + 1) * 272 + lane * 4) = pkh(rb);
          if (r < 30) { ra = cmulp(ra, z16, s16c); rb = cmulp(rb, z16, s16c); }
        }
        f32x16 acc = {};
#pragma unroll
        for (int i = 0; i < 8; ++i) {
          const f16x8 af = *(const f16x8*)(reg + s * 272 + i * 32 + hi * 16);
          acc = __builtin_amdgcn_mfma_f32_32x32x16_f16(af, bfr[i], acc, 0, 0, 0);
        }
#pragma unroll
        for (int qd = 0; qd < 4; ++qd)
          *(f32x4*)(reg + qd * 1024 + lane * 16) = f32x4{acc[4 * qd] * fs, acc[4 * qd + 1] * fs, acc[4 * qd + 2] * fs, acc[4 * qd + 3] * fs};
      }
      if (pass == 0) { asm volatile("s_waitcnt lgkmcnt(0)\n\ts_barrier" ::: "memory"); STAMP(13); }
    }
    if (dl == 0 || !mainwg) {
      WAIT_VM0();
      asm volatile("s_waitcnt vmcnt(0)\n\ts_mov_b64 exec, 1\n\tds_add_u32 %0, %1\n\ts_mov_b64 exec, -1" :: "v"(lds0 + ARR_CNT), "v"(1) : "memory");
    }
    if (w == 4) {
      GAS u32* rp = ctl + EP_R + (b & 15) * 32;
#pragma unroll 1
      for (unsigned spins = 0; spins < SPIN_CAP; ++spins) {
        unsigned long long r;
        asm volatile("s_mov_b64 exec, 1\n\tglobal_load_dwordx2 %1, %4, off sc1\n\tglobal_atomic_or_x2 %0, %2, %3, off sc0\n\ts_mov_b64 exec, -1\n\ts_waitcnt vmcnt(0)"
                     : "=&v"(r), "=&v"(ep) : "v"(rp), "v"(0ull), "v"(ctl + EP_E) : "memory");
        ep += 1;
        if ((__builtin_amdgcn_ballot_w64(r != ep) & 1) == 0) break;
      }
      const u32 need = mainwg ? 4u : 8u;
#pragma unroll 1
      for (unsigned spins = 0; spins < 16 * SPIN_CAP; ++spins) {
        if (*(volatile LAS u32*)(size_t)(lds0 + ARR_CNT) >= need) break;
        __builtin_amdgcn_s_sleep(1);
      }
      STAMP(11);
      if (lane < 16) __hip_atomic_fetch_add(ctl + C0_CNT + (lane * 8 + xcd) * 32, 1u, RLX_AGENT);
      if (mainwg) wait_cnt<8, 6>(ctl + C0_CNT + ((b & 15) * 8 + lane) * 32, 32u, pl0, pl1, pl2);
    }
    epoch = ep;
    asm volatile("s_waitcnt lgkmcnt(0)\n\ts_barrier" ::: "memory");
  }
  if (!mainwg) {
    const int fr = b - 224;
    if (fr < 28) {
      u32x4 v[7];
#pragma unroll
      for (int i = 0; i < 7; ++i) v[i] = LDIN((const u32x4*)(w2 + (size_t)(64 * fr + (tid >> 3)) * D_ + ((tid & 7) + 8 * i) * 8));
#pragma unroll
      for (int i = 0; i < 7; ++i) *(u32x4*)(smem + (tid >> 3) * 912 + ((tid & 7) + 8 * i) * 16) = v[i];
      asm volatile("s_waitcnt lgkmcnt(0)\n\ts_barrier" ::: "memory");
      const char* rbase = smem + (16 * (w & 3) + 8 * hi + qq) * 912 + (w >> 2) * 448 + (16 * (G & 1) + 4 * pp) * 2;
      const int dst = (int)WS_W2F + ((7 * (w >> 2)) * 112 + 4 * fr + (w & 3)) * 1024 + lane * 16;
#pragma unroll
      for (int s = 0; s < 7; ++s) {
        u32x2 lo = trread(rbase + s * 64), hh = trread(rbase + s * 64 + 4 * 912);
        __builtin_amdgcn_raw_buffer_store_b128(u32x4{lo.x, lo.y, hh.x, hh.y}, rs, dst + s * 112 * 1024, 0, 16);
      }
    }
    asm volatile("s_waitcnt vmcnt(0)\n\ts_barrier" ::: "memory");
    if (w == 4) {
      wait_cnt<8, 6>(ctl + C0_CNT + ((b & 15) * 8 + lane) * 32, 32u, pl0, pl1, pl2);
      asm volatile("s_waitcnt vmcnt(0)" ::: "memory");
      POLL_KEEP();
      if (b == 251 && lane == 0) __hip_atomic_store((GAS unsigned long long*)(ctl + EP_E), epoch, RLX_AGENT);
      if (lane < 16 && fr < 28) __hip_atomic_fetch_add(ctl + C1_CNT + (lane * 9 + 8) * 32, 1u, RLX_AGENT);
      wait_cnt<8, 6>(ctl + C1_CNT + ((b & 15) * 9 + lane) * 32, 28u, pl0, pl1, pl2);
      asm volatile("s_waitcnt vmcnt(0)" ::: "memory");
      POLL_KEEP();
    }
    asm volatile("s_barrier" ::: "memory");
    {
      const char* xsrc = ws + WS_X1T + (size_t)(2 * fr + (w >> 2)) * 28672 + (w & 3) * 1024;
#pragma unroll
      for (int i = 0; i < 7; ++i) glds16(voff, xsrc + i * 4096, lds0 + w * 7168 + i * 1024);
      const char* sp = smem + w * 7168 + (8 * hi + qq) * 64 + (G & 1) * 32 + pp * 8;
      float s0 = 0.f, q0 = 0.f, s1 = 0.f, q1 = 0.f;
      static_for<0, 7>([&](auto ic) __attribute__((always_inline)) {
        constexpr int i = decltype(ic)::value;
        asm volatile("s_waitcnt vmcnt(%0)" :: "i"(6 - i) : "memory");
        const u32x2 lo = trread(sp + i * 1024), hh = trread(sp + i * 1024 + 256);
        s0 = dot2(lo.x, 0x3f803f80u, s0); q0 = dot2(lo.x, lo.x, q0); s1 = dot2(lo.y, 0x3f803f80u, s1); q1 = dot2(lo.y, lo.y, q1);
        s0 = dot2(hh.x, 0x3f803f80u, s0); q0 = dot2(hh.x, hh.x, q0); s1 = dot2(hh.y, 0x3f803f80u, s1); q1 = dot2(hh.y, hh.y, q1);
      });
      auto r = __builtin_amdgcn_permlane32_swap(F2U(s0 + s1), F2U(q0 + q1), false, false);
      *(float*)(smem + 65536 + w * 256 + (lane & 31) * 8 + hi * 4) = U2F(r[0]) + U2F(r[1]);
      asm volatile("s_waitcnt lgkmcnt(0)\n\ts_barrier" ::: "memory");
      if (w == 0) {
        const char* pt = smem + 65536 + hi * 1024 + (lane & 31) * 8;
        const f32x2 ts = (*(const f32x2*)pt + *(const f32x2*)(pt + 256)) + (*(const f32x2*)(pt + 512) + *(const f32x2*)(pt + 768));
        const float m = ts[0] * (1.f / D_);
        const float var = __builtin_fmaxf(ts[1] * (1.f / D_) - m * m, 0.f);
        const float sd = __builtin_amdgcn_sqrtf(rbf(rbf(var) + 1e-5f));
        __hip_atomic_store((GAS unsigned long long*)(ws + ST2_OFF) + 64 * fr + lane, (1ull << 32) | pkbf(-m, sd), RLX_AGENT);
      }
    }
    return;
  }
  const int rt = 4 * (xcd >> 1) + idx / 7, ct = 7 * (xcd & 1) + idx % 7;
  const int ft = 8 * ct + w;
  const int r2 = 4 * xcd + idx / 7, dg = idx % 7, kq = w >> 1, dt = w & 1;
  const int te = 4 * (r2 >> 1) + (r2 & 1), tl = 4 * (r2 >> 1) + 3 - (r2 & 1);
  const int wv = (int)WS_W13 + ft * 28672 + lane * 16;
#define WLOAD(p) wf[p] = __builtin_amdgcn_raw_buffer_load_b128(rs, wv + ((p) & 3) * 1024, ((p) & ~3) * 1024, 16)

  u32x4 wf[28];
  u32x4 wex;
  const int hbase = (int)WS_HF + (4 * rt * 112 + ft) * 1024 + lane * 16;

  {
    const int a = lane & 15;
    const int l0 = 256 * w + 16 * a + 4 * G;
    const int xoff = ((l0 >> 5) * 448 + 2 * b) * 64 + (l0 & 31) * 2;
    STAMP(9);
    u32x2 xx[2]; u32x4 st[2]; f32x4 ms[4];
#pragma unroll
    for (int i = 0; i < 2; ++i) {
      xx[i] = __builtin_amdgcn_raw_buffer_load_b64(rs, (int)WS_XT + xoff + i * 64, 0, 16);
      st[i] = __builtin_amdgcn_raw_buffer_load_b128(rs, (int)WS_ST1 + l0 * 4, i * 8192, 16);
    }
#pragma unroll
    for (int i = 0; i < 4; ++i) ms[i] = __builtin_bit_cast(f32x4, __builtin_amdgcn_raw_buffer_load_b128(rs, (int)WS_MODA + i * 16, b * 64, 16));
    const u32 dskr = *(const u32*)(Dsk + 2 * b);
    SB();
    {
      const char* rp = smem + dl * 4 * KG_REG + q4 * 1024 + lane * 16;
      const f32x4 t = (*(const f32x4*)rp + *(const f32x4*)(rp + KG_REG)) + (*(const f32x4*)(rp + 2 * KG_REG) + *(const f32x4*)(rp + 3 * KG_REG));
      const u32 ta = pkbf(t[0], t[1]), tc = pkbf(t[2], t[3]);
      char* q = smem + kg_wb;
      *(u16*)q = (u16)ta; *(u16*)(q + kg_wst) = (u16)(ta >> 16); *(u16*)(q + 2 * kg_wst) = (u16)tc; *(u16*)(q + 3 * kg_wst) = (u16)(tc >> 16);
    }
    asm volatile("s_waitcnt lgkmcnt(0)\n\ts_barrier" ::: "memory");
    SB();
    const f32x4 tb = __builtin_bit_cast(f32x4, __builtin_amdgcn_raw_buffer_load_b128(rs, tid < 448 ? (int)WS_MODB + tid * 16 : (int)WS_MODC + (64 * dg + tid - 448) * 8, 0, 16));
#pragma unroll
    for (int i = 0; i < W_N0; ++i) WLOAD(i);
    SB();
    {
      u32 kw[6];
#pragma unroll
      for (int h = 0; h < 2; ++h) {
        const int lb = 2040 - 8 * t256 - 8 * h;
        const int kr = lb < 256 ? 0 : 24 - __builtin_clz(lb);
        const float mB = U2F((u32)(150 + kr) << 23) + (float)(lb < 0 ? 0 : lb);
        const u32 kpa = (u32)(KG_TAB + dl * 1536) + 256u * kr - ((u32)(150 + kr) << 24);
#pragma unroll
        for (int e = 0; e < (h ? 2 : 4); ++e) {
          const u32 i0 = (F2U(mB + (float)(7 - 2 * e)) << 1) + kpa, i1 = (F2U(mB + (float)(6 - 2 * e)) << 1) + kpa;
          kw[4 * h + e] = (u32)*(const u16*)(smem + i0) | ((u32)*(const u16*)(smem + i1) << 16);
          if (h && t256 == 255) kw[4 * h + e] = 0;
        }
      }
      char* cp = smem + dl * CV_DSTR;
      if (t256 < 32) *(u32x4*)(cp + 4 * 4160 + t256 * 16) = u32x4{0, 0, 0, 0};
      else if (t256 < 40) *(u32x4*)(cp + ((t256 - 32) >> 1) * 4160 + 4096 + (t256 & 1) * 16) = u32x4{0, 0, 0, 0};
      u32 al[5];
#pragma unroll
      for (int i = 0; i < 5; ++i) al[i] = __builtin_amdgcn_alignbit(kw[i + 1], kw[i], 16);
      char* q = cp + t256 * 16;
      *(u32x4*)(q) = u32x4{kw[0], kw[1], kw[2], kw[3]};
      *(u32x4*)(q + 4160) = u32x4{al[0], al[1], al[2], al[3]};
      *(u32x4*)(q + 8320) = u32x4{kw[1], kw[2], kw[3], kw[4]};
      *(u32x4*)(q + 12480) = u32x4{al[1], al[2], al[3], al[4]};
    }
    float ga[2], b2v[2];
    const int swl = (lane ^ ((lane >> 5) << 3)) * 16;
#pragma unroll
    for (int i = 0; i < 2; ++i) {
      const float sa = rbf(ms[2 * i][0] + ms[2 * i][1]), beta = rbf(1.f + rbf(ms[2 * i][2] + ms[2 * i][3]));
      ga[i] = rbf(ms[2 * i + 1][0] + ms[2 * i + 1][1]); b2v[i] = rbf(1.f + rbf(ms[2 * i + 1][2] + ms[2 * i + 1][3]));
      const float gd = ga[i] * (i ? bfhi(dskr) : bflo(dskr));
      const float xv[4] = {bflo(xx[i].x), bfhi(xx[i].x), bflo(xx[i].y), bfhi(xx[i].y)};
      float hv[4];
#pragma unroll
      for (int e = 0; e < 4; ++e)
        hv[e] = __builtin_fmaf(__builtin_fmaf(xv[e], U2F(st[0][e]), U2F(st[1][e])), beta, sa);
      *(u32x2*)(smem + i * CV_DSTR + 4 * 4160 + 512 + l0 * 2) = u32x2{pkbf(hv[0], hv[1]), pkbf(hv[2], hv[3])};
      f32x4 base;
#pragma unroll
      for (int e = 0; e < 4; ++e) base[e] = __builtin_fmaf(hv[e], gd, xv[e]);
      *(f32x4*)(smem + CV_RED + (i * 40 + w * 5 + 4) * 1024 + swl) = base;
    }
    SB();
#pragma unroll
    for (int i = W_N0; i < W_N1; ++i) WLOAD(i);
    SB();
    asm volatile("s_waitcnt lgkmcnt(0)\n\ts_barrier" ::: "memory");
    POLL_KEEP();
    STAMP(1);
    f32x4 acc[8];
#pragma unroll
    for (int t = 0; t < 8; ++t) acc[t] = f32x4{0, 0, 0, 0};
    const int im = 15 - a;
    const u32 abase = lds0 + dl * CV_DSTR + (im & 3) * 4160 + (2016 + 8 * G + 4 * (im >> 2)) * 2 - q4 * 128 - 3648;
    const u32 bbase = lds0 + dl * CV_DSTR + 4 * 4160 + (256 - 16 + 16 * a + 8 * G) * 2 - q4 * 128 - 64;
    char* dump = smem + CV_RED + (dl * 40 + q4) * 1024 + swl;
#pragma unroll
    for (int cc = 0; cc < 2; ++cc) {
      u32x2 alo[8], ahi[8]; u32x4 bfr[8];
#pragma unroll
      for (int c1 = 0; c1 < 8; ++c1)
        asm volatile("ds_read_b64 %0, %3 offset:%5\n\tds_read_b64 %1, %3 offset:%6\n\tds_read_b128 %2, %4 offset:%7"
                     : "=&v"(alo[c1]), "=&v"(ahi[c1]), "=&v"(bfr[c1])
                     : "v"(abase), "v"(bbase), "i"(3648 - (8 * c1 + cc) * 64), "i"(3656 - (8 * c1 + cc) * 64), "i"(64 + (8 * c1 - cc) * 64) : "memory");
      SB();
      if (cc == 0) __builtin_amdgcn_s_setprio(1); else __builtin_amdgcn_s_setprio(0);
#pragma unroll
      for (int t = 0; t < 8; ++t) {
        if (t == 0) asm volatile("s_waitcnt lgkmcnt(15)" : "+v"(alo[0]), "+v"(ahi[0]), "+v"(bfr[0]), "+v"(alo[1]), "+v"(ahi[1]), "+v"(bfr[1]), "+v"(alo[2]), "+v"(ahi[2]), "+v"(bfr[2]));
        if (t == 3) asm volatile("s_waitcnt lgkmcnt(9)" : "+v"(alo[3]), "+v"(ahi[3]), "+v"(bfr[3]), "+v"(alo[4]), "+v"(ahi[4]), "+v"(bfr[4]));
        if (t == 5) asm volatile("s_waitcnt lgkmcnt(0)" : "+v"(alo[5]), "+v"(ahi[5]), "+v"(bfr[5]), "+v"(alo[6]), "+v"(ahi[6]), "+v"(bfr[6]), "+v"(alo[7]), "+v"(ahi[7]), "+v"(bfr[7]));
#pragma unroll
        for (int c1 = 0; c1 <= t; ++c1) {
          acc[t] = __builtin_amdgcn_mfma_f32_16x16x32_bf16(__builtin_bit_cast(bf16x8, u32x4{alo[c1].x, alo[c1].y, ahi[c1].x, ahi[c1].y}),
                                                           __builtin_bit_cast(bf16x8, bfr[t - c1]), acc[t], 0, 0, 0);
          const int n = t * (t + 1) / 2 + c1;
          if ((n % W_ST) == W_ST - 1 && W_N1 + (36 / W_ST) * cc + n / W_ST < W_N2) { SB(); WLOAD(W_N1 + (36 / W_ST) * cc + n / W_ST); SB(); }
          if (cc == 1 && t >= 5 && c1 >= 1) {
            const int u = (t == 5 ? c1 - 1 : (t == 6 ? (c1 == 1 ? 5 : -1) : (c1 == 1 ? 6 : -1)));
            if (u >= 0) { SB(); *(f32x4*)(dump + u * 5120) = acc[u]; SB(); }
          }
        }
        SB();
      }
    }
    *(f32x4*)(dump + 7 * 5120) = acc[7];
    STAMP(2);
    SB();
#pragma unroll
    for (int i = W_N2; i < 28; ++i) WLOAD(i);
    SB();
    if (tid < 448) {
      const float be = rbf(1.f + rbf(tb[2] + tb[3]));
      *(u16*)(smem + TAB_SB + tid * 2) = (u16)(F2U(rbf(tb[0] + tb[1])) >> 16);
      *(u16*)(smem + TAB_SB + 896 + tid * 2) = (u16)(F2U(be) >> 16);
    } else {
      *(float*)(smem + TAB_GB + (tid - 448) * 4) = rbf(tb[0] + tb[1]);
    }
    asm volatile("s_waitcnt lgkmcnt(0)\n\ts_barrier" ::: "memory");
    {
      const int ch = (lane >> 2) & 1;
      const float gac = ch ? ga[1] : ga[0];
      const float b2 = ch ? b2v[1] : b2v[0];
      const char* rp = smem + CV_RED + (ch * 40 + w * 5) * 1024 + (lane & 1) * 512 + ((2 * (lane >> 3) + ((lane >> 1) & 1)) ^ ((lane & 1) << 3)) * 16;
      u32x4 o, ob;
#pragma unroll
      for (int i = 0; i < 2; ++i) {
        const char* q = rp + i * 256;
        const f32x4 y = (*(const f32x4*)q + *(const f32x4*)(q + 1024)) + (*(const f32x4*)(q + 2048) + *(const f32x4*)(q + 3072));
        const f32x4 bs = *(const f32x4*)(q + 4096);
        o[2 * i] = pkbf(__builtin_fmaf(y[0], gac, bs[0]), __builtin_fmaf(y[1], gac, bs[1]));
        o[2 * i + 1] = pkbf(__builtin_fmaf(y[2], gac, bs[2]), __builtin_fmaf(y[3], gac, bs[3]));
      }
#pragma unroll
      for (int i = 0; i < 4; ++i) ob[i] = pkbf(bflo(o[i]) * b2, bfhi(o[i]) * b2);
      const int so = ((8 * w + (lane >> 3)) * 448 + 2 * b) * 64 + (lane & 7) * 16;
      __builtin_amdgcn_raw_buffer_store_b128(ob, rs, (int)WS_XB + so, 0, 16);
      __builtin_amdgcn_raw_buffer_store_b128(o, rs, (int)WS_X1T + so, 0, 16);
    }
    SB();
    STAMP(10);
  }
  u32x4 w2f[28];
  const int rot = (r2 & 1) * 7168;
  const char* w2src = ws + WS_W2F + ((size_t)(2 * dg) * 112 + 14 * w) * 1024;
  WAIT_VM0();
  asm volatile("s_waitcnt vmcnt(0)\n\ts_barrier" ::: "memory");
  STAMP(3);
  if (w == 0 && lane < 16) __hip_atomic_fetch_add(ctl + C1_CNT + (lane * 9 + xcd) * 32, 1u, RLX_AGENT);
  SB();
  auto uvcalc = [&]() __attribute__((always_inline)) {
    f32x16 accw;
#pragma unroll
    for (int r = 0; r < 16; ++r) accw[r] = 0.f;
    const char* tsb = smem + TAB_SB + (G & 1) * 896 + hi * 16;
#pragma unroll
    for (int p = 0; p < 28; ++p) accw = MFMA32(*(const u32x4*)(tsb + p * 32), wf[p], accw);
    const float us = accw[8], vs = accw[0];
    const float uh = rbf(us), vh = rbf(vs);
    wex = u32x4{hi ? 0u : pkbf(uh, us - uh), hi ? 0u : pkbf(vh, vs - vh), 0u, 0u};
    pin(wex);
  };
  if (w == 0) wait_cnt<9, 6>(ctl + C1_CNT + ((b & 15) * 9 + lane) * 32, 28u, pl0, pl1, pl2); else uvcalc();
  asm volatile("s_barrier" ::: "memory");
  STAMP(4);
  {
    const char* xsrc = ws + WS_XB + (size_t)(4 * rt) * 28672 + w * 1024;
#pragma unroll
    for (int i = 0; i < 14; ++i) glds16(voff, xsrc + i * 8192, lds0 + w * 1024 + i * 8192);
    glds16(voff, ws + WS_X1T + ((size_t)(w >> 2 ? tl : te) * 448 + 64 * dg) * 64 + (w & 3) * 1024, lds0 + G2_X1 + w * 1024);
  }
  if (w == 0) uvcalc();
  GAS u32* c2p = ctl + C2_CNT + rt * 1024 + (ft / 14) * 32;
  GAS u32* c2e = ctl + C2_CNT + te * 256 + w * 32;
  GAS u32* c2l = ctl + C2_CNT + tl * 256 + w * 32;
  u32 pv = 14;
#define ANNOUNCE(n, tt) asm volatile("s_waitcnt vmcnt(%3)\n\ts_mov_b64 exec, 1\n\tglobal_atomic_add %0, %1, %2 offset:%4\n\ts_mov_b64 exec, -1" :: "v"(0), "v"(1), "s"(c2p), "i"(n), "i"((tt) * 1024) : "memory")
  {
    u32 rb = lds0 + (8 * hi + qq) * 64 + (G & 1) * 32 + pp * 8;
    GAS unsigned long long* sgp = (GAS unsigned long long*)(ws + ST2_OFF) + 128 * rt + (lane & 31);

    u32x4 xf[4];
    f32x16 acc[2];
    float nal, s2, ex[8], pr[8];
    unsigned long long sg = 0;
#define G1_READ(q) do { const LAS char* p_ = (const LAS char*)(size_t)rb + (q) * 1024; u32x2 lo = trread3(p_), hh = trread3(p_ + 256); xf[(q) & 3] = u32x4{lo.x, lo.y, hh.x, hh.y}; } while (0)
#define SG_WAIT(g, ptr) do { _Pragma("unroll 1") for (unsigned spins = 0; spins < SPIN_CAP; ++spins) { if (__builtin_amdgcn_ballot_w64((u32)((g) >> 32) != 1u) == 0) break; sgreload(g, ptr); } } while (0)
#define SG_APPLY(g, A) do { const u32 st_ = (u32)(g); const u32 mm_ = __builtin_amdgcn_perm(st_, st_, 0x01000100u), ss_ = __builtin_amdgcn_perm(st_, st_, 0x03020302u); \
      const float sd_ = bfhi(st_); nal = -1.4426950408889634f * __builtin_amdgcn_rcpf(sd_); s2 = sd_ * sd_; \
      pinf(nal); pinf(s2); A = MFMA32(wex, (u32x4{mm_, ss_, mm_, ss_}), A); } while (0)
#define EPI_STAGE(A, st, v) do { if constexpr ((st) == 0) { ex[v] = A[v] * nal; pr[v] = A[v] * A[8 + (v)]; pinf(pr[v]); } \
      if constexpr ((st) == 1) ex[v] = __builtin_amdgcn_exp2f(ex[v]); if constexpr ((st) == 2) ex[v] = __builtin_fmaf(ex[v], s2, s2); \
      if constexpr ((st) == 3) ex[v] = __builtin_amdgcn_rcpf(ex[v]); if constexpr ((st) == 4) { pr[v] *= ex[v]; pinf(pr[v]); } else pinf(ex[v]); } while (0)
#define EPI_STORE(tt) __builtin_amdgcn_raw_buffer_store_b128(u32x4{pkbf(pr[0], pr[1]), pkbf(pr[2], pr[3]), pkbf(pr[4], pr[5]), pkbf(pr[6], pr[7])}, rs, hbase, (tt) * 112 * 1024, 16)
    asm volatile("s_waitcnt vmcnt(11)\n\ts_barrier" ::: "memory");
    POLL_KEEP();
    STAMP(12);
    G1_READ(0); G1_READ(1);
    static_for<0, 112>([&](auto nc) __attribute__((always_inline)) {
      constexpr int n = decltype(nc)::value, t = n / 28, q = n % 28;
      constexpr int wl = g1_w2(n);
      if constexpr (t >= 1 && q == g1_e0(t) - 2) {
        asm volatile("s_waitcnt vmcnt(%1)" : "+v"(sg) : "i"(g1_vm(g1_sg(t - 1), n)) : "memory");
        SG_WAIT(sg, sgp + 32 * (t - 1));
      }
      if constexpr (n == 24) asm volatile("s_waitcnt vmcnt(0)\n\ts_barrier" ::: "memory");
      if constexpr (t >= 2 && q == g1_an(t)) ANNOUNCE(g1_vm(28 * (t - 1) + g1_st(t - 1), n), t - 2);
      if constexpr (n == 108) POLL_ISSUE(pv, c2e, 1);
      if constexpr (n == g1_sg(t)) sgload(sg, sgp + 32 * t);
      if constexpr (q == 26 && (t & 1)) { rb += 57344; asm volatile("" : "+v"(rb)); }
      SB();
      if constexpr (n < 110) G1_READ((n + 2) % 56);
      SB();
      if constexpr (q == 0) { _Pragma("unroll") for (int r = 0; r < 16; ++r) acc[t & 1][r] = 0.f; }
      acc[t & 1] = MFMA32(wf[q], xf[q & 3], acc[t & 1]);
      if constexpr (t >= 1) {
        if constexpr (q == g1_e0(t) - 2) SG_APPLY(sg, acc[(t - 1) & 1]);
        static_for<0, 5>([&](auto sc) __attribute__((always_inline)) {
          constexpr int st = decltype(sc)::value, g = q - g1_e0(t) - st, v = g / 2;
          if constexpr (g >= 0 && (g & 1) == 0 && v < 8) EPI_STAGE(acc[(t - 1) & 1], st, v);
        });
        if constexpr (q == g1_st(t)) { SB(); EPI_STORE(t - 1); }
      }
      if constexpr (wl >= 0) { SB(); w2f[wl] = gload<(((wl >> 1) % 7) & 3) * 1024>(voff, w2src + (wl & 1) * 114688 + (wl >= 14 ? 7168 - rot : rot) + (((wl >> 1) % 7) >> 2) * 4096); }
    });
    SB();
    STAMP(5);
    asm volatile("s_waitcnt vmcnt(%1)" : "+v"(sg) : "i"(g1_vm(g1_sg(3), 112)) : "memory");
    SG_WAIT(sg, sgp + 96);
    SG_APPLY(sg, acc[1]);
    static_for<0, 5>([&](auto sc) __attribute__((always_inline)) {
      static_for<0, 8>([&](auto vc) __attribute__((always_inline)) { EPI_STAGE(acc[1], decltype(sc)::value, decltype(vc)::value); });
    });
    EPI_STORE(3);
    SB();
  }
  {
    const f32x4 gbv = *(const f32x4*)(smem + TAB_GB + (32 * dt + 8 * kq + 4 * hi) * 4);
    u32x4 hf[2][14];
    const char* hsrc[2] = {ws + WS_HF + ((size_t)te * 112 + 14 * w) * 1024, ws + WS_HF + ((size_t)tl * 112 + 14 * w) * 1024};
#define H_LOAD(j, lo, hi_) static_for<lo, hi_>([&](auto ic) __attribute__((always_inline)) { constexpr int i = decltype(ic)::value; hf[j][i] = gload<((i % 7) & 3) * 1024>(voff, hsrc[j] + (i >= 7 ? 7168 - rot : rot) + ((i % 7) >> 2) * 4096); })
    pl0 = pl1 = pl2 = 0;
#define ANNOUNCE23(n) asm volatile("s_waitcnt vmcnt(%3)\n\ts_mov_b64 exec, 3\n\tglobal_atomic_add %0, %1, %2 offset:2048\n\ts_mov_b64 exec, -1" :: "v"(lane << 10), "v"(1), "s"(c2p), "i"(n) : "memory")
    asm volatile("s_waitcnt vmcnt(1)\n\ts_barrier" : "+v"(pv) :: "memory");
    const bool slow = __builtin_amdgcn_readfirstlane(pv) < 14u;
    if (slow) {
      ANNOUNCE23(0);
      wait_cnt<1, 6>(c2e, 14u, pl0, pl1, pl2);
    }
    STAMP(6);
    H_LOAD(0, 0, G2_B1);
    if (!slow) ANNOUNCE23(G2_B1);
    H_LOAD(0, G2_B1, 14);
    static_for<0, 2>([&](auto jc) __attribute__((always_inline)) {
      constexpr int j = decltype(jc)::value;
      f32x16 acc[2];
#pragma unroll
      for (int c = 0; c < 2; ++c)
#pragma unroll
        for (int r = 0; r < 16; ++r) acc[c][r] = 0.f;
      static_for<0, 14>([&](auto sc) __attribute__((always_inline)) {
        constexpr int s = decltype(sc)::value;
        asm volatile("s_waitcnt vmcnt(%1)" : "+v"(hf[j][s]) : "i"(13 - s + (j == 0 && s > G2_PL ? 1 : 0)) : "memory");
        if constexpr (s == 0) POLL_KEEP();
        if constexpr (j == 0 && s == 0) { PIN14(w2f, 0); PIN14(w2f, 14); }
        if constexpr (j == 0 && s == G2_PL) POLL_ISSUE(pv, c2l, 1);
        acc[0] = MFMA32(w2f[2 * s], hf[j][s], acc[0]);
        acc[1] = MFMA32(w2f[2 * s + 1], hf[j][s], acc[1]);
        SB();
      });
      if constexpr (j == 0) {
        STAMP(14);
        asm volatile("s_waitcnt vmcnt(0)" : "+v"(pv) :: "memory");
        if (__builtin_amdgcn_readfirstlane(pv) < 14u) wait_cnt<1, 6>(c2l, 14u, pl0, pl1, pl2);
        H_LOAD(1, 0, 14);
      } else {
        STAMP(7);
      }
      char* red = smem + j * 65536;
#pragma unroll
      for (int P = 0; P < 8; ++P)
        *(f32x4*)(red + (P * 8 + w) * 1024 + lane * 16) = f32x4{acc[P & 1][4 * (P >> 1)], acc[P & 1][4 * (P >> 1) + 1], acc[P & 1][4 * (P >> 1) + 2], acc[P & 1][4 * (P >> 1) + 3]};
      if constexpr (j == 1) {
        asm volatile("s_waitcnt lgkmcnt(0)\n\ts_barrier" ::: "memory");
#pragma unroll
        for (int jj = 0; jj < 2; ++jj) {
          const char* rp = smem + jj * 65536 + w * 8192 + lane * 16;
          const f32x4 y = ((*(const f32x4*)rp + *(const f32x4*)(rp + 1024)) + (*(const f32x4*)(rp + 2048) + *(const f32x4*)(rp + 3072))) +
                          ((*(const f32x4*)(rp + 4096) + *(const f32x4*)(rp + 5120)) + (*(const f32x4*)(rp + 6144) + *(const f32x4*)(rp + 7168)));
          const u32x2 xr = trread(smem + G2_X1 + jj * 4096 + (32 * dt + 8 * kq + 4 * hi + qq) * 64 + (G & 1) * 32 + pp * 8);
          u32x2 o; o.x = pkbf(__builtin_fmaf(y[0], gbv[0], bflo(xr.x)), __builtin_fmaf(y[1], gbv[1], bfhi(xr.x)));
          o.y = pkbf(__builtin_fmaf(y[2], gbv[2], bflo(xr.y)), __builtin_fmaf(y[3], gbv[3], bfhi(xr.y)));
          ostore(o, out + (size_t)(32 * (jj ? tl : te) + (lane & 31)) * D_ + 64 * dg + 32 * dt + 8 * kq + 4 * hi);
        }
      }
      SB();
    });
  }
  STAMP(15);
  if (b == 100) { const unsigned long long lim = stv[15] + 4 * (stv[DIAG_B] - stv[DIAG_A]); while (RT() < lim) __builtin_amdgcn_s_sleep(1); }
}

extern "C" void kernel_launch(void* const* d_in, const int* in_sizes, int n_in, void* d_out, int out_size, void* d_ws, size_t ws_size,
                              hipStream_t stream) {
  const u16* x = (const u16*)d_in[0]; const u16* logA = (const u16*)d_in[1]; const u16* Aim = (const u16*)d_in[2];
  const u16* Cr = (const u16*)d_in[3]; const u16* Ci = (const u16*)d_in[4]; const u16* logdt = (const u16*)d_in[5]; const u16* Dsk = (const u16*)d_in[6];
  const u16* w1 = (const u16*)d_in[8]; const u16* w3 = (const u16*)d_in[9]; const u16* w2 = (const u16*)d_in[10];
  const u16* c = (const u16*)d_in[11]; const u16* wm = (const u16*)d_in[12];
  char* ws = (char*)d_ws;
  unsigned long long* stamps = (unsigned long long*)(ws + (64u << 20));
  kf_main<<<256, 512, 0, stream>>>(x, logA, Aim, Cr, Ci, logdt, Dsk, w1, w3, w2, c, wm, ws, (u16*)d_out, stamps);
}
```

```cpp
#include <hip/hip_runtime.h>

typedef unsigned short u16;
typedef unsigned u32;
typedef u32 u32x4 __attribute__((ext_vector_type(4)));
typedef u32 u32x2 __attribute__((ext_vector_type(2)));
typedef short s16x4 __attribute__((ext_vector_type(4)));
typedef __bf16 bf16x8 __attribute__((ext_vector_type(8)));
typedef __bf16 bf16x2 __attribute__((ext_vector_type(2)));
typedef _Float16 f16x8 __attribute__((ext_vector_type(8)));
typedef _Float16 f16x2 __attribute__((ext_vector_type(2)));
typedef float f32x2 __attribute__((ext_vector_type(2)));
typedef float f32x4 __attribute__((ext_vector_type(4)));
typedef float f32x16 __attribute__((ext_vector_type(16)));
#define DI __device__ __forceinline__
#define U2F(x) __builtin_bit_cast(float, (u32)(x))
#define F2U(x) __builtin_bit_cast(u32, (float)(x))
#define I2F(x) __builtin_bit_cast(float, (int)(x))
#define F2I(x) __builtin_bit_cast(int, (float)(x))
#if defined(__HIP_DEVICE_COMPILE__)
#define NOPK __attribute__((target("no-packed-fp32-ops")))
#else
#define NOPK
#endif
#define LDSP(T, p) ((__attribute__((address_space(3))) T*)(p))
#define GAS __attribute__((address_space(1)))
#define RLX_AGENT __ATOMIC_RELAXED, __HIP_MEMORY_SCOPE_AGENT
#define SB() __builtin_amdgcn_sched_barrier(0)
#define LDIN(p) __builtin_nontemporal_load(p)
template <int V> struct IC { static constexpr int value = V; };
template <int I, int N, class F> __device__ __forceinline__ void static_for(F&& f) { if constexpr (I < N) { f(IC<I>{}); static_for<I + 1, N>(f); } }

constexpr int L_ = 2048, D_ = 448, N_ = 256, F_ = 1792;

constexpr int EP_E = 0;
constexpr int EP_R = 64;
constexpr int C0_CNT = 1024;
constexpr int C1_CNT = 9216;
constexpr int C2_CNT = 14336;
constexpr int ST2_OFF = 122880;
constexpr int ZERO_LO = 36864, ZERO_HI = 139264;
constexpr size_t WS_MODA = 139264;
constexpr size_t WS_MODB = WS_MODA + 448 * 32;
constexpr size_t WS_MODC = WS_MODB + 448 * 16;
constexpr size_t WS_ST1 = 167936;
constexpr size_t WS_XT = WS_ST1 + 16384;
constexpr size_t WS_X1T = WS_XT + 64 * 448 * 64;
constexpr size_t WS_XB = WS_X1T + 64 * 448 * 64;
constexpr size_t WS_W13 = WS_XB + 64 * 448 * 64;
constexpr size_t WS_W2F = WS_W13 + 112 * 28 * 1024;
constexpr size_t WS_HF = WS_W2F + 14 * 112 * 1024;
constexpr size_t WS_END = WS_HF + 64 * 112 * 1024;

DI float bflo(u32 v) { return U2F(v << 16); }
DI float bfhi(u32 v) { return U2F(v & 0xffff0000u); }
DI float bf2f(u16 v) { return U2F(((u32)v) << 16); }
DI u32 pkbf(float lo, float hi) { f32x2 p = {lo, hi}; return __builtin_bit_cast(u32, __builtin_convertvector(p, bf16x2)); }
DI u32 pkh(f32x2 p) { return __builtin_bit_cast(u32, __builtin_convertvector(p, f16x2)); }
DI float rbf(float f) { return bflo(pkbf(f, f)); }
DI float dot2(u32 a, u32 b, float c) { return __builtin_amdgcn_fdot2_f32_bf16(__builtin_bit_cast(bf16x2, a), __builtin_bit_cast(bf16x2, b), c, false); }
DI float fexp(float x) { return __builtin_amdgcn_exp2f(x * 1.4426950408889634f); }
DI float silu_bf(float c) { return rbf(c * __builtin_amdgcn_rcpf(1.f + fexp(-c))); }
template <int CTRL> DI float dppf(float x) { return I2F(__builtin_amdgcn_update_dpp(0, F2I(x), CTRL, 0xf, 0xf, false)); }
DI float sum16(float x) { x += dppf<0x128>(x); x += dppf<0x124>(x); x += dppf<0x122>(x); x += dppf<0x121>(x); return x; }
DI float max16(float x) { x = __builtin_fmaxf(x, dppf<0x128>(x)); x = __builtin_fmaxf(x, dppf<0x124>(x)); x = __builtin_fmaxf(x, dppf<0x122>(x)); x = __builtin_fmaxf(x, dppf<0x121>(x)); return x; }
DI float add32(float x) { auto r = __builtin_amdgcn_permlane32_swap(F2U(x), F2U(x), false, false); return U2F(r[0]) + U2F(r[1]); }
DI float add16(float x) { auto r = __builtin_amdgcn_permlane16_swap(F2U(x), F2U(x), false, false); return U2F(r[0]) + U2F(r[1]); }
DI float max64(float x) {
  x = max16(x);
  auto r = __builtin_amdgcn_permlane16_swap(F2U(x), F2U(x), false, false);
  x = __builtin_fmaxf(U2F(r[0]), U2F(r[1]));
  auto q = __builtin_amdgcn_permlane32_swap(F2U(x), F2U(x), false, false);
  return __builtin_fmaxf(U2F(q[0]), U2F(q[1]));
}
DI u32x2 trread(const char* p) { return __builtin_bit_cast(u32x2, __builtin_amdgcn_ds_read_tr16_b64_v4i16(LDSP(s16x4, p))); }
DI u32x2 trread3(const __attribute__((address_space(3))) char* p) { return __builtin_bit_cast(u32x2, __builtin_amdgcn_ds_read_tr16_b64_v4i16((__attribute__((address_space(3))) s16x4*)p)); }
DI void pinf(float& v) { asm volatile("" : "+v"(v)); }
DI void pin(u32x4& v) { asm volatile("" : "+v"(v)); }
DI void ostore(u32x2 v, u16* p) { *(u32x2*)p = v; }
DI f32x2 cmulp(f32x2 a, f32x2 z, f32x2 zs) { return __builtin_elementwise_fma(a.yx, zs, a * z.xx); }

#define STAMP_DECL
#define STAMP(i)
#define CSTAMP(i)
#define STAMP_OUT(buf, slot)
#define STAMP_OUT2(buf, slot)

#define MFMA32(a, b, c) __builtin_amdgcn_mfma_f32_32x32x16_bf16(__builtin_bit_cast(bf16x8, a), __builtin_bit_cast(bf16x8, b), c, 0, 0, 0)
#define ST16(v, off) __builtin_amdgcn_raw_buffer_store_b128(v, rs, (int)(off), 0, 16)
template <int IMM> DI u32x4 gload(u32 voff, const char* sbase) {
  u32x4 r; asm volatile("global_load_dwordx4 %0, %1, %2 offset:%3 sc1" : "=v"(r) : "v"(voff), "s"(sbase), "i"(IMM)); return r;
}
DI void glds16(u32 voff, const char* sbase, u32 ldsaddr) {
  asm volatile("s_mov_b32 m0, %2\n\ts_nop 0\n\tglobal_load_lds_dwordx4 %0, %1 sc1" :: "v"(voff), "s"(sbase), "s"(ldsaddr) : "memory");
}
constexpr unsigned SPIN_CAP = 1u << 16;
template <int OFF> DI u32 ldu16(const u16* p) { u32 r; asm volatile("global_load_ushort %0, %1, off offset:%2" : "=v"(r) : "v"(p), "i"(OFF) : "memory"); return r; }
template <int OFF> DI u32x4 ldnt(const u16* p) { u32x4 r; asm volatile("global_load_dwordx4 %0, %1, off offset:%2 nt" : "=v"(r) : "v"(p), "i"(OFF) : "memory"); return r; }
DI void sgload(unsigned long long& g, GAS unsigned long long* p) { asm volatile("global_load_dwordx2 %0, %1, off sc1" : "+v"(g) : "v"(p) : "memory"); }
DI void sgwait(unsigned long long& g) { asm volatile("s_waitcnt vmcnt(1)" : "+v"(g) :: "memory"); }
DI void sgreload(unsigned long long& g, GAS unsigned long long* p) { asm volatile("global_load_dwordx2 %0, %1, off sc1\n\ts_waitcnt vmcnt(0)" : "+v"(g) : "v"(p) : "memory"); }
#define POLL_ISSUE(v, p, m) asm volatile("s_mov_b64 exec, %3\n\tglobal_atomic_or %0, %1, %2, off sc0\n\ts_mov_b64 exec, -1" : "+v"(v) : "v"(p), "v"(0), "i"(m) : "memory")
template <int NL, int SLP> DI void wait_cnt(GAS u32* p, u32 want, u32& v0, u32& v1, u32& v2) {
  v0 = want; v1 = want; v2 = want;
  POLL_ISSUE(v0, p, (1 << NL) - 1); __builtin_amdgcn_s_sleep(SLP);
  POLL_ISSUE(v1, p, (1 << NL) - 1); __builtin_amdgcn_s_sleep(SLP);
#pragma unroll 1
  for (unsigned spins = 0; spins < SPIN_CAP; ++spins) {
    POLL_ISSUE(v2, p, (1 << NL) - 1);
    asm volatile("s_waitcnt vmcnt(2)" : "+v"(v0) :: "memory");
    if (__builtin_amdgcn_ballot_w64(v0 < want) == 0) break;
    POLL_ISSUE(v0, p, (1 << NL) - 1);
    asm volatile("s_waitcnt vmcnt(2)" : "+v"(v1) :: "memory");
    if (__builtin_amdgcn_ballot_w64(v1 < want) == 0) break;
    POLL_ISSUE(v1, p, (1 << NL) - 1);
    asm volatile("s_waitcnt vmcnt(2)" : "+v"(v2) :: "memory");
    if (__builtin_amdgcn_ballot_w64(v2 < want) == 0) break;
  }
}
#define POLL_KEEP() asm volatile("" :: "v"(pl0), "v"(pl1), "v"(pl2) : "memory")
#define WAIT_VM0() __builtin_amdgcn_s_waitcnt(0x0F70)
#define BAR() __builtin_amdgcn_s_barrier()

constexpr int CV_DSTR = 4 * 4160 + 4608;
constexpr int CV_RED = 2 * CV_DSTR;
constexpr int G1_ST = 4 * 28672;
constexpr int G2_RED = 4 * 28672;
constexpr int G2_X1 = 18 * 8192;
constexpr int TAB_GB = G2_X1 + 8192;
constexpr int TAB_SB = TAB_GB + 256;
constexpr int ARR_CNT = TAB_SB + 1792;
constexpr int KF_LDS = ARR_CNT + 16;
constexpr int KG_REG = 32 * 272;
constexpr int KG_IMG = 2 * CV_DSTR;
constexpr int TASK_LDS = KG_IMG + 8 * KG_REG;
constexpr int KG_TAB = TASK_LDS + 30464;
#define HSLOT stv[14]
#define WSLOT stv[0]
#define W2_N0 13
#define W13_SLP 1
#define G2_B1 6
#define G2_PL 8
#define EPI0 7
#define W_N0 2
#define W_NM 18
#define W_ST 4
#define W_NB 2
#define W_N1 (W_N0 + W_NB)
#define W_N2 (W_N1 + W_NM)
#define SC_SIG 6
#define SC_POLL 21
#define LAS __attribute__((address_space(3)))
#define PIN14(a, o) asm volatile("" : "+v"(a[o]), "+v"(a[o + 1]), "+v"(a[o + 2]), "+v"(a[o + 3]), "+v"(a[o + 4]), "+v"(a[o + 5]), "+v"(a[o + 6]), "+v"(a[o + 7]), \
    "+v"(a[o + 8]), "+v"(a[o + 9]), "+v"(a[o + 10]), "+v"(a[o + 11]), "+v"(a[o + 12]), "+v"(a[o + 13]) :: "memory")

constexpr int g1_e0(int t) { return t == 1 ? EPI0 : 3; }
constexpr int g1_st(int t) { return g1_e0(t) + 19; }
constexpr int g1_an(int t) { return t == 2 ? 18 : 14; }
constexpr int g1_w2(int n) {
  if (n >= 25 && n < 25 + 14 * 4 && (n - 25) % 4 == 0) return (n - 25) / 4;
  if (n >= 84 && n < 84 + 23) { const int q = n - 84; if (q % 3 != 2) return 14 + q - q / 3 < 28 ? 14 + q - q / 3 : -1; }
  return -1;
}
constexpr int g1_sg(int t) { return t == 0 ? 24 : 28 * t + 10; }
constexpr int g1_vm(int a, int b) {
  int c = 0;
  for (int n = a; n < b; ++n) {
    const int t = n / 28, q = n % 28;
    if (n > a) { if (t >= 2 && q == g1_an(t)) ++c; if (n == 108) ++c; if (n == g1_sg(t)) ++c; }
    if (t >= 1 && q == g1_st(t) && n != a) ++c;
    if (g1_w2(n) >= 0) ++c;
  }
  return c;
}
__global__ __launch_bounds__(512) void kf_main(const u16* __restrict__ x, const u16* __restrict__ logA, const u16* __restrict__ Aim,
                                               const u16* __restrict__ Cr, const u16* __restrict__ Ci, const u16* __restrict__ logdt,
                                               const u16* __restrict__ Dsk, const u16* __restrict__ w1, const u16* __restrict__ w3,
                                               const u16* __restrict__ w2, const u16* __restrict__ cvec, const u16* __restrict__ wmod,
                                               char* __restrict__ ws, u16* __restrict__ out, unsigned long long* stamps) {
  __shared__ __attribute__((aligned(16))) char smem[KF_LDS];
  const int tid = __builtin_amdgcn_workitem_id_x(), lane = tid & 63;
  const int w = __builtin_amdgcn_readfirstlane(tid >> 6);
  const int b = __builtin_amdgcn_workgroup_id_x(), xcd = b & 7, idx = b >> 3;
  const int hi = lane >> 5;
  const int G = lane >> 4, qq = (lane & 15) >> 2, pp = lane & 3;
  const u32 voff = lane * 16;
  GAS u32* ctl = (GAS u32*)ws;
  auto rs = __builtin_amdgcn_make_buffer_rsrc(ws, 0, (int)WS_END, 0x00020000);
  const u32 lds0 = (u32)(size_t)LDSP(char, smem);
  STAMP_DECL;
  STAMP(0);
  u32 pl0 = 0, pl1 = 0, pl2 = 0;
  const bool mainwg = b < 224;
  const int dl = w >> 2, q4 = w & 3, t256 = tid & 255;
  auto arrive0 = [&]() __attribute__((always_inline)) {
    u32 old;
    asm volatile("s_waitcnt vmcnt(0)\n\ts_mov_b64 exec, 1\n\tds_add_rtn_u32 %0, %1, %2\n\ts_mov_b64 exec, -1\n\ts_waitcnt lgkmcnt(0)" : "=&v"(old) : "v"(lds0 + ARR_CNT), "v"(1) : "memory");
    if (__builtin_amdgcn_readfirstlane(old) == (mainwg ? 4u : (b < 249 ? 8u : 0u)) && lane < 16) __hip_atomic_fetch_add(ctl + C0_CNT + (lane * 8 + xcd) * 32, 1u, RLX_AGENT);
  };
  GAS u32* c0p = ctl + C0_CNT + ((b & 15) * 8 + lane) * 32;
  int kg_wb, kg_wst;
  u32 kga[12];
  u32 dskr;
  unsigned long long epoch;
  {
    u32 lar = 0, air = 0, crr = 0, cir = 0, ldt = 0;
    if (mainwg) {
      const int d = 2 * b + dl;
      asm volatile("global_load_ushort %0, %5, %6 nt\n\tglobal_load_ushort %1, %5, %7 nt\n\tglobal_load_ushort %4, %10, %11\n\tglobal_load_ushort %2, %5, %8 nt\n\tglobal_load_ushort %3, %5, %9 nt"
                   : "+v"(lar), "+v"(air), "+v"(crr), "+v"(cir), "+v"(ldt) : "v"((d * N_ + t256) * 2), "s"(logA), "s"(Aim), "s"(Cr), "s"(Ci), "v"(d * 2), "s"(logdt) : "memory");
    }
    unsigned long long ep = 0;
    u32 pe = 32u;
    const int task = b >= 249 ? 2 * b - 364 + dl : (dl || b >= 246 ? -1 : (b < 134 ? b : b + 14));
    char* stg = smem + (dl ? 0 : TASK_LDS);
    u32x4 v[7]; u32 cv[7];
    auto taskload = [&]() __attribute__((always_inline)) {
      const u16* src; u32 step;
      if (task < 64) { src = x + (size_t)(task * 32 + (t256 >> 3)) * D_ + (t256 & 7) * 8; step = 128; }
      else { const int m = task - 64; src = wmod + (size_t)(224 * (m & 1) + (t256 >> 3)) * 2688 + (m >> 1) * 64 + (t256 & 7) * 8; step = 64 * 2688; }
#pragma unroll
      for (int i = 0; i < 7; ++i) asm volatile("global_load_dwordx4 %0, %1, off nt" : "=v"(v[i]) : "v"((const char*)src + (size_t)i * step) : "memory");
      const u16* cp = cvec + 224 * (task & 1) + (t256 >> 3);
      static_for<0, 7>([&](auto ic) __attribute__((always_inline)) { constexpr int i = decltype(ic)::value; cv[i] = ldu16<64 * i>(cp); });
    };
    if (task >= 0 && task < 134) {
      taskload();
    } else if (task >= 148) {
      const int m = task - 148;
      const u16* src = ((t256 & 128) ? w3 : w1) + (size_t)(8 * (m >> 1) + ((t256 >> 4) & 7)) * F_ + (m & 1) * 896 + (t256 & 15) * 8;
      static_for<0, 7>([&](auto ic) __attribute__((always_inline)) { constexpr int i = decltype(ic)::value; v[i] = ldnt<256 * i>(src); });
    } else if (task < 0 && !mainwg) {
      if (dl) ST16((u32x4{0, 0, 0, 0}), ZERO_LO + (b - 224) * 4096 + t256 * 16);
      if (b == 248 && w == 0) {
        const unsigned long long e0 = __hip_atomic_load((GAS unsigned long long*)(ctl + EP_E), RLX_AGENT);
#pragma unroll
        for (int i = 0; i < 2; ++i) __hip_atomic_store(ctl + C0_CNT + i * 2048 + lane * 32, 0u, RLX_AGENT);
        WAIT_VM0();
        asm volatile("s_waitcnt vmcnt(0)" ::: "memory");
        if (lane < 16) __hip_atomic_store((GAS unsigned long long*)(ctl + EP_R + lane * 32), e0 + 1, RLX_AGENT);
      }
    }
    SB();
    dskr = *(const u32*)(Dsk + 2 * (mainwg ? b : 0));
    asm volatile("" :: "s"(dskr));
    if (tid < 4) *(u32*)(smem + ARR_CNT + tid * 4) = 0;
    asm volatile("s_waitcnt lgkmcnt(0)\n\ts_barrier" ::: "memory");
    SB();
    auto meet = [&](u32 want) __attribute__((always_inline)) {
      asm volatile("s_waitcnt lgkmcnt(0)\n\ts_mov_b64 exec, 1\n\tds_add_u32 %0, %1 offset:4\n\ts_mov_b64 exec, -1" :: "v"(lds0 + ARR_CNT + dl * 4), "v"(1) : "memory");
#pragma unroll 1
      for (unsigned spins = 0; spins < 16 * SPIN_CAP; ++spins) {
        if (*(volatile LAS u32*)(size_t)(lds0 + ARR_CNT + 4 + dl * 4) >= want) break;
        __builtin_amdgcn_s_sleep(1);
      }
      asm volatile("" ::: "memory");
    };
#pragma unroll
    for (int h = 0; h < 2; ++h) {
      const int lb = 2040 - 8 * t256 - 8 * h;
      const int kr = lb < 256 ? 0 : 24 - __builtin_clz(lb);
      const float mB = U2F((u32)(150 + kr) << 23) + (float)(lb < 0 ? 704 : lb);
      const u32 kpa = lds0 + (u32)(KG_TAB + dl * 1536) + 256u * kr - ((u32)(150 + kr) << 24);
#pragma unroll
      for (int e = 0; e < (h ? 4 : 8); ++e) { kga[8 * h + e] = (F2U(mB + (float)(7 - e)) << 1) + kpa; asm volatile("" : "+v"(kga[8 * h + e])); }
    }
    if (t256 == 0) *(u32x4*)(smem + KG_TAB + dl * 1536 + 1408) = u32x4{0, 0, 0, 0};
    {
      char* cp = smem + dl * CV_DSTR;
      if (mainwg) {
        if (t256 < 32) *(u32x4*)(cp + 4 * 4160 + t256 * 16) = u32x4{0, 0, 0, 0};
        else if (t256 < 40) *(u32x4*)(cp + ((t256 - 32) >> 1) * 4160 + 4096 + (t256 & 1) * 16) = u32x4{0, 0, 0, 0};
      }
    }
    const int s = lane & 31;
    {
      const int kw = s < 8 ? 0 : (s < 20 ? (s >> 2) - 1 : 3);
      int wb = s < 12 ? (s < 8 ? s : 248 + s) : (s < 16 ? 384 + ((s & 2) << 5) + (s & 1) : 32 * s);
      kg_wst = 16 >> kw;
      kg_wb = (wb > 672 ? 672 : wb) * 2 + KG_TAB + dl * 1536 + (8 * q4 + 4 * hi) * kg_wst;
    }
    char* reg = smem + KG_IMG + w * KG_REG;
    f16x8 bfr[8];
    f32x2 z8, z16; float scl = 0.f, fs = 0.f;
    if (mainwg) {
      if (task < 0) asm volatile("s_waitcnt vmcnt(0)" ::: "memory"); else if (task < 148) asm volatile("s_waitcnt vmcnt(14)" ::: "memory"); else asm volatile("s_waitcnt vmcnt(7)" ::: "memory");
      asm volatile("" : "+v"(lar), "+v"(air), "+v"(crr), "+v"(cir), "+v"(ldt) :: "memory");
    }
    if (mainwg) {
      const float dt = rbf(fexp(bflo(ldt)));
      const float ar = -rbf(fexp(bflo(lar))) * dt * 1.4426950408889634f;
      const float ai = bflo(air) * dt;
      const float I2PH = 0.15915494f, I2PL = 6.4206382e-9f;
      const float th = ai * I2PH;
      const float tl = __builtin_fmaf(ai, I2PL, __builtin_fmaf(ai, I2PH, -th));
      auto zpow = [&](float sg) __attribute__((always_inline)) {
        float e = __builtin_amdgcn_exp2f(ar * sg);
        float rev = __builtin_fmaf(tl, sg, __builtin_amdgcn_fractf(th * sg));
        return f32x2{e * __builtin_amdgcn_cosf(rev), e * __builtin_amdgcn_sinf(rev)};
      };
      const f32x2 z1 = zpow(1.f), z256 = zpow(256.f);
      z16 = zpow(16.f);
      const f32x2 s1 = {-z1.y, z1.y}, s256 = {-z256.y, z256.y};
      u32 eb = ((crr & 0x7f80u) > (cir & 0x7f80u) ? (crr & 0x7f80u) : (cir & 0x7f80u));
      asm("s_nop 1\n\tv_max_u32_dpp %0, %0, %0 row_ror:8 row_mask:0xf bank_mask:0xf\n\ts_nop 1\n\tv_max_u32_dpp %0, %0, %0 row_ror:4 row_mask:0xf bank_mask:0xf\n\ts_nop 1\n\t"
          "v_max_u32_dpp %0, %0, %0 row_ror:2 row_mask:0xf bank_mask:0xf\n\ts_nop 1\n\tv_max_u32_dpp %0, %0, %0 row_ror:1 row_mask:0xf bank_mask:0xf\n\ts_nop 1" : "+v"(eb));
      {
        const u32 e0 = __builtin_amdgcn_readlane(eb, 0), e1 = __builtin_amdgcn_readlane(eb, 16), e2 = __builtin_amdgcn_readlane(eb, 32), e3 = __builtin_amdgcn_readlane(eb, 48);
        const u32 m0 = e0 > e1 ? e0 : e1, m1 = e2 > e3 ? e2 : e3;
        eb = (m0 > m1 ? m0 : m1) >> 7;
      }
      eb = eb < 1u ? 1u : (eb > 252u ? 252u : eb);
      scl = U2F((253u - eb) << 23);
      fs = 2.f * dt * U2F((eb + 1u) << 23);
      f32x2 lo[9], hp[9];
      lo[1] = z1; hp[1] = z256;
#pragma unroll
      for (int i = 2; i <= 8; ++i) { lo[i] = cmulp(lo[i - 1], z1, s1); hp[i] = cmulp(hp[i - 1], z256, s256); }
      const f32x2 a2 = cmulp(lo[2], z256, s256), a4 = cmulp(lo[4], z256, s256), a6 = cmulp(lo[6], z256, s256);
      const f32x2 b4 = cmulp(a4, z256, s256), c4 = cmulp(b4, z256, s256);
      const u32 col[21] = {0x00003c00u, pkh(lo[1]), pkh(lo[2]), pkh(lo[3]), pkh(lo[4]), pkh(lo[5]), pkh(lo[6]), pkh(lo[7]),
                           pkh(hp[1]), pkh(a2), pkh(a4), pkh(a6), pkh(hp[2]), pkh(b4), pkh(hp[3]), pkh(c4),
                           pkh(hp[4]), pkh(hp[5]), pkh(hp[6]), pkh(hp[7]), pkh(hp[8])};
#pragma unroll
      for (int i = 0; i < 21; ++i) *(u32*)(reg + i * 272 + lane * 4) = col[i];
      z8 = lo[8];
#pragma unroll
      for (int i = 0; i < 8; ++i) bfr[i] = *(const f16x8*)(reg + s * 272 + i * 32 + hi * 16);
      asm volatile("s_waitcnt lgkmcnt(0)" : "+v"(bfr[0]), "+v"(bfr[1]), "+v"(bfr[2]), "+v"(bfr[3]), "+v"(bfr[4]), "+v"(bfr[5]), "+v"(bfr[6]), "+v"(bfr[7]) :: "memory");
    }
    STAMP(8);
    unsigned long long tagr = 0;
    GAS u32* tagp = ctl + EP_R + (b & 15) * 32;
#define EP_ISSUE() asm volatile("s_mov_b64 exec, 1\n\tglobal_load_dwordx2 %1, %4, off sc1\n\tglobal_atomic_or_x2 %0, %2, %3, off sc0\n\ts_mov_b64 exec, -1" \
                     : "+&v"(tagr), "+&v"(ep) : "v"(tagp), "v"(0ull), "v"(ctl + EP_E) : "memory")
    auto epcheck = [&]() __attribute__((always_inline)) {
#pragma unroll 1
      for (unsigned spins = 0; spins < SPIN_CAP; ++spins) {
        asm volatile("s_waitcnt vmcnt(0)" : "+v"(tagr), "+v"(ep) :: "memory");
        if ((__builtin_amdgcn_ballot_w64(tagr != ep + 1) & 1) == 0) break;
        EP_ISSUE();
      }
      arrive0();
    };
    if (w == 4) { EP_ISSUE(); if (b >= 249) epcheck(); }
    if (b >= 249) taskload();
#define PIN_V() asm volatile("s_waitcnt vmcnt(0)" : "+v"(v[0]), "+v"(v[1]), "+v"(v[2]), "+v"(v[3]), "+v"(v[4]), "+v"(v[5]), "+v"(v[6]) :: "memory")
    if (task >= 0 && task < 148) {
      PIN_V();
      if (task < 64) {
            const int row = t256 >> 3, p = t256 & 7;
            float sm = 0.f, sq = 0.f;
#pragma unroll
            for (int i = 0; i < 7; ++i) {
              pin(v[i]);
              *(u32x4*)(stg + row * 928 + (p + 8 * i) * 16) = v[i];
#pragma unroll
              for (int j = 0; j < 4; ++j) { sm = dot2(v[i][j], 0x3f803f80u, sm); sq = dot2(v[i][j], v[i][j], sq); }
            }
            sm += dppf<0xB1>(sm); sq += dppf<0xB1>(sq); sm += dppf<0x4E>(sm); sq += dppf<0x4E>(sq); sm += dppf<0x141>(sm); sq += dppf<0x141>(sq);
            const float m = sm * (1.f / D_);
            const float var = __builtin_fmaxf(sq * (1.f / D_) - m * m, 0.f);
            const float sd = rbf(__builtin_amdgcn_sqrtf(rbf(rbf(var) + 1e-5f)));
            const float rsd = __builtin_amdgcn_rcpf(sd);
            if (p < 2) __builtin_amdgcn_raw_buffer_store_b32(F2U(p ? -rbf(m) * rsd : rsd), rs, (int)WS_ST1 + p * 8192 + (task * 32 + row) * 4, 0, 16);
        meet(4u);
        const char* rb = stg + (8 * G + qq) * 928 + q4 * 224 + pp * 8;
#pragma unroll
        for (int i = 0; i < 7; ++i) {
          const u32x2 lo = trread(rb + i * 32), hh = trread(rb + i * 32 + 4 * 928);
          ST16((u32x4{lo.x, lo.y, hh.x, hh.y}), (int)WS_XT + task * 28672 + q4 * 7168 + (lane & 15) * 64 + G * 16 + i * 1024);
        }
      } else {
            float acc[8] = {0, 0, 0, 0, 0, 0, 0, 0};
#pragma unroll
            for (int i = 0; i < 7; ++i) {
              asm volatile("" : "+v"(cv[i]));
              const u32 sh = F2U(silu_bf(bflo(cv[i]))), sl = sh >> 16;
#pragma unroll
              for (int j = 0; j < 4; ++j) { acc[2 * j] = dot2(v[i][j], sl, acc[2 * j]); acc[2 * j + 1] = dot2(v[i][j], sh, acc[2 * j + 1]); }
            }
#pragma unroll
            for (int j = 0; j < 8; ++j) { acc[j] += dppf<0x128>(acc[j]); acc[j] = add32(acc[j]); }
            if ((lane & 40) == 0) {
              float* red = (float*)stg + (q4 * 2 + (lane >> 4)) * 64 + (lane & 7) * 8;
              *(f32x4*)red = f32x4{acc[0], acc[1], acc[2], acc[3]};
              *(f32x4*)(red + 4) = f32x4{acc[4], acc[5], acc[6], acc[7]};
            }
        meet(4u);
            if (q4 == 0) {
              const int m = task - 64, cg = m >> 1;
              float t = 0.f;
#pragma unroll
              for (int j = 0; j < 8; ++j) t += ((const float*)stg)[j * 64 + lane];
              const int sec = cg / 7, dd = (cg % 7) * 64 + lane;
              const int o1 = sec == 3 ? (int)WS_MODB + dd * 16 : (sec == 5 ? (int)WS_MODC + dd * 8 : (int)WS_MODA + dd * 32 + (sec == 4 ? 3 : sec) * 8);
              __builtin_amdgcn_raw_buffer_store_b32(F2U(t), rs, o1 + (m & 1) * 4, 0, 16);
              if (sec == 4) __builtin_amdgcn_raw_buffer_store_b32(F2U(t), rs, (int)WS_MODB + dd * 16 + 8 + (m & 1) * 4, 0, 16);
            }
      }
      STAMP(13);
    }
    if (task >= 148) {
      PIN_V();
#pragma unroll
      for (int i = 0; i < 7; ++i) *(u32x4*)(stg + (t256 >> 4) * 1824 + ((t256 & 15) + 16 * i) * 16) = v[i];
      meet(4u);
      const int m = task - 148;
      const char* rb = stg + (8 * (G & 1) + qq) * 1824 + (14 * q4 + hi) * 32 + (8 * (pp & 1) + 4 * (pp >> 1)) * 2;
      const int dst = (int)WS_W13 + ((56 * (m & 1) + 14 * q4 + hi) * 28 + (m >> 2)) * 1024 + ((m >> 1) & 1) * 512 + (lane & 31) * 16;
#pragma unroll
      for (int i = 0; i < 7; ++i) {
        const u32x2 lo = trread(rb + i * 64), hh = trread(rb + i * 64 + 4 * 1824);
        ST16((u32x4{lo.x, lo.y, hh.x, hh.y}), dst + i * 57344);
      }
    }
    if (mainwg) {
        pinf(scl);
        f32x2 ra = {bflo(crr) * scl, -bflo(cir) * scl};
        f32x2 rb = cmulp(ra, z8, f32x2{z8.y, -z8.y});
        const f32x2 s16c = {z16.y, -z16.y};
#pragma unroll
        for (int r = 0; r < 32; r += 2) {
          *(u32*)(reg + r * 272 + lane * 4) = pkh(ra); *(u32*)(reg + (r + 1) * 272 + lane * 4) = pkh(rb);
          if (r < 30) { ra = cmulp(ra, z16, s16c); rb = cmulp(rb, z16, s16c); }
        }
        if (task >= 0) { WAIT_VM0(); arrive0(); STAMP(11); }
        if (w == 4) epcheck();
        f32x16 acc = {};
#pragma unroll
        for (int i = 0; i < 8; ++i) {
          const f16x8 af = *(const f16x8*)(reg + s * 272 + i * 32 + hi * 16);
          acc = __builtin_amdgcn_mfma_f32_32x32x16_f16(af, bfr[i], acc, 0, 0, 0);
        }
#pragma unroll
        for (int qd = 0; qd < 4; ++qd)
          *(f32x4*)(reg + qd * 1024 + lane * 16) = f32x4{acc[4 * qd] * fs, acc[4 * qd + 1] * fs, acc[4 * qd + 2] * fs, acc[4 * qd + 3] * fs};
    }
    if (mainwg) {
      if (w == 4) POLL_ISSUE(pe, c0p, 255);
      meet(dl ? 4u : 8u);
    {
      const char* rp = smem + KG_IMG + dl * 4 * KG_REG + q4 * 1024 + lane * 16;
      const f32x4 t = (*(const f32x4*)rp + *(const f32x4*)(rp + KG_REG)) + (*(const f32x4*)(rp + 2 * KG_REG) + *(const f32x4*)(rp + 3 * KG_REG));
      const u32 ta = pkbf(t[0], t[1]), tc = pkbf(t[2], t[3]);
      char* q = smem + kg_wb;
      *(u16*)q = (u16)ta; *(u16*)(q + kg_wst) = (u16)(ta >> 16); *(u16*)(q + 2 * kg_wst) = (u16)tc; *(u16*)(q + 3 * kg_wst) = (u16)(tc >> 16);
    }
    }
    if (!mainwg && b < 249) { WAIT_VM0(); arrive0(); }
    if (w == 4) {
      asm volatile("s_waitcnt vmcnt(0)" : "+v"(pe) :: "memory");
      if (mainwg) { if (__builtin_amdgcn_ballot_w64(pe < 32u) != 0) wait_cnt<8, 6>(c0p, 32u, pl0, pl1, pl2); }
      else if (b < 249) epcheck();
    }
    STAMP(14);
    epoch = ep + 1;
    asm volatile("s_waitcnt lgkmcnt(0)\n\ts_barrier" ::: "memory");
  }
  if (!mainwg) {
    const int fr = b - 224;
    if (fr < 28) {
      u32x4 v[7];
#pragma unroll
      for (int i = 0; i < 7; ++i) v[i] = LDIN((const u32x4*)(w2 + (size_t)(64 * fr + (tid >> 3)) * D_ + ((tid & 7) + 8 * i) * 8));
#pragma unroll
      for (int i = 0; i < 7; ++i) *(u32x4*)(smem + (tid >> 3) * 912 + ((tid & 7) + 8 * i) * 16) = v[i];
      asm volatile("s_waitcnt lgkmcnt(0)\n\ts_barrier" ::: "memory");
      const char* rbase = smem + (16 * (w & 3) + 8 * hi + qq) * 912 + (w >> 2) * 448 + (16 * (G & 1) + 4 * pp) * 2;
      const int dst = (int)WS_W2F + ((7 * (w >> 2)) * 112 + 4 * fr + (w & 3)) * 1024 + lane * 16;
#pragma unroll
      for (int s = 0; s < 7; ++s) {
        u32x2 lo = trread(rbase + s * 64), hh = trread(rbase + s * 64 + 4 * 912);
        __builtin_amdgcn_raw_buffer_store_b128(u32x4{lo.x, lo.y, hh.x, hh.y}, rs, dst + s * 112 * 1024, 0, 16);
      }
    }
    asm volatile("s_waitcnt vmcnt(0)\n\ts_barrier" ::: "memory");
    if (w == 4) {
      wait_cnt<8, 6>(c0p, 32u, pl0, pl1, pl2);
      asm volatile("s_waitcnt vmcnt(0)" ::: "memory");
      POLL_KEEP();
      if (b == 248 && lane == 0) __hip_atomic_store((GAS unsigned long long*)(ctl + EP_E), epoch, RLX_AGENT);
      if (lane < 16) __hip_atomic_fetch_add(ctl + C1_CNT + (lane * 9 + 8) * 32, 1u, RLX_AGENT);
      wait_cnt<8, 6>(ctl + C1_CNT + ((b & 15) * 9 + lane) * 32, 28u, pl0, pl1, pl2);
      asm volatile("s_waitcnt vmcnt(0)" ::: "memory");
      POLL_KEEP();
    }
    asm volatile("s_barrier" ::: "memory");
    {
      const char* xsrc = ws + WS_X1T + (size_t)(2 * fr + (w >> 2)) * 28672 + (w & 3) * 1024;
#pragma unroll
      for (int i = 0; i < 7; ++i) glds16(voff, xsrc + i * 4096, lds0 + w * 7168 + i * 1024);
      const char* sp = smem + w * 7168 + (8 * hi + qq) * 64 + (G & 1) * 32 + pp * 8;
      float s0 = 0.f, q0 = 0.f, s1 = 0.f, q1 = 0.f;
      static_for<0, 7>([&](auto ic) __attribute__((always_inline)) {
        constexpr int i = decltype(ic)::value;
        asm volatile("s_waitcnt vmcnt(%0)" :: "i"(6 - i) : "memory");
        const u32x2 lo = trread(sp + i * 1024), hh = trread(sp + i * 1024 + 256);
        s0 = dot2(lo.x, 0x3f803f80u, s0); q0 = dot2(lo.x, lo.x, q0); s1 = dot2(lo.y, 0x3f803f80u, s1); q1 = dot2(lo.y, lo.y, q1);
        s0 = dot2(hh.x, 0x3f803f80u, s0); q0 = dot2(hh.x, hh.x, q0); s1 = dot2(hh.y, 0x3f803f80u, s1); q1 = dot2(hh.y, hh.y, q1);
      });
      auto r = __builtin_amdgcn_permlane32_swap(F2U(s0 + s1), F2U(q0 + q1), false, false);
      *(float*)(smem + 65536 + w * 256 + (lane & 31) * 8 + hi * 4) = U2F(r[0]) + U2F(r[1]);
      asm volatile("s_waitcnt lgkmcnt(0)\n\ts_barrier" ::: "memory");
      if (w == 0) {
        const char* pt = smem + 65536 + hi * 1024 + (lane & 31) * 8;
        const f32x2 ts = (*(const f32x2*)pt + *(const f32x2*)(pt + 256)) + (*(const f32x2*)(pt + 512) + *(const f32x2*)(pt + 768));
        const float m = ts[0] * (1.f / D_);
        const float var = __builtin_fmaxf(ts[1] * (1.f / D_) - m * m, 0.f);
        const float sd = __builtin_amdgcn_sqrtf(rbf(rbf(var) + 1e-5f));
        __hip_atomic_store((GAS unsigned long long*)(ws + ST2_OFF) + 64 * fr + lane, (1ull << 32) | pkbf(-m, sd), RLX_AGENT);
      }
    }
    return;
  }
  const int rt = 4 * (xcd >> 1) + idx / 7, ct = 7 * (xcd & 1) + idx % 7;
  const int ft = 8 * ct + w;
  const int r2 = 4 * xcd + idx / 7, dg = idx % 7, kq = w >> 1, dt = w & 1;
  const int te = 4 * (r2 >> 1) + (r2 & 1), tl = 4 * (r2 >> 1) + 3 - (r2 & 1);
  const int wv = (int)WS_W13 + ft * 28672 + lane * 16;
#define WLOAD(p) wf[p] = __builtin_amdgcn_raw_buffer_load_b128(rs, wv + ((p) & 3) * 1024, ((p) & ~3) * 1024, 16)

  u32x4 wf[28];
  u32x4 wex;
  const int hbase = (int)WS_HF + (4 * rt * 112 + ft) * 1024 + lane * 16;

  {
    const int a = lane & 15;
    const int l0 = 256 * w + 16 * a + 4 * G;
    const int xoff = ((l0 >> 5) * 448 + 2 * b) * 64 + (l0 & 31) * 2;
    STAMP(9);
    u32x2 xx[2]; u32x4 st[2];
    float msv = U2F(__builtin_amdgcn_raw_buffer_load_b32(rs, (int)WS_MODA + (lane & 15) * 4, b * 64, 16));
#pragma unroll
    for (int i = 0; i < 2; ++i) {
      xx[i] = __builtin_amdgcn_raw_buffer_load_b64(rs, (int)WS_XT + xoff + i * 64, 0, 16);
      st[i] = __builtin_amdgcn_raw_buffer_load_b128(rs, (int)WS_ST1 + l0 * 4, i * 8192, 16);
    }
    SB();
    {
      u32 kw[6];
#pragma unroll
      for (int e = 0; e < 6; ++e) kw[e] = (u32)*(const LAS u16*)(size_t)kga[2 * e] | ((u32)*(const LAS u16*)(size_t)kga[2 * e + 1] << 16);
      char* cp = smem + dl * CV_DSTR;
      u32 al[5];
#pragma unroll
      for (int i = 0; i < 5; ++i) al[i] = __builtin_amdgcn_alignbit(kw[i + 1], kw[i], 16);
      char* q = cp + t256 * 16;
      *(u32x4*)(q) = u32x4{kw[0], kw[1], kw[2], kw[3]};
      *(u32x4*)(q + 4160) = u32x4{al[0], al[1], al[2], al[3]};
      *(u32x4*)(q + 8320) = u32x4{kw[1], kw[2], kw[3], kw[4]};
      *(u32x4*)(q + 12480) = u32x4{al[1], al[2], al[3], al[4]};
    }
    SB();
    const f32x4 tb = __builtin_bit_cast(f32x4, __builtin_amdgcn_raw_buffer_load_b128(rs, (int)WS_MODB + (tid < 448 ? tid : 447) * 16, 0, 16));
    SB();
#pragma unroll
    for (int i = 0; i < W_N1; ++i) WLOAD(i);
    SB();
    msv = rbf(msv + dppf<0xB1>(msv));
    if (lane & 2) msv = rbf(1.f + msv);
#define MSV(i) U2F(__builtin_amdgcn_readlane(F2U(msv), i))
    float ga[2], b2v[2];
    const int swl = (lane ^ ((lane >> 5) << 3)) * 16;
#pragma unroll
    for (int i = 0; i < 2; ++i) {
      const float sa = MSV(8 * i), beta = MSV(8 * i + 2);
      ga[i] = MSV(8 * i + 4); b2v[i] = MSV(8 * i + 6);
      const float gd = ga[i] * (i ? bfhi(dskr) : bflo(dskr));
      const float xv[4] = {bflo(xx[i].x), bfhi(xx[i].x), bflo(xx[i].y), bfhi(xx[i].y)};
      float hv[4];
#pragma unroll
      for (int e = 0; e < 4; ++e)
        hv[e] = __builtin_fmaf(__builtin_fmaf(xv[e], U2F(st[0][e]), U2F(st[1][e])), beta, sa);
      *(u32x2*)(smem + i * CV_DSTR + 4 * 4160 + 512 + l0 * 2) = u32x2{pkbf(hv[0], hv[1]), pkbf(hv[2], hv[3])};
      f32x4 base;
#pragma unroll
      for (int e = 0; e < 4; ++e) base[e] = __builtin_fmaf(hv[e], gd, xv[e]);
      *(f32x4*)(smem + CV_RED + (i * 40 + w * 5 + 4) * 1024 + swl) = base;
    }
    SB();
    POLL_KEEP();
    asm volatile("s_waitcnt lgkmcnt(0)\n\ts_barrier" ::: "memory");
    STAMP(1);
    f32x4 acc[8];
#pragma unroll
    for (int t = 0; t < 8; ++t) acc[t] = f32x4{0, 0, 0, 0};
    const int im = 15 - a;
    const u32 abase = lds0 + dl * CV_DSTR + (im & 3) * 4160 + (2016 + 8 * G + 4 * (im >> 2)) * 2 - q4 * 128 - 3648;
    const u32 bbase = lds0 + dl * CV_DSTR + 4 * 4160 + (256 - 16 + 16 * a + 8 * G) * 2 - q4 * 128 - 64;
    char* dump = smem + CV_RED + (dl * 40 + q4) * 1024 + swl;
#pragma unroll
    for (int cc = 0; cc < 2; ++cc) {
      u32x2 alo[8], ahi[8]; u32x4 bfr[8];
#pragma unroll
      for (int c1 = 0; c1 < 8; ++c1)
        asm volatile("ds_read_b64 %0, %3 offset:%5\n\tds_read_b64 %1, %3 offset:%6\n\tds_read_b128 %2, %4 offset:%7"
                     : "=&v"(alo[c1]), "=&v"(ahi[c1]), "=&v"(bfr[c1])
                     : "v"(abase), "v"(bbase), "i"(3648 - (8 * c1 + cc) * 64), "i"(3656 - (8 * c1 + cc) * 64), "i"(64 + (8 * c1 - cc) * 64) : "memory");
      SB();
      if (cc == 0) __builtin_amdgcn_s_setprio(1); else __builtin_amdgcn_s_setprio(0);
#pragma unroll
      for (int t = 0; t < 8; ++t) {
        if (t == 0) asm volatile("s_waitcnt lgkmcnt(15)" : "+v"(alo[0]), "+v"(ahi[0]), "+v"(bfr[0]), "+v"(alo[1]), "+v"(ahi[1]), "+v"(bfr[1]), "+v"(alo[2]), "+v"(ahi[2]), "+v"(bfr[2]));
        if (t == 3) asm volatile("s_waitcnt lgkmcnt(9)" : "+v"(alo[3]), "+v"(ahi[3]), "+v"(bfr[3]), "+v"(alo[4]), "+v"(ahi[4]), "+v"(bfr[4]));
        if (t == 5) asm volatile("s_waitcnt lgkmcnt(0)" : "+v"(alo[5]), "+v"(ahi[5]), "+v"(bfr[5]), "+v"(alo[6]), "+v"(ahi[6]), "+v"(bfr[6]), "+v"(alo[7]), "+v"(ahi[7]), "+v"(bfr[7]));
#pragma unroll
        for (int c1 = 0; c1 <= t; ++c1) {
          acc[t] = __builtin_amdgcn_mfma_f32_16x16x32_bf16(__builtin_bit_cast(bf16x8, u32x4{alo[c1].x, alo[c1].y, ahi[c1].x, ahi[c1].y}),
                                                           __builtin_bit_cast(bf16x8, bfr[t - c1]), acc[t], 0, 0, 0);
          const int n = t * (t + 1) / 2 + c1;
          if ((n % W_ST) == W_ST - 1 && W_N1 + (36 / W_ST) * cc + n / W_ST < W_N2) { SB(); WLOAD(W_N1 + (36 / W_ST) * cc + n / W_ST); SB(); }
          if (cc == 1 && t >= 5 && c1 >= 1) {
            const int u = (t == 5 ? c1 - 1 : (t == 6 ? (c1 == 1 ? 5 : -1) : (c1 == 1 ? 6 : -1)));
            if (u >= 0) { SB(); *(f32x4*)(dump + u * 5120) = acc[u]; SB(); }
          }
        }
        SB();
      }
    }
    *(f32x4*)(dump + 7 * 5120) = acc[7];
    STAMP(2);
    SB();
#pragma unroll
    for (int i = W_N2; i < 28; ++i) WLOAD(i);
    SB();
    if (tid < 448) {
      const float be = rbf(1.f + rbf(tb[2] + tb[3]));
      *(u16*)(smem + TAB_SB + tid * 2) = (u16)(F2U(rbf(tb[0] + tb[1])) >> 16);
      *(u16*)(smem + TAB_SB + 896 + tid * 2) = (u16)(F2U(be) >> 16);
    }
    asm volatile("s_waitcnt lgkmcnt(0)\n\ts_barrier" ::: "memory");
    {
      const int ch = (lane >> 2) & 1;
      const float gac = ch ? ga[1] : ga[0];
      const float b2 = ch ? b2v[1] : b2v[0];
      const char* rp = smem + CV_RED + (ch * 40 + w * 5) * 1024 + (lane & 1) * 512 + ((2 * (lane >> 3) + ((lane >> 1) & 1)) ^ ((lane & 1) << 3)) * 16;
      u32x4 o, ob;
#pragma unroll
      for (int i = 0; i < 2; ++i) {
        const char* q = rp + i * 256;
        const f32x4 y = (*(const f32x4*)q + *(const f32x4*)(q + 1024)) + (*(const f32x4*)(q + 2048) + *(const f32x4*)(q + 3072));
        const f32x4 bs = *(const f32x4*)(q + 4096);
        o[2 * i] = pkbf(__builtin_fmaf(y[0], gac, bs[0]), __builtin_fmaf(y[1], gac, bs[1]));
        o[2 * i + 1] = pkbf(__builtin_fmaf(y[2], gac, bs[2]), __builtin_fmaf(y[3], gac, bs[3]));
      }
#pragma unroll
      for (int i = 0; i < 4; ++i) ob[i] = pkbf(bflo(o[i]) * b2, bfhi(o[i]) * b2);
      const int so = ((8 * w + (lane >> 3)) * 448 + 2 * b) * 64 + (lane & 7) * 16;
      __builtin_amdgcn_raw_buffer_store_b128(ob, rs, (int)WS_XB + so, 0, 16);
      __builtin_amdgcn_raw_buffer_store_b128(o, rs, (int)WS_X1T + so, 0, 16);
    }
    SB();
    STAMP(10);
  }
  u32x4 w2f[28];
  const int rot = (r2 & 1) * 7168;
  const char* w2src = ws + WS_W2F + ((size_t)(2 * dg) * 112 + 14 * w) * 1024;
  WAIT_VM0();
  asm volatile("s_waitcnt vmcnt(0)\n\ts_barrier" ::: "memory");
  STAMP(3);
  if (w == 0 && lane < 16) __hip_atomic_fetch_add(ctl + C1_CNT + (lane * 9 + xcd) * 32, 1u, RLX_AGENT);
  SB();
  auto uvcalc = [&]() __attribute__((always_inline)) {
    f32x16 accw;
#pragma unroll
    for (int r = 0; r < 16; ++r) accw[r] = 0.f;
    const char* tsb = smem + TAB_SB + (G & 1) * 896 + hi * 16;
#pragma unroll
    for (int p = 0; p < 28; ++p) accw = MFMA32(*(const u32x4*)(tsb + p * 32), wf[p], accw);
    const float us = accw[8], vs = accw[0];
    const float uh = rbf(us), vh = rbf(vs);
    wex = u32x4{hi ? 0u : pkbf(uh, us - uh), hi ? 0u : pkbf(vh, vs - vh), 0u, 0u};
    pin(wex);
  };
  if (w == 0) wait_cnt<9, 6>(ctl + C1_CNT + ((b & 15) * 9 + lane) * 32, lane == 8 ? 32u : 28u, pl0, pl1, pl2); else uvcalc();
  asm volatile("s_barrier" ::: "memory");
  STAMP(4);
  {
    const char* xsrc = ws + WS_XB + (size_t)(4 * rt) * 28672 + w * 1024;
#pragma unroll
    for (int i = 0; i < 14; ++i) glds16(voff, xsrc + i * 8192, lds0 + w * 1024 + i * 8192);
    glds16(voff, ws + WS_X1T + ((size_t)(w >> 2 ? tl : te) * 448 + 64 * dg) * 64 + (w & 3) * 1024, lds0 + G2_X1 + w * 1024);
  }
  if (w == 0) uvcalc();
  GAS u32* c2p = ctl + C2_CNT + rt * 1024 + (ft / 14) * 32;
  GAS u32* c2e = ctl + C2_CNT + te * 256 + w * 32;
  GAS u32* c2l = ctl + C2_CNT + tl * 256 + w * 32;
  u32 pv = 14;
#define ANNOUNCE(n, tt) asm volatile("s_waitcnt vmcnt(%3)\n\ts_mov_b64 exec, 1\n\tglobal_atomic_add %0, %1, %2 offset:%4\n\ts_mov_b64 exec, -1" :: "v"(0), "v"(1), "s"(c2p), "i"(n), "i"((tt) * 1024) : "memory")
  {
    u32 rb = lds0 + (8 * hi + qq) * 64 + (G & 1) * 32 + pp * 8;
    GAS unsigned long long* sgp = (GAS unsigned long long*)(ws + ST2_OFF) + 128 * rt + (lane & 31);

    u32x4 xf[4];
    f32x16 acc[2];
    float nal, s2, ex[8], pr[8];
    unsigned long long sg = 0;
#define G1_READ(q) do { const LAS char* p_ = (const LAS char*)(size_t)rb + (q) * 1024; u32x2 lo = trread3(p_), hh = trread3(p_ + 256); xf[(q) & 3] = u32x4{lo.x, lo.y, hh.x, hh.y}; } while (0)
#define SG_WAIT(g, ptr) do { _Pragma("unroll 1") for (unsigned spins = 0; spins < SPIN_CAP; ++spins) { if (__builtin_amdgcn_ballot_w64((u32)((g) >> 32) != 1u) == 0) break; sgreload(g, ptr); } } while (0)
#define SG_APPLY(g, A) do { const u32 st_ = (u32)(g); const u32 mm_ = __builtin_amdgcn_perm(st_, st_, 0x01000100u), ss_ = __builtin_amdgcn_perm(st_, st_, 0x03020302u); \
      const float sd_ = bfhi(st_); nal = -1.4426950408889634f * __builtin_amdgcn_rcpf(sd_); s2 = sd_ * sd_; \
      pinf(nal); pinf(s2); A = MFMA32(wex, (u32x4{mm_, ss_, mm_, ss_}), A); } while (0)
#define EPI_STAGE(A, st, v) do { if constexpr ((st) == 0) { ex[v] = A[v] * nal; pr[v] = A[v] * A[8 + (v)]; pinf(pr[v]); } \
      if constexpr ((st) == 1) ex[v] = __builtin_amdgcn_exp2f(ex[v]); if constexpr ((st) == 2) ex[v] = __builtin_fmaf(ex[v], s2, s2); \
      if constexpr ((st) == 3) ex[v] = __builtin_amdgcn_rcpf(ex[v]); if constexpr ((st) == 4) { pr[v] *= ex[v]; pinf(pr[v]); } else pinf(ex[v]); } while (0)
#define EPI_STORE(tt) __builtin_amdgcn_raw_buffer_store_b128(u32x4{pkbf(pr[0], pr[1]), pkbf(pr[2], pr[3]), pkbf(pr[4], pr[5]), pkbf(pr[6], pr[7])}, rs, hbase, (tt) * 112 * 1024, 16)
    asm volatile("s_waitcnt vmcnt(11)\n\ts_barrier" ::: "memory");
    POLL_KEEP();
    STAMP(12);
    G1_READ(0); G1_READ(1);
    static_for<0, 112>([&](auto nc) __attribute__((always_inline)) {
      constexpr int n = decltype(nc)::value, t = n / 28, q = n % 28;
      constexpr int wl = g1_w2(n);
      if constexpr (t >= 1 && q == g1_e0(t) - 2) {
        asm volatile("s_waitcnt vmcnt(%1)" : "+v"(sg) : "i"(g1_vm(g1_sg(t - 1), n)) : "memory");
        SG_WAIT(sg, sgp + 32 * (t - 1));
      }
      if constexpr (n == 24) asm volatile("s_waitcnt vmcnt(0)\n\ts_barrier" ::: "memory");
      if constexpr (t >= 2 && q == g1_an(t)) ANNOUNCE(g1_vm(28 * (t - 1) + g1_st(t - 1), n), t - 2);
      if constexpr (n == 108) POLL_ISSUE(pv, c2e, 1);
      if constexpr (n == g1_sg(t)) sgload(sg, sgp + 32 * t);
      if constexpr (q == 26 && (t & 1)) { rb += 57344; asm volatile("" : "+v"(rb)); }
      SB();
      if constexpr (n < 110) G1_READ((n + 2) % 56);
      SB();
      if constexpr (q == 0) { _Pragma("unroll") for (int r = 0; r < 16; ++r) acc[t & 1][r] = 0.f; }
      acc[t & 1] = MFMA32(wf[q], xf[q & 3], acc[t & 1]);
      if constexpr (t >= 1) {
        if constexpr (q == g1_e0(t) - 2) SG_APPLY(sg, acc[(t - 1) & 1]);
        static_for<0, 5>([&](auto sc) __attribute__((always_inline)) {
          constexpr int st = decltype(sc)::value, g = q - g1_e0(t) - st, v = g / 2;
          if constexpr (g >= 0 && (g & 1) == 0 && v < 8) EPI_STAGE(acc[(t - 1) & 1], st, v);
        });
        if constexpr (q == g1_st(t)) { SB(); EPI_STORE(t - 1); }
      }
      if constexpr (wl >= 0) { SB(); w2f[wl] = gload<(((wl >> 1) % 7) & 3) * 1024>(voff, w2src + (wl & 1) * 114688 + (wl >= 14 ? 7168 - rot : rot) + (((wl >> 1) % 7) >> 2) * 4096); }
    });
    SB();
    STAMP(5);
    asm volatile("s_waitcnt vmcnt(%1)" : "+v"(sg) : "i"(g1_vm(g1_sg(3), 112)) : "memory");
    SG_WAIT(sg, sgp + 96);
    SG_APPLY(sg, acc[1]);
    static_for<0, 5>([&](auto sc) __attribute__((always_inline)) {
      static_for<0, 8>([&](auto vc) __attribute__((always_inline)) { EPI_STAGE(acc[1], decltype(sc)::value, decltype(vc)::value); });
    });
    EPI_STORE(3);
    SB();
  }
  {
    u32x4 gq[2];
    u32x4 hf[2][14];
    const char* hsrc[2] = {ws + WS_HF + ((size_t)te * 112 + 14 * w) * 1024, ws + WS_HF + ((size_t)tl * 112 + 14 * w) * 1024};
#define H_LOAD(j, lo, hi_) static_for<lo, hi_>([&](auto ic) __attribute__((always_inline)) { constexpr int i = decltype(ic)::value; hf[j][i] = gload<((i % 7) & 3) * 1024>(voff, hsrc[j] + (i >= 7 ? 7168 - rot : rot) + ((i % 7) >> 2) * 4096); })
    pl0 = pl1 = pl2 = 0;
#define ANNOUNCE23(n) asm volatile("s_waitcnt vmcnt(%3)\n\ts_mov_b64 exec, 3\n\tglobal_atomic_add %0, %1, %2 offset:2048\n\ts_mov_b64 exec, -1" :: "v"(lane << 10), "v"(1), "s"(c2p), "i"(n) : "memory")
    asm volatile("s_waitcnt vmcnt(1)\n\ts_barrier" : "+v"(pv) :: "memory");
    const bool slow = __builtin_amdgcn_readfirstlane(pv) < 14u;
    if (slow) {
      ANNOUNCE23(0);
      wait_cnt<1, 6>(c2e, 14u, pl0, pl1, pl2);
    }
    STAMP(6);
    { const char* gp = ws + WS_MODC + (64 * dg + 32 * dt + 8 * kq) * 8; gq[0] = gload<0>(hi * 32, gp); gq[1] = gload<16>(hi * 32, gp); }
    H_LOAD(0, 0, G2_B1);
    if (!slow) ANNOUNCE23(G2_B1 + 2);
    H_LOAD(0, G2_B1, 14);
    static_for<0, 2>([&](auto jc) __attribute__((always_inline)) {
      constexpr int j = decltype(jc)::value;
      f32x16 acc[2];
#pragma unroll
      for (int c = 0; c < 2; ++c)
#pragma unroll
        for (int r = 0; r < 16; ++r) acc[c][r] = 0.f;
      static_for<0, 14>([&](auto sc) __attribute__((always_inline)) {
        constexpr int s = decltype(sc)::value;
        asm volatile("s_waitcnt vmcnt(%1)" : "+v"(hf[j][s]) : "i"(13 - s + (j == 0 && s > G2_PL ? 1 : 0)) : "memory");
        if constexpr (s == 0) POLL_KEEP();
        if constexpr (j == 0 && s == 0) { PIN14(w2f, 0); PIN14(w2f, 14); pin(gq[0]); pin(gq[1]); }
        if constexpr (j == 0 && s == G2_PL) POLL_ISSUE(pv, c2l, 1);
        acc[0] = MFMA32(w2f[2 * s], hf[j][s], acc[0]);
        acc[1] = MFMA32(w2f[2 * s + 1], hf[j][s], acc[1]);
        SB();
      });
      if constexpr (j == 0) {
        asm volatile("s_waitcnt vmcnt(0)" : "+v"(pv) :: "memory");
        if (__builtin_amdgcn_readfirstlane(pv) < 14u) wait_cnt<1, 6>(c2l, 14u, pl0, pl1, pl2);
        H_LOAD(1, 0, 14);
      } else {
        STAMP(7);
      }
      char* red = smem + j * 65536;
#pragma unroll
      for (int P = 0; P < 8; ++P)
        *(f32x4*)(red + (P * 8 + w) * 1024 + lane * 16) = f32x4{acc[P & 1][4 * (P >> 1)], acc[P & 1][4 * (P >> 1) + 1], acc[P & 1][4 * (P >> 1) + 2], acc[P & 1][4 * (P >> 1) + 3]};
      if constexpr (j == 1) {
        const float gbv[4] = {rbf(U2F(gq[0].x) + U2F(gq[0].y)), rbf(U2F(gq[0].z) + U2F(gq[0].w)), rbf(U2F(gq[1].x) + U2F(gq[1].y)), rbf(U2F(gq[1].z) + U2F(gq[1].w))};
        asm volatile("s_waitcnt lgkmcnt(0)\n\ts_barrier" ::: "memory");
#pragma unroll
        for (int jj = 0; jj < 2; ++jj) {
          const char* rp = smem + jj * 65536 + w * 8192 + lane * 16;
          const f32x4 y = ((*(const f32x4*)rp + *(const f32x4*)(rp + 1024)) + (*(const f32x4*)(rp + 2048) + *(const f32x4*)(rp + 3072))) +
                          ((*(const f32x4*)(rp + 4096) + *(const f32x4*)(rp + 5120)) + (*(const f32x4*)(rp + 6144) + *(const f32x4*)(rp + 7168)));
          const u32x2 xr = trread(smem + G2_X1 + jj * 4096 + (32 * dt + 8 * kq + 4 * hi + qq) * 64 + (G & 1) * 32 + pp * 8);
          u32x2 o; o.x = pkbf(__builtin_fmaf(y[0], gbv[0], bflo(xr.x)), __builtin_fmaf(y[1], gbv[1], bfhi(xr.x)));
          o.y = pkbf(__builtin_fmaf(y[2], gbv[2], bflo(xr.y)), __builtin_fmaf(y[3], gbv[3], bfhi(xr.y)));
          ostore(o, out + (size_t)(32 * (jj ? tl : te) + (lane & 31)) * D_ + 64 * dg + 32 * dt + 8 * kq + 4 * hi);
        }
      }
      SB();
    });
  }
  STAMP(15);
  STAMP_OUT(stamps, 1024 + b);
  STAMP_OUT2(stamps, 2048 + b);
}

extern "C" void kernel_launch(void* const* d_in, const int* in_sizes, int n_in, void* d_out, int out_size, void* d_ws, size_t ws_size,
                              hipStream_t stream) {
  const u16* x = (const u16*)d_in[0]; const u16* logA = (const u16*)d_in[1]; const u16* Aim = (const u16*)d_in[2];
  const u16* Cr = (const u16*)d_in[3]; const u16* Ci = (const u16*)d_in[4]; const u16* logdt = (const u16*)d_in[5]; const u16* Dsk = (const u16*)d_in[6];
  const u16* w1 = (const u16*)d_in[8]; const u16* w3 = (const u16*)d_in[9]; const u16* w2 = (const u16*)d_in[10];
  const u16* c = (const u16*)d_in[11]; const u16* wm = (const u16*)d_in[12];
  char* ws = (char*)d_ws;
  unsigned long long* stamps = (unsigned long long*)(ws + (64u << 20));
  kf_main<<<256, 512, 0, stream>>>(x, logA, Aim, Cr, Ci, logdt, Dsk, w1, w3, w2, c, wm, ws, (u16*)d_out, stamps);
}
```

```cpp
#define DIAG_A 0
#define DIAG_B 0
#define DIAG_WG 100
#include <hip/hip_runtime.h>

typedef unsigned short u16;
typedef unsigned u32;
typedef u32 u32x4 __attribute__((ext_vector_type(4)));
typedef u32 u32x2 __attribute__((ext_vector_type(2)));
typedef short s16x4 __attribute__((ext_vector_type(4)));
typedef __bf16 bf16x8 __attribute__((ext_vector_type(8)));
typedef __bf16 bf16x2 __attribute__((ext_vector_type(2)));
typedef _Float16 f16x8 __attribute__((ext_vector_type(8)));
typedef _Float16 f16x2 __attribute__((ext_vector_type(2)));
typedef float f32x2 __attribute__((ext_vector_type(2)));
typedef float f32x4 __attribute__((ext_vector_type(4)));
typedef float f32x16 __attribute__((ext_vector_type(16)));
#define DI __device__ __forceinline__
#define U2F(x) __builtin_bit_cast(float, (u32)(x))
#define F2U(x) __builtin_bit_cast(u32, (float)(x))
#define I2F(x) __builtin_bit_cast(float, (int)(x))
#define F2I(x) __builtin_bit_cast(int, (float)(x))
#if defined(__HIP_DEVICE_COMPILE__)
#define NOPK __attribute__((target("no-packed-fp32-ops")))
#else
#define NOPK
#endif
#define LDSP(T, p) ((__attribute__((address_space(3))) T*)(p))
#define GAS __attribute__((address_space(1)))
#define RLX_AGENT __ATOMIC_RELAXED, __HIP_MEMORY_SCOPE_AGENT
#define SB() __builtin_amdgcn_sched_barrier(0)
#define LDIN(p) __builtin_nontemporal_load(p)
template <int V> struct IC { static constexpr int value = V; };
template <int I, int N, class F> __device__ __forceinline__ void static_for(F&& f) { if constexpr (I < N) { f(IC<I>{}); static_for<I + 1, N>(f); } }

constexpr int L_ = 2048, D_ = 448, N_ = 256, F_ = 1792;

constexpr int EP_E = 0;
constexpr int EP_R = 64;
constexpr int C0_CNT = 1024;
constexpr int C1_CNT = 9216;
constexpr int C2_CNT = 14336;
constexpr int ST2_OFF = 122880;
constexpr int ZERO_LO = 36864, ZERO_HI = 139264;
constexpr size_t WS_MODA = 139264;
constexpr size_t WS_MODB = WS_MODA + 448 * 32;
constexpr size_t WS_MODC = WS_MODB + 448 * 16;
constexpr size_t WS_ST1 = 167936;
constexpr size_t WS_XT = WS_ST1 + 16384;
constexpr size_t WS_X1T = WS_XT + 64 * 448 * 64;
constexpr size_t WS_XB = WS_X1T + 64 * 448 * 64;
constexpr size_t WS_W13 = WS_XB + 64 * 448 * 64;
constexpr size_t WS_W2F = WS_W13 + 112 * 28 * 1024;
constexpr size_t WS_HF = WS_W2F + 14 * 112 * 1024;
constexpr size_t WS_END = WS_HF + 64 * 112 * 1024;

DI float bflo(u32 v) { return U2F(v << 16); }
DI float bfhi(u32 v) { return U2F(v & 0xffff0000u); }
DI float bf2f(u16 v) { return U2F(((u32)v) << 16); }
DI u32 pkbf(float lo, float hi) { f32x2 p = {lo, hi}; return __builtin_bit_cast(u32, __builtin_convertvector(p, bf16x2)); }
DI u32 pkh(f32x2 p) { return __builtin_bit_cast(u32, __builtin_convertvector(p, f16x2)); }
DI float rbf(float f) { return bflo(pkbf(f, f)); }
DI float dot2(u32 a, u32 b, float c) { return __builtin_amdgcn_fdot2_f32_bf16(__builtin_bit_cast(bf16x2, a), __builtin_bit_cast(bf16x2, b), c, false); }
DI float fexp(float x) { return __builtin_amdgcn_exp2f(x * 1.4426950408889634f); }
DI float silu_bf(float c) { return rbf(c * __builtin_amdgcn_rcpf(1.f + fexp(-c))); }
template <int CTRL> DI float dppf(float x) { return I2F(__builtin_amdgcn_update_dpp(0, F2I(x), CTRL, 0xf, 0xf, false)); }
DI float sum16(float x) { x += dppf<0x128>(x); x += dppf<0x124>(x); x += dppf<0x122>(x); x += dppf<0x121>(x); return x; }
DI float max16(float x) { x = __builtin_fmaxf(x, dppf<0x128>(x)); x = __builtin_fmaxf(x, dppf<0x124>(x)); x = __builtin_fmaxf(x, dppf<0x122>(x)); x = __builtin_fmaxf(x, dppf<0x121>(x)); return x; }
DI float add32(float x) { auto r = __builtin_amdgcn_permlane32_swap(F2U(x), F2U(x), false, false); return U2F(r[0]) + U2F(r[1]); }
DI float add16(float x) { auto r = __builtin_amdgcn_permlane16_swap(F2U(x), F2U(x), false, false); return U2F(r[0]) + U2F(r[1]); }
DI float max64(float x) {
  x = max16(x);
  auto r = __builtin_amdgcn_permlane16_swap(F2U(x), F2U(x), false, false);
  x = __builtin_fmaxf(U2F(r[0]), U2F(r[1]));
  auto q = __builtin_amdgcn_permlane32_swap(F2U(x), F2U(x), false, false);
  return __builtin_fmaxf(U2F(q[0]), U2F(q[1]));
}
DI u32x2 trread(const char* p) { return __builtin_bit_cast(u32x2, __builtin_amdgcn_ds_read_tr16_b64_v4i16(LDSP(s16x4, p))); }
DI u32x2 trread3(const __attribute__((address_space(3))) char* p) { return __builtin_bit_cast(u32x2, __builtin_amdgcn_ds_read_tr16_b64_v4i16((__attribute__((address_space(3))) s16x4*)p)); }
DI void pinf(float& v) { asm volatile("" : "+v"(v)); }
DI void pin(u32x4& v) { asm volatile("" : "+v"(v)); }
DI void ostore(u32x2 v, u16* p) { *(u32x2*)p = v; }
DI f32x2 cmulp(f32x2 a, f32x2 z, f32x2 zs) { return __builtin_elementwise_fma(a.yx, zs, a * z.xx); }

#define STAMPS
DI unsigned long long RT() { unsigned long long t; SB(); asm volatile("s_memrealtime %0\n\ts_waitcnt lgkmcnt(0)" : "=s"(t) :: "memory"); SB(); return t; }
#define STAMP_DECL unsigned long long stv[16] = {0, 0, 0, 0, 0, 0, 0, 0, 0, 0, 0, 0, 0, 0, 0, 0}
#define STAMP(i) stv[i] = RT()
DI unsigned long long CT() { unsigned long long t; SB(); asm volatile("s_memtime %0\n\ts_waitcnt lgkmcnt(0)" : "=s"(t) :: "memory"); SB(); return t; }
#define CSTAMP(i) ctv[i] = CT()
#define STAMP_OUT(buf, slot) do { if (__builtin_amdgcn_workitem_id_x() == 0) { _Pragma("unroll") for (int i_ = 0; i_ < 8; ++i_) (buf)[(slot) * 8 + i_] = stv[i_]; } } while (0)
#define STAMP_OUT2(buf, slot) do { if (__builtin_amdgcn_workitem_id_x() == 0) { _Pragma("unroll") for (int i_ = 0; i_ < 8; ++i_) (buf)[(slot) * 8 + i_] = stv[8 + i_]; } } while (0)

#define MFMA32(a, b, c) __builtin_amdgcn_mfma_f32_32x32x16_bf16(__builtin_bit_cast(bf16x8, a), __builtin_bit_cast(bf16x8, b), c, 0, 0, 0)
#define ST16(v, off) __builtin_amdgcn_raw_buffer_store_b128(v, rs, (int)(off), 0, 16)
template <int IMM> DI u32x4 gload(u32 voff, const char* sbase) {
  u32x4 r; asm volatile("global_load_dwordx4 %0, %1, %2 offset:%3 sc1" : "=v"(r) : "v"(voff), "s"(sbase), "i"(IMM)); return r;
}
DI void glds16(u32 voff, const char* sbase, u32 ldsaddr) {
  asm volatile("s_mov_b32 m0, %2\n\ts_nop 0\n\tglobal_load_lds_dwordx4 %0, %1 sc1" :: "v"(voff), "s"(sbase), "s"(ldsaddr) : "memory");
}
constexpr unsigned SPIN_CAP = 1u << 16;
template <int OFF> DI u32 ldu16(const u16* p) { u32 r; asm volatile("global_load_ushort %0, %1, off offset:%2" : "=v"(r) : "v"(p), "i"(OFF) : "memory"); return r; }
template <int OFF> DI u32x4 ldnt(const u16* p) { u32x4 r; asm volatile("global_load_dwordx4 %0, %1, off offset:%2 nt" : "=v"(r) : "v"(p), "i"(OFF) : "memory"); return r; }
DI void sgload(unsigned long long& g, GAS unsigned long long* p) { asm volatile("global_load_dwordx2 %0, %1, off sc1" : "+v"(g) : "v"(p) : "memory"); }
DI void sgwait(unsigned long long& g) { asm volatile("s_waitcnt vmcnt(1)" : "+v"(g) :: "memory"); }
DI void sgreload(unsigned long long& g, GAS unsigned long long* p) { asm volatile("global_load_dwordx2 %0, %1, off sc1\n\ts_waitcnt vmcnt(0)" : "+v"(g) : "v"(p) : "memory"); }
#define POLL_ISSUE(v, p, m) asm volatile("s_mov_b64 exec, %3\n\tglobal_atomic_or %0, %1, %2, off sc0\n\ts_mov_b64 exec, -1" : "+v"(v) : "v"(p), "v"(0), "i"(m) : "memory")
template <int NL, int SLP> DI void wait_cnt(GAS u32* p, u32 want, u32& v0, u32& v1, u32& v2) {
  v0 = want; v1 = want; v2 = want;
  POLL_ISSUE(v0, p, (1 << NL) - 1); __builtin_amdgcn_s_sleep(SLP);
  POLL_ISSUE(v1, p, (1 << NL) - 1); __builtin_amdgcn_s_sleep(SLP);
#pragma unroll 1
  for (unsigned spins = 0; spins < SPIN_CAP; ++spins) {
    POLL_ISSUE(v2, p, (1 << NL) - 1);
    asm volatile("s_waitcnt vmcnt(2)" : "+v"(v0) :: "memory");
    if (__builtin_amdgcn_ballot_w64(v0 < want) == 0) break;
    POLL_ISSUE(v0, p, (1 << NL) - 1);
    asm volatile("s_waitcnt vmcnt(2)" : "+v"(v1) :: "memory");
    if (__builtin_amdgcn_ballot_w64(v1 < want) == 0) break;
    POLL_ISSUE(v1, p, (1 << NL) - 1);
    asm volatile("s_waitcnt vmcnt(2)" : "+v"(v2) :: "memory");
    if (__builtin_amdgcn_ballot_w64(v2 < want) == 0) break;
  }
}
#define POLL_KEEP() asm volatile("" :: "v"(pl0), "v"(pl1), "v"(pl2) : "memory")
#define WAIT_VM0() __builtin_amdgcn_s_waitcnt(0x0F70)
#define BAR() __builtin_amdgcn_s_barrier()

constexpr int CV_DSTR = 4 * 4160 + 4608;
constexpr int CV_RED = 2 * CV_DSTR;
constexpr int G1_ST = 4 * 28672;
constexpr int G2_RED = 4 * 28672;
constexpr int G2_X1 = 18 * 8192;
constexpr int TAB_GB = G2_X1 + 8192;
constexpr int TAB_SB = TAB_GB + 256;
constexpr int ARR_CNT = TAB_SB + 1792;
constexpr int KF_LDS = ARR_CNT + 16;
constexpr int KG_REG = 32 * 272;
constexpr int KG_IMG = 2 * CV_DSTR;
constexpr int TASK_LDS = KG_IMG + 8 * KG_REG;
constexpr int KG_TAB = TASK_LDS + 30464;
#define HSLOT stv[14]
#define WSLOT stv[0]
#define W2_N0 13
#define W13_SLP 1
#define G2_B1 6
#define G2_PL 8
#define EPI0 7
#define W_N0 2
#define W_NM 18
#define W_ST 4
#define W_NB 2
#define W_N1 (W_N0 + W_NB)
#define W_N2 (W_N1 + W_NM)
#define SC_SIG 6
#define SC_POLL 21
#define LAS __attribute__((address_space(3)))
#define PIN14(a, o) asm volatile("" : "+v"(a[o]), "+v"(a[o + 1]), "+v"(a[o + 2]), "+v"(a[o + 3]), "+v"(a[o + 4]), "+v"(a[o + 5]), "+v"(a[o + 6]), "+v"(a[o + 7]), \
    "+v"(a[o + 8]), "+v"(a[o + 9]), "+v"(a[o + 10]), "+v"(a[o + 11]), "+v"(a[o + 12]), "+v"(a[o + 13]) :: "memory")

constexpr int g1_e0(int t) { return t == 1 ? EPI0 : 3; }
constexpr int g1_st(int t) { return g1_e0(t) + 19; }
constexpr int g1_an(int t) { return t == 2 ? 18 : 14; }
constexpr int g1_w2(int n) {
  if (n >= 25 && n < 25 + 14 * 4 && (n - 25) % 4 == 0) return (n - 25) / 4;
  if (n >= 84 && n < 84 + 23) { const int q = n - 84; if (q % 3 != 2) return 14 + q - q / 3 < 28 ? 14 + q - q / 3 : -1; }
  return -1;
}
constexpr int g1_sg(int t) { return t == 0 ? 24 : 28 * t + 10; }
constexpr int g1_vm(int a, int b) {
  int c = 0;
  for (int n = a; n < b; ++n) {
    const int t = n / 28, q = n % 28;
    if (n > a) { if (t >= 2 && q == g1_an(t)) ++c; if (n == 108) ++c; if (n == g1_sg(t)) ++c; }
    if (t >= 1 && q == g1_st(t) && n != a) ++c;
    if (g1_w2(n) >= 0) ++c;
  }
  return c;
}
__global__ __launch_bounds__(512) void kf_main(const u16* __restrict__ x, const u16* __restrict__ logA, const u16* __restrict__ Aim,
                                               const u16* __restrict__ Cr, const u16* __restrict__ Ci, const u16* __restrict__ logdt,
                                               const u16* __restrict__ Dsk, const u16* __restrict__ w1, const u16* __restrict__ w3,
                                               const u16* __restrict__ w2, const u16* __restrict__ cvec, const u16* __restrict__ wmod,
                                               char* __restrict__ ws, u16* __restrict__ out, unsigned long long* stamps) {
  __shared__ __attribute__((aligned(16))) char smem[KF_LDS];
  const int tid = __builtin_amdgcn_workitem_id_x(), lane = tid & 63;
  const int w = __builtin_amdgcn_readfirstlane(tid >> 6);
  const int b = __builtin_amdgcn_workgroup_id_x(), xcd = b & 7, idx = b >> 3;
  const int hi = lane >> 5;
  const int G = lane >> 4, qq = (lane & 15) >> 2, pp = lane & 3;
  const u32 voff = lane * 16;
  GAS u32* ctl = (GAS u32*)ws;
  auto rs = __builtin_amdgcn_make_buffer_rsrc(ws, 0, (int)WS_END, 0x00020000);
  const u32 lds0 = (u32)(size_t)LDSP(char, smem);
  STAMP_DECL;
  STAMP(0);
  u32 pl0 = 0, pl1 = 0, pl2 = 0;
  const bool mainwg = b < 224;
  const int dl = w >> 2, q4 = w & 3, t256 = tid & 255;
  auto arrive0 = [&]() __attribute__((always_inline)) {
    u32 old;
    asm volatile("s_waitcnt vmcnt(0)\n\ts_mov_b64 exec, 1\n\tds_add_rtn_u32 %0, %1, %2\n\ts_mov_b64 exec, -1\n\ts_waitcnt lgkmcnt(0)" : "=&v"(old) : "v"(lds0 + ARR_CNT), "v"(1) : "memory");
    if (__builtin_amdgcn_readfirstlane(old) == (mainwg ? 4u : (b < 249 ? 8u : 0u)) && lane < 16) __hip_atomic_fetch_add(ctl + C0_CNT + (lane * 8 + xcd) * 32, 1u, RLX_AGENT);
  };
  GAS u32* c0p = ctl + C0_CNT + ((b & 15) * 8 + lane) * 32;
  int kg_wb, kg_wst;
  u32 kga[12];
  u32 dskr;
  unsigned long long epoch;
  {
    u32 lar = 0, air = 0, crr = 0, cir = 0, ldt = 0;
    if (mainwg) {
      const int d = 2 * b + dl;
      asm volatile("global_load_ushort %0, %5, %6 nt\n\tglobal_load_ushort %1, %5, %7 nt\n\tglobal_load_ushort %4, %10, %11\n\tglobal_load_ushort %2, %5, %8 nt\n\tglobal_load_ushort %3, %5, %9 nt"
                   : "+v"(lar), "+v"(air), "+v"(crr), "+v"(cir), "+v"(ldt) : "v"((d * N_ + t256) * 2), "s"(logA), "s"(Aim), "s"(Cr), "s"(Ci), "v"(d * 2), "s"(logdt) : "memory");
    }
    unsigned long long ep = 0;
    u32 pe = 32u;
    const int task = b >= 249 ? 2 * b - 364 + dl : (dl || b >= 246 ? -1 : (b < 134 ? b : b + 14));
    char* stg = smem + (dl ? 0 : TASK_LDS);
    u32x4 v[7]; u32 cv[7];
    auto taskload = [&]() __attribute__((always_inline)) {
      const u16* src; u32 step;
      if (task < 64) { src = x + (size_t)(task * 32 + (t256 >> 3)) * D_ + (t256 & 7) * 8; step = 128; }
      else { const int m = task - 64; src = wmod + (size_t)(224 * (m & 1) + (t256 >> 3)) * 2688 + (m >> 1) * 64 + (t256 & 7) * 8; step = 64 * 2688; }
#pragma unroll
      for (int i = 0; i < 7; ++i) asm volatile("global_load_dwordx4 %0, %1, off nt" : "=v"(v[i]) : "v"((const char*)src + (size_t)i * step) : "memory");
      const u16* cp = cvec + 224 * (task & 1) + (t256 >> 3);
      static_for<0, 7>([&](auto ic) __attribute__((always_inline)) { constexpr int i = decltype(ic)::value; cv[i] = ldu16<64 * i>(cp); });
    };
    if (task >= 0 && task < 134) {
      taskload();
    } else if (task >= 148) {
      const int m = task - 148;
      const u16* src = ((t256 & 128) ? w3 : w1) + (size_t)(8 * (m >> 1) + ((t256 >> 4) & 7)) * F_ + (m & 1) * 896 + (t256 & 15) * 8;
      static_for<0, 7>([&](auto ic) __attribute__((always_inline)) { constexpr int i = decltype(ic)::value; v[i] = ldnt<256 * i>(src); });
    } else if (task < 0 && !mainwg) {
      if (dl) ST16((u32x4{0, 0, 0, 0}), ZERO_LO + (b - 224) * 4096 + t256 * 16);
      if (b == 248 && w == 0) {
        const unsigned long long e0 = __hip_atomic_load((GAS unsigned long long*)(ctl + EP_E), RLX_AGENT);
#pragma unroll
        for (int i = 0; i < 2; ++i) __hip_atomic_store(ctl + C0_CNT + i * 2048 + lane * 32, 0u, RLX_AGENT);
        WAIT_VM0();
        asm volatile("s_waitcnt vmcnt(0)" ::: "memory");
        if (lane < 16) __hip_atomic_store((GAS unsigned long long*)(ctl + EP_R + lane * 32), e0 + 1, RLX_AGENT);
      }
    }
    SB();
    dskr = *(const u32*)(Dsk + 2 * (mainwg ? b : 0));
    asm volatile("" :: "s"(dskr));
    if (tid < 4) *(u32*)(smem + ARR_CNT + tid * 4) = 0;
    asm volatile("s_waitcnt lgkmcnt(0)\n\ts_barrier" ::: "memory");
    SB();
    auto meet = [&](u32 want) __attribute__((always_inline)) {
      asm volatile("s_waitcnt lgkmcnt(0)\n\ts_mov_b64 exec, 1\n\tds_add_u32 %0, %1 offset:4\n\ts_mov_b64 exec, -1" :: "v"(lds0 + ARR_CNT + dl * 4), "v"(1) : "memory");
#pragma unroll 1
      for (unsigned spins = 0; spins < 16 * SPIN_CAP; ++spins) {
        if (*(volatile LAS u32*)(size_t)(lds0 + ARR_CNT + 4 + dl * 4) >= want) break;
        __builtin_amdgcn_s_sleep(1);
      }
      asm volatile("" ::: "memory");
    };
#pragma unroll
    for (int h = 0; h < 2; ++h) {
      const int lb = 2040 - 8 * t256 - 8 * h;
      const int kr = lb < 256 ? 0 : 24 - __builtin_clz(lb);
      const float mB = U2F((u32)(150 + kr) << 23) + (float)(lb < 0 ? 704 : lb);
      const u32 kpa = lds0 + (u32)(KG_TAB + dl * 1536) + 256u * kr - ((u32)(150 + kr) << 24);
#pragma unroll
      for (int e = 0; e < (h ? 4 : 8); ++e) { kga[8 * h + e] = (F2U(mB + (float)(7 - e)) << 1) + kpa; asm volatile("" : "+v"(kga[8 * h + e])); }
    }
    if (t256 == 0) *(u32x4*)(smem + KG_TAB + dl * 1536 + 1408) = u32x4{0, 0, 0, 0};
    {
      char* cp = smem + dl * CV_DSTR;
      if (mainwg) {
        if (t256 < 32) *(u32x4*)(cp + 4 * 4160 + t256 * 16) = u32x4{0, 0, 0, 0};
        else if (t256 < 40) *(u32x4*)(cp + ((t256 - 32) >> 1) * 4160 + 4096 + (t256 & 1) * 16) = u32x4{0, 0, 0, 0};
      }
    }
    const int s = lane & 31;
    {
      const int kw = s < 8 ? 0 : (s < 20 ? (s >> 2) - 1 : 3);
      int wb = s < 12 ? (s < 8 ? s : 248 + s) : (s < 16 ? 384 + ((s & 2) << 5) + (s & 1) : 32 * s);
      kg_wst = 16 >> kw;
      kg_wb = (wb > 672 ? 672 : wb) * 2 + KG_TAB + dl * 1536 + (8 * q4 + 4 * hi) * kg_wst;
    }
    char* reg = smem + KG_IMG + w * KG_REG;
    f16x8 bfr[8];
    f32x2 z8, z16; float scl = 0.f, fs = 0.f;
    if (mainwg) {
      if (task < 0) asm volatile("s_waitcnt vmcnt(0)" ::: "memory"); else if (task < 148) asm volatile("s_waitcnt vmcnt(14)" ::: "memory"); else asm volatile("s_waitcnt vmcnt(7)" ::: "memory");
      asm volatile("" : "+v"(lar), "+v"(air), "+v"(crr), "+v"(cir), "+v"(ldt) :: "memory");
    }
    if (mainwg) {
      const float dt = rbf(fexp(bflo(ldt)));
      const float ar = -rbf(fexp(bflo(lar))) * dt * 1.4426950408889634f;
      const float ai = bflo(air) * dt;
      const float I2PH = 0.15915494f, I2PL = 6.4206382e-9f;
      const float th = ai * I2PH;
      const float tl = __builtin_fmaf(ai, I2PL, __builtin_fmaf(ai, I2PH, -th));
      auto zpow = [&](float sg) __attribute__((always_inline)) {
        float e = __builtin_amdgcn_exp2f(ar * sg);
        float rev = __builtin_fmaf(tl, sg, __builtin_amdgcn_fractf(th * sg));
        return f32x2{e * __builtin_amdgcn_cosf(rev), e * __builtin_amdgcn_sinf(rev)};
      };
      const f32x2 z1 = zpow(1.f), z256 = zpow(256.f);
      z16 = zpow(16.f);
      const f32x2 s1 = {-z1.y, z1.y}, s256 = {-z256.y, z256.y};
      u32 eb = ((crr & 0x7f80u) > (cir & 0x7f80u) ? (crr & 0x7f80u) : (cir & 0x7f80u));
      asm("s_nop 1\n\tv_max_u32_dpp %0, %0, %0 row_ror:8 row_mask:0xf bank_mask:0xf\n\ts_nop 1\n\tv_max_u32_dpp %0, %0, %0 row_ror:4 row_mask:0xf bank_mask:0xf\n\ts_nop 1\n\t"
          "v_max_u32_dpp %0, %0, %0 row_ror:2 row_mask:0xf bank_mask:0xf\n\ts_nop 1\n\tv_max_u32_dpp %0, %0, %0 row_ror:1 row_mask:0xf bank_mask:0xf\n\ts_nop 1" : "+v"(eb));
      {
        const u32 e0 = __builtin_amdgcn_readlane(eb, 0), e1 = __builtin_amdgcn_readlane(eb, 16), e2 = __builtin_amdgcn_readlane(eb, 32), e3 = __builtin_amdgcn_readlane(eb, 48);
        const u32 m0 = e0 > e1 ? e0 : e1, m1 = e2 > e3 ? e2 : e3;
        eb = (m0 > m1 ? m0 : m1) >> 7;
      }
      eb = eb < 1u ? 1u : (eb > 252u ? 252u : eb);
      scl = U2F((253u - eb) << 23);
      fs = 2.f * dt * U2F((eb + 1u) << 23);
      f32x2 lo[9], hp[9];
      lo[1] = z1; hp[1] = z256;
#pragma unroll
      for (int i = 2; i <= 8; ++i) { lo[i] = cmulp(lo[i - 1], z1, s1); hp[i] = cmulp(hp[i - 1], z256, s256); }
      const f32x2 a2 = cmulp(lo[2], z256, s256), a4 = cmulp(lo[4], z256, s256), a6 = cmulp(lo[6], z256, s256);
      const f32x2 b4 = cmulp(a4, z256, s256), c4 = cmulp(b4, z256, s256);
      const u32 col[21] = {0x00003c00u, pkh(lo[1]), pkh(lo[2]), pkh(lo[3]), pkh(lo[4]), pkh(lo[5]), pkh(lo[6]), pkh(lo[7]),
                           pkh(hp[1]), pkh(a2), pkh(a4), pkh(a6), pkh(hp[2]), pkh(b4), pkh(hp[3]), pkh(c4),
                           pkh(hp[4]), pkh(hp[5]), pkh(hp[6]), pkh(hp[7]), pkh(hp[8])};
#pragma unroll
      for (int i = 0; i < 21; ++i) *(u32*)(reg + i * 272 + lane * 4) = col[i];
      z8 = lo[8];
#pragma unroll
      for (int i = 0; i < 8; ++i) bfr[i] = *(const f16x8*)(reg + s * 272 + i * 32 + hi * 16);
      asm volatile("s_waitcnt lgkmcnt(0)" : "+v"(bfr[0]), "+v"(bfr[1]), "+v"(bfr[2]), "+v"(bfr[3]), "+v"(bfr[4]), "+v"(bfr[5]), "+v"(bfr[6]), "+v"(bfr[7]) :: "memory");
    }
    STAMP(8);
    unsigned long long tagr = 0;
    GAS u32* tagp = ctl + EP_R + (b & 15) * 32;
#define EP_ISSUE() asm volatile("s_mov_b64 exec, 1\n\tglobal_load_dwordx2 %1, %4, off sc1\n\tglobal_atomic_or_x2 %0, %2, %3, off sc0\n\ts_mov_b64 exec, -1" \
                     : "+&v"(tagr), "+&v"(ep) : "v"(tagp), "v"(0ull), "v"(ctl + EP_E) : "memory")
    auto epcheck = [&]() __attribute__((always_inline)) {
#pragma unroll 1
      for (unsigned spins = 0; spins < SPIN_CAP; ++spins) {
        asm volatile("s_waitcnt vmcnt(0)" : "+v"(tagr), "+v"(ep) :: "memory");
        if ((__builtin_amdgcn_ballot_w64(tagr != ep + 1) & 1) == 0) break;
        EP_ISSUE();
      }
      arrive0();
    };
    if (w == 4) { EP_ISSUE(); if (b >= 249) epcheck(); }
    if (b >= 249) taskload();
#define PIN_V() asm volatile("s_waitcnt vmcnt(0)" : "+v"(v[0]), "+v"(v[1]), "+v"(v[2]), "+v"(v[3]), "+v"(v[4]), "+v"(v[5]), "+v"(v[6]) :: "memory")
    if (task >= 0 && task < 148) {
      PIN_V();
      if (task < 64) {
            const int row = t256 >> 3, p = t256 & 7;
            float sm = 0.f, sq = 0.f;
#pragma unroll
            for (int i = 0; i < 7; ++i) {
              pin(v[i]);
              *(u32x4*)(stg + row * 928 + (p + 8 * i) * 16) = v[i];
#pragma unroll
              for (int j = 0; j < 4; ++j) { sm = dot2(v[i][j], 0x3f803f80u, sm); sq = dot2(v[i][j], v[i][j], sq); }
            }
            sm += dppf<0xB1>(sm); sq += dppf<0xB1>(sq); sm += dppf<0x4E>(sm); sq += dppf<0x4E>(sq); sm += dppf<0x141>(sm); sq += dppf<0x141>(sq);
            const float m = sm * (1.f / D_);
            const float var = __builtin_fmaxf(sq * (1.f / D_) - m * m, 0.f);
            const float sd = rbf(__builtin_amdgcn_sqrtf(rbf(rbf(var) + 1e-5f)));
            const float rsd = __builtin_amdgcn_rcpf(sd);
            if (p < 2) __builtin_amdgcn_raw_buffer_store_b32(F2U(p ? -rbf(m) * rsd : rsd), rs, (int)WS_ST1 + p * 8192 + (task * 32 + row) * 4, 0, 16);
        meet(4u);
        const char* rb = stg + (8 * G + qq) * 928 + q4 * 224 + pp * 8;
#pragma unroll
        for (int i = 0; i < 7; ++i) {
          const u32x2 lo = trread(rb + i * 32), hh = trread(rb + i * 32 + 4 * 928);
          ST16((u32x4{lo.x, lo.y, hh.x, hh.y}), (int)WS_XT + task * 28672 + q4 * 7168 + (lane & 15) * 64 + G * 16 + i * 1024);
        }
      } else {
            float acc[8] = {0, 0, 0, 0, 0, 0, 0, 0};
#pragma unroll
            for (int i = 0; i < 7; ++i) {
              asm volatile("" : "+v"(cv[i]));
              const u32 sh = F2U(silu_bf(bflo(cv[i]))), sl = sh >> 16;
#pragma unroll
              for (int j = 0; j < 4; ++j) { acc[2 * j] = dot2(v[i][j], sl, acc[2 * j]); acc[2 * j + 1] = dot2(v[i][j], sh, acc[2 * j + 1]); }
            }
#pragma unroll
            for (int j = 0; j < 8; ++j) { acc[j] += dppf<0x128>(acc[j]); acc[j] = add32(acc[j]); }
            if ((lane & 40) == 0) {
              float* red = (float*)stg + (q4 * 2 + (lane >> 4)) * 64 + (lane & 7) * 8;
              *(f32x4*)red = f32x4{acc[0], acc[1], acc[2], acc[3]};
              *(f32x4*)(red + 4) = f32x4{acc[4], acc[5], acc[6], acc[7]};
            }
        meet(4u);
            if (q4 == 0) {
              const int m = task - 64, cg = m >> 1;
              float t = 0.f;
#pragma unroll
              for (int j = 0; j < 8; ++j) t += ((const float*)stg)[j * 64 + lane];
              const int sec = cg / 7, dd = (cg % 7) * 64 + lane;
              const int o1 = sec == 3 ? (int)WS_MODB + dd * 16 : (sec == 5 ? (int)WS_MODC + dd * 8 : (int)WS_MODA + dd * 32 + (sec == 4 ? 3 : sec) * 8);
              __builtin_amdgcn_raw_buffer_store_b32(F2U(t), rs, o1 + (m & 1) * 4, 0, 16);
              if (sec == 4) __builtin_amdgcn_raw_buffer_store_b32(F2U(t), rs, (int)WS_MODB + dd * 16 + 8 + (m & 1) * 4, 0, 16);
            }
      }
      STAMP(13);
    }
    if (task >= 148) {
      PIN_V();
#pragma unroll
      for (int i = 0; i < 7; ++i) *(u32x4*)(stg + (t256 >> 4) * 1824 + ((t256 & 15) + 16 * i) * 16) = v[i];
      meet(4u);
      const int m = task - 148;
      const char* rb = stg + (8 * (G & 1) + qq) * 1824 + (14 * q4 + hi) * 32 + (8 * (pp & 1) + 4 * (pp >> 1)) * 2;
      const int dst = (int)WS_W13 + ((56 * (m & 1) + 14 * q4 + hi) * 28 + (m >> 2)) * 1024 + ((m >> 1) & 1) * 512 + (lane & 31) * 16;
#pragma unroll
      for (int i = 0; i < 7; ++i) {
        const u32x2 lo = trread(rb + i * 64), hh = trread(rb + i * 64 + 4 * 1824);
        ST16((u32x4{lo.x, lo.y, hh.x, hh.y}), dst + i * 57344);
      }
    }
    if (mainwg) {
        pinf(scl);
        f32x2 ra = {bflo(crr) * scl, -bflo(cir) * scl};
        f32x2 rb = cmulp(ra, z8, f32x2{z8.y, -z8.y});
        const f32x2 s16c = {z16.y, -z16.y};
#pragma unroll
        for (int r = 0; r < 32; r += 2) {
          *(u32*)(reg + r * 272 + lane * 4) = pkh(ra); *(u32*)(reg + (r + 1) * 272 + lane * 4) = pkh(rb);
          if (r < 30) { ra = cmulp(ra, z16, s16c); rb = cmulp(rb, z16, s16c); }
        }
        if (task >= 0) { WAIT_VM0(); arrive0(); STAMP(11); }
        if (w == 4) epcheck();
        f32x16 acc = {};
#pragma unroll
        for (int i = 0; i < 8; ++i) {
          const f16x8 af = *(const f16x8*)(reg + s * 272 + i * 32 + hi * 16);
          acc = __builtin_amdgcn_mfma_f32_32x32x16_f16(af, bfr[i], acc, 0, 0, 0);
        }
#pragma unroll
        for (int qd = 0; qd < 4; ++qd)
          *(f32x4*)(reg + qd * 1024 + lane * 16) = f32x4{acc[4 * qd] * fs, acc[4 * qd + 1] * fs, acc[4 * qd + 2] * fs, acc[4 * qd + 3] * fs};
    }
    if (mainwg) {
      if (w == 4) POLL_ISSUE(pe, c0p, 255);
      meet(dl ? 4u : 8u);
    {
      const char* rp = smem + KG_IMG + dl * 4 * KG_REG + q4 * 1024 + lane * 16;
      const f32x4 t = (*(const f32x4*)rp + *(const f32x4*)(rp + KG_REG)) + (*(const f32x4*)(rp + 2 * KG_REG) + *(const f32x4*)(rp + 3 * KG_REG));
      const u32 ta = pkbf(t[0], t[1]), tc = pkbf(t[2], t[3]);
      char* q = smem + kg_wb;
      *(u16*)q = (u16)ta; *(u16*)(q + kg_wst) = (u16)(ta >> 16); *(u16*)(q + 2 * kg_wst) = (u16)tc; *(u16*)(q + 3 * kg_wst) = (u16)(tc >> 16);
    }
    }
    if (!mainwg && b < 249) { WAIT_VM0(); arrive0(); }
    if (w == 4) {
      asm volatile("s_waitcnt vmcnt(0)" : "+v"(pe) :: "memory");
      if (mainwg) { if (__builtin_amdgcn_ballot_w64(pe < 32u) != 0) wait_cnt<8, 6>(c0p, 32u, pl0, pl1, pl2); }
      else if (b < 249) epcheck();
    }
    STAMP(14);
    epoch = ep + 1;
    asm volatile("s_waitcnt lgkmcnt(0)\n\ts_barrier" ::: "memory");
  }
  if (!mainwg) {
    const int fr = b - 224;
    if (fr < 28) {
      u32x4 v[7];
#pragma unroll
      for (int i = 0; i < 7; ++i) v[i] = LDIN((const u32x4*)(w2 + (size_t)(64 * fr + (tid >> 3)) * D_ + ((tid & 7) + 8 * i) * 8));
#pragma unroll
      for (int i = 0; i < 7; ++i) *(u32x4*)(smem + (tid >> 3) * 912 + ((tid & 7) + 8 * i) * 16) = v[i];
      asm volatile("s_waitcnt lgkmcnt(0)\n\ts_barrier" ::: "memory");
      const char* rbase = smem + (16 * (w & 3) + 8 * hi + qq) * 912 + (w >> 2) * 448 + (16 * (G & 1) + 4 * pp) * 2;
      const int dst = (int)WS_W2F + ((7 * (w >> 2)) * 112 + 4 * fr + (w & 3)) * 1024 + lane * 16;
#pragma unroll
      for (int s = 0; s < 7; ++s) {
        u32x2 lo = trread(rbase + s * 64), hh = trread(rbase + s * 64 + 4 * 912);
        __builtin_amdgcn_raw_buffer_store_b128(u32x4{lo.x, lo.y, hh.x, hh.y}, rs, dst + s * 112 * 1024, 0, 16);
      }
    }
    asm volatile("s_waitcnt vmcnt(0)\n\ts_barrier" ::: "memory");
    if (w == 4) {
      wait_cnt<8, 6>(c0p, 32u, pl0, pl1, pl2);
      asm volatile("s_waitcnt vmcnt(0)" ::: "memory");
      POLL_KEEP();
      if (b == 248 && lane == 0) __hip_atomic_store((GAS unsigned long long*)(ctl + EP_E), epoch, RLX_AGENT);
      if (lane < 16) __hip_atomic_fetch_add(ctl + C1_CNT + (lane * 9 + 8) * 32, 1u, RLX_AGENT);
      wait_cnt<8, 6>(ctl + C1_CNT + ((b & 15) * 9 + lane) * 32, 28u, pl0, pl1, pl2);
      asm volatile("s_waitcnt vmcnt(0)" ::: "memory");
      POLL_KEEP();
    }
    asm volatile("s_barrier" ::: "memory");
    {
      const char* xsrc = ws + WS_X1T + (size_t)(2 * fr + (w >> 2)) * 28672 + (w & 3) * 1024;
#pragma unroll
      for (int i = 0; i < 7; ++i) glds16(voff, xsrc + i * 4096, lds0 + w * 7168 + i * 1024);
      const char* sp = smem + w * 7168 + (8 * hi + qq) * 64 + (G & 1) * 32 + pp * 8;
      float s0 = 0.f, q0 = 0.f, s1 = 0.f, q1 = 0.f;
      static_for<0, 7>([&](auto ic) __attribute__((always_inline)) {
        constexpr int i = decltype(ic)::value;
        asm volatile("s_waitcnt vmcnt(%0)" :: "i"(6 - i) : "memory");
        const u32x2 lo = trread(sp + i * 1024), hh = trread(sp + i * 1024 + 256);
        s0 = dot2(lo.x, 0x3f803f80u, s0); q0 = dot2(lo.x, lo.x, q0); s1 = dot2(lo.y, 0x3f803f80u, s1); q1 = dot2(lo.y, lo.y, q1);
        s0 = dot2(hh.x, 0x3f803f80u, s0); q0 = dot2(hh.x, hh.x, q0); s1 = dot2(hh.y, 0x3f803f80u, s1); q1 = dot2(hh.y, hh.y, q1);
      });
      auto r = __builtin_amdgcn_permlane32_swap(F2U(s0 + s1), F2U(q0 + q1), false, false);
      *(float*)(smem + 65536 + w * 256 + (lane & 31) * 8 + hi * 4) = U2F(r[0]) + U2F(r[1]);
      asm volatile("s_waitcnt lgkmcnt(0)\n\ts_barrier" ::: "memory");
      if (w == 0) {
        const char* pt = smem + 65536 + hi * 1024 + (lane & 31) * 8;
        const f32x2 ts = (*(const f32x2*)pt + *(const f32x2*)(pt + 256)) + (*(const f32x2*)(pt + 512) + *(const f32x2*)(pt + 768));
        const float m = ts[0] * (1.f / D_);
        const float var = __builtin_fmaxf(ts[1] * (1.f / D_) - m * m, 0.f);
        const float sd = __builtin_amdgcn_sqrtf(rbf(rbf(var) + 1e-5f));
        __hip_atomic_store((GAS unsigned long long*)(ws + ST2_OFF) + 64 * fr + lane, (1ull << 32) | pkbf(-m, sd), RLX_AGENT);
      }
    }
    return;
  }
  const int rt = 4 * (xcd >> 1) + idx / 7, ct = 7 * (xcd & 1) + idx % 7;
  const int ft = 8 * ct + w;
  const int r2 = 4 * xcd + idx / 7, dg = idx % 7, kq = w >> 1, dt = w & 1;
  const int te = 4 * (r2 >> 1) + (r2 & 1), tl = 4 * (r2 >> 1) + 3 - (r2 & 1);
  const int wv = (int)WS_W13 + ft * 28672 + lane * 16;
#define WLOAD(p) wf[p] = __builtin_amdgcn_raw_buffer_load_b128(rs, wv + ((p) & 3) * 1024, ((p) & ~3) * 1024, 16)

  u32x4 wf[28];
  u32x4 wex;
  const int hbase = (int)WS_HF + (4 * rt * 112 + ft) * 1024 + lane * 16;

  {
    const int a = lane & 15;
    const int l0 = 256 * w + 16 * a + 4 * G;
    const int xoff = ((l0 >> 5) * 448 + 2 * b) * 64 + (l0 & 31) * 2;
    STAMP(9);
    u32x2 xx[2]; u32x4 st[2];
    float msv = U2F(__builtin_amdgcn_raw_buffer_load_b32(rs, (int)WS_MODA + (lane & 15) * 4, b * 64, 16));
#pragma unroll
    for (int i = 0; i < 2; ++i) {
      xx[i] = __builtin_amdgcn_raw_buffer_load_b64(rs, (int)WS_XT + xoff + i * 64, 0, 16);
      st[i] = __builtin_amdgcn_raw_buffer_load_b128(rs, (int)WS_ST1 + l0 * 4, i * 8192, 16);
    }
    SB();
    {
      u32 kw[6];
#pragma unroll
      for (int e = 0; e < 6; ++e) kw[e] = (u32)*(const LAS u16*)(size_t)kga[2 * e] | ((u32)*(const LAS u16*)(size_t)kga[2 * e + 1] << 16);
      char* cp = smem + dl * CV_DSTR;
      u32 al[5];
#pragma unroll
      for (int i = 0; i < 5; ++i) al[i] = __builtin_amdgcn_alignbit(kw[i + 1], kw[i], 16);
      char* q = cp + t256 * 16;
      *(u32x4*)(q) = u32x4{kw[0], kw[1], kw[2], kw[3]};
      *(u32x4*)(q + 4160) = u32x4{al[0], al[1], al[2], al[3]};
      *(u32x4*)(q + 8320) = u32x4{kw[1], kw[2], kw[3], kw[4]};
      *(u32x4*)(q + 12480) = u32x4{al[1], al[2], al[3], al[4]};
    }
    SB();
    const f32x4 tb = __builtin_bit_cast(f32x4, __builtin_amdgcn_raw_buffer_load_b128(rs, (int)WS_MODB + (tid < 448 ? tid : 447) * 16, 0, 16));
    SB();
#pragma unroll
    for (int i = 0; i < W_N1; ++i) WLOAD(i);
    SB();
    msv = rbf(msv + dppf<0xB1>(msv));
    if (lane & 2) msv = rbf(1.f + msv);
#define MSV(i) U2F(__builtin_amdgcn_readlane(F2U(msv), i))
    float ga[2], b2v[2];
    const int swl = (lane ^ ((lane >> 5) << 3)) * 16;
#pragma unroll
    for (int i = 0; i < 2; ++i) {
      const float sa = MSV(8 * i), beta = MSV(8 * i + 2);
      ga[i] = MSV(8 * i + 4); b2v[i] = MSV(8 * i + 6);
      const float gd = ga[i] * (i ? bfhi(dskr) : bflo(dskr));
      const float xv[4] = {bflo(xx[i].x), bfhi(xx[i].x), bflo(xx[i].y), bfhi(xx[i].y)};
      float hv[4];
#pragma unroll
      for (int e = 0; e < 4; ++e)
        hv[e] = __builtin_fmaf(__builtin_fmaf(xv[e], U2F(st[0][e]), U2F(st[1][e])), beta, sa);
      *(u32x2*)(smem + i * CV_DSTR + 4 * 4160 + 512 + l0 * 2) = u32x2{pkbf(hv[0], hv[1]), pkbf(hv[2], hv[3])};
      f32x4 base;
#pragma unroll
      for (int e = 0; e < 4; ++e) base[e] = __builtin_fmaf(hv[e], gd, xv[e]);
      *(f32x4*)(smem + CV_RED + (i * 40 + w * 5 + 4) * 1024 + swl) = base;
    }
    SB();
    POLL_KEEP();
    asm volatile("s_waitcnt lgkmcnt(0)\n\ts_barrier" ::: "memory");
    STAMP(1);
    f32x4 acc[8];
#pragma unroll
    for (int t = 0; t < 8; ++t) acc[t] = f32x4{0, 0, 0, 0};
    const int im = 15 - a;
    const u32 abase = lds0 + dl * CV_DSTR + (im & 3) * 4160 + (2016 + 8 * G + 4 * (im >> 2)) * 2 - q4 * 128 - 3648;
    const u32 bbase = lds0 + dl * CV_DSTR + 4 * 4160 + (256 - 16 + 16 * a + 8 * G) * 2 - q4 * 128 - 64;
    char* dump = smem + CV_RED + (dl * 40 + q4) * 1024 + swl;
#pragma unroll
    for (int cc = 0; cc < 2; ++cc) {
      u32x2 alo[8], ahi[8]; u32x4 bfr[8];
#pragma unroll
      for (int c1 = 0; c1 < 8; ++c1)
        asm volatile("ds_read_b64 %0, %3 offset:%5\n\tds_read_b64 %1, %3 offset:%6\n\tds_read_b128 %2, %4 offset:%7"
                     : "=&v"(alo[c1]), "=&v"(ahi[c1]), "=&v"(bfr[c1])
                     : "v"(abase), "v"(bbase), "i"(3648 - (8 * c1 + cc) * 64), "i"(3656 - (8 * c1 + cc) * 64), "i"(64 + (8 * c1 - cc) * 64) : "memory");
      SB();
      if (cc == 0) __builtin_amdgcn_s_setprio(1); else __builtin_amdgcn_s_setprio(0);
#pragma unroll
      for (int t = 0; t < 8; ++t) {
        if (t == 0) asm volatile("s_waitcnt lgkmcnt(15)" : "+v"(alo[0]), "+v"(ahi[0]), "+v"(bfr[0]), "+v"(alo[1]), "+v"(ahi[1]), "+v"(bfr[1]), "+v"(alo[2]), "+v"(ahi[2]), "+v"(bfr[2]));
        if (t == 3) asm volatile("s_waitcnt lgkmcnt(9)" : "+v"(alo[3]), "+v"(ahi[3]), "+v"(bfr[3]), "+v"(alo[4]), "+v"(ahi[4]), "+v"(bfr[4]));
        if (t == 5) asm volatile("s_waitcnt lgkmcnt(0)" : "+v"(alo[5]), "+v"(ahi[5]), "+v"(bfr[5]), "+v"(alo[6]), "+v"(ahi[6]), "+v"(bfr[6]), "+v"(alo[7]), "+v"(ahi[7]), "+v"(bfr[7]));
#pragma unroll
        for (int c1 = 0; c1 <= t; ++c1) {
          acc[t] = __builtin_amdgcn_mfma_f32_16x16x32_bf16(__builtin_bit_cast(bf16x8, u32x4{alo[c1].x, alo[c1].y, ahi[c1].x, ahi[c1].y}),
                                                           __builtin_bit_cast(bf16x8, bfr[t - c1]), acc[t], 0, 0, 0);
          const int n = t * (t + 1) / 2 + c1;
          if ((n % W_ST) == W_ST - 1 && W_N1 + (36 / W_ST) * cc + n / W_ST < W_N2) { SB(); WLOAD(W_N1 + (36 / W_ST) * cc + n / W_ST); SB(); }
          if (cc == 1 && t >= 5 && c1 >= 1) {
            const int u = (t == 5 ? c1 - 1 : (t == 6 ? (c1 == 1 ? 5 : -1) : (c1 == 1 ? 6 : -1)));
            if (u >= 0) { SB(); *(f32x4*)(dump + u * 5120) = acc[u]; SB(); }
          }
        }
        SB();
      }
    }
    *(f32x4*)(dump + 7 * 5120) = acc[7];
    STAMP(2);
    SB();
#pragma unroll
    for (int i = W_N2; i < 28; ++i) WLOAD(i);
    SB();
    if (tid < 448) {
      const float be = rbf(1.f + rbf(tb[2] + tb[3]));
      *(u16*)(smem + TAB_SB + tid * 2) = (u16)(F2U(rbf(tb[0] + tb[1])) >> 16);
      *(u16*)(smem + TAB_SB + 896 + tid * 2) = (u16)(F2U(be) >> 16);
    }
    asm volatile("s_waitcnt lgkmcnt(0)\n\ts_barrier" ::: "memory");
    {
      const int ch = (lane >> 2) & 1;
      const float gac = ch ? ga[1] : ga[0];
      const float b2 = ch ? b2v[1] : b2v[0];
      const char* rp = smem + CV_RED + (ch * 40 + w * 5) * 1024 + (lane & 1) * 512 + ((2 * (lane >> 3) + ((lane >> 1) & 1)) ^ ((lane & 1) << 3)) * 16;
      u32x4 o, ob;
#pragma unroll
      for (int i = 0; i < 2; ++i) {
        const char* q = rp + i * 256;
        const f32x4 y = (*(const f32x4*)q + *(const f32x4*)(q + 1024)) + (*(const f32x4*)(q + 2048) + *(const f32x4*)(q + 3072));
        const f32x4 bs = *(const f32x4*)(q + 4096);
        o[2 * i] = pkbf(__builtin_fmaf(y[0], gac, bs[0]), __builtin_fmaf(y[1], gac, bs[1]));
        o[2 * i + 1] = pkbf(__builtin_fmaf(y[2], gac, bs[2]), __builtin_fmaf(y[3], gac, bs[3]));
      }
#pragma unroll
      for (int i = 0; i < 4; ++i) ob[i] = pkbf(bflo(o[i]) * b2, bfhi(o[i]) * b2);
      const int so = ((8 * w + (lane >> 3)) * 448 + 2 * b) * 64 + (lane & 7) * 16;
      __builtin_amdgcn_raw_buffer_store_b128(ob, rs, (int)WS_XB + so, 0, 16);
      __builtin_amdgcn_raw_buffer_store_b128(o, rs, (int)WS_X1T + so, 0, 16);
    }
    SB();
    STAMP(10);
  }
  u32x4 w2f[28];
  const int rot = (r2 & 1) * 7168;
  const char* w2src = ws + WS_W2F + ((size_t)(2 * dg) * 112 + 14 * w) * 1024;
  WAIT_VM0();
  asm volatile("s_waitcnt vmcnt(0)\n\ts_barrier" ::: "memory");
  STAMP(3);
  if (w == 0 && lane < 16) __hip_atomic_fetch_add(ctl + C1_CNT + (lane * 9 + xcd) * 32, 1u, RLX_AGENT);
  SB();
  auto uvcalc = [&]() __attribute__((always_inline)) {
    f32x16 accw;
#pragma unroll
    for (int r = 0; r < 16; ++r) accw[r] = 0.f;
    const char* tsb = smem + TAB_SB + (G & 1) * 896 + hi * 16;
#pragma unroll
    for (int p = 0; p < 28; ++p) accw = MFMA32(*(const u32x4*)(tsb + p * 32), wf[p], accw);
    const float us = accw[8], vs = accw[0];
    const float uh = rbf(us), vh = rbf(vs);
    wex = u32x4{hi ? 0u : pkbf(uh, us - uh), hi ? 0u : pkbf(vh, vs - vh), 0u, 0u};
    pin(wex);
  };
  if (w == 0) wait_cnt<9, 6>(ctl + C1_CNT + ((b & 15) * 9 + lane) * 32, lane == 8 ? 32u : 28u, pl0, pl1, pl2); else uvcalc();
  asm volatile("s_barrier" ::: "memory");
  STAMP(4);
  {
    const char* xsrc = ws + WS_XB + (size_t)(4 * rt) * 28672 + w * 1024;
#pragma unroll
    for (int i = 0; i < 14; ++i) glds16(voff, xsrc + i * 8192, lds0 + w * 1024 + i * 8192);
    glds16(voff, ws + WS_X1T + ((size_t)(w >> 2 ? tl : te) * 448 + 64 * dg) * 64 + (w & 3) * 1024, lds0 + G2_X1 + w * 1024);
  }
  if (w == 0) uvcalc();
  GAS u32* c2p = ctl + C2_CNT + rt * 1024 + (ft / 14) * 32;
  GAS u32* c2e = ctl + C2_CNT + te * 256 + w * 32;
  GAS u32* c2l = ctl + C2_CNT + tl * 256 + w * 32;
  u32 pv = 14;
#define ANNOUNCE(n, tt) asm volatile("s_waitcnt vmcnt(%3)\n\ts_mov_b64 exec, 1\n\tglobal_atomic_add %0, %1, %2 offset:%4\n\ts_mov_b64 exec, -1" :: "v"(0), "v"(1), "s"(c2p), "i"(n), "i"((tt) * 1024) : "memory")
  {
    u32 rb = lds0 + (8 * hi + qq) * 64 + (G & 1) * 32 + pp * 8;
    GAS unsigned long long* sgp = (GAS unsigned long long*)(ws + ST2_OFF) + 128 * rt + (lane & 31);

    u32x4 xf[4];
    f32x16 acc[2];
    float nal, s2, ex[8], pr[8];
    unsigned long long sg = 0;
#define G1_READ(q) do { const LAS char* p_ = (const LAS char*)(size_t)rb + (q) * 1024; u32x2 lo = trread3(p_), hh = trread3(p_ + 256); xf[(q) & 3] = u32x4{lo.x, lo.y, hh.x, hh.y}; } while (0)
#define SG_WAIT(g, ptr) do { _Pragma("unroll 1") for (unsigned spins = 0; spins < SPIN_CAP; ++spins) { if (__builtin_amdgcn_ballot_w64((u32)((g) >> 32) != 1u) == 0) break; sgreload(g, ptr); } } while (0)
#define SG_APPLY(g, A) do { const u32 st_ = (u32)(g); const u32 mm_ = __builtin_amdgcn_perm(st_, st_, 0x01000100u), ss_ = __builtin_amdgcn_perm(st_, st_, 0x03020302u); \
      const float sd_ = bfhi(st_); nal = -1.4426950408889634f * __builtin_amdgcn_rcpf(sd_); s2 = sd_ * sd_; \
      pinf(nal); pinf(s2); A = MFMA32(wex, (u32x4{mm_, ss_, mm_, ss_}), A); } while (0)
#define EPI_STAGE(A, st, v) do { if constexpr ((st) == 0) { ex[v] = A[v] * nal; pr[v] = A[v] * A[8 + (v)]; pinf(pr[v]); } \
      if constexpr ((st) == 1) ex[v] = __builtin_amdgcn_exp2f(ex[v]); if constexpr ((st) == 2) ex[v] = __builtin_fmaf(ex[v], s2, s2); \
      if constexpr ((st) == 3) ex[v] = __builtin_amdgcn_rcpf(ex[v]); if constexpr ((st) == 4) { pr[v] *= ex[v]; pinf(pr[v]); } else pinf(ex[v]); } while (0)
#define EPI_STORE(tt) __builtin_amdgcn_raw_buffer_store_b128(u32x4{pkbf(pr[0], pr[1]), pkbf(pr[2], pr[3]), pkbf(pr[4], pr[5]), pkbf(pr[6], pr[7])}, rs, hbase, (tt) * 112 * 1024, 16)
    asm volatile("s_waitcnt vmcnt(11)\n\ts_barrier" ::: "memory");
    POLL_KEEP();
    STAMP(12);
    G1_READ(0); G1_READ(1);
    static_for<0, 112>([&](auto nc) __attribute__((always_inline)) {
      constexpr int n = decltype(nc)::value, t = n / 28, q = n % 28;
      constexpr int wl = g1_w2(n);
      if constexpr (t >= 1 && q == g1_e0(t) - 2) {
        asm volatile("s_waitcnt vmcnt(%1)" : "+v"(sg) : "i"(g1_vm(g1_sg(t - 1), n)) : "memory");
        SG_WAIT(sg, sgp + 32 * (t - 1));
      }
      if constexpr (n == 24) asm volatile("s_waitcnt vmcnt(0)\n\ts_barrier" ::: "memory");
      if constexpr (t >= 2 && q == g1_an(t)) ANNOUNCE(g1_vm(28 * (t - 1) + g1_st(t - 1), n), t - 2);
      if constexpr (n == 108) POLL_ISSUE(pv, c2e, 1);
      if constexpr (n == g1_sg(t)) sgload(sg, sgp + 32 * t);
      if constexpr (q == 26 && (t & 1)) { rb += 57344; asm volatile("" : "+v"(rb)); }
      SB();
      if constexpr (n < 110) G1_READ((n + 2) % 56);
      SB();
      if constexpr (q == 0) { _Pragma("unroll") for (int r = 0; r < 16; ++r) acc[t & 1][r] = 0.f; }
      acc[t & 1] = MFMA32(wf[q], xf[q & 3], acc[t & 1]);
      if constexpr (t >= 1) {
        if constexpr (q == g1_e0(t) - 2) SG_APPLY(sg, acc[(t - 1) & 1]);
        static_for<0, 5>([&](auto sc) __attribute__((always_inline)) {
          constexpr int st = decltype(sc)::value, g = q - g1_e0(t) - st, v = g / 2;
          if constexpr (g >= 0 && (g & 1) == 0 && v < 8) EPI_STAGE(acc[(t - 1) & 1], st, v);
        });
        if constexpr (q == g1_st(t)) { SB(); EPI_STORE(t - 1); }
      }
      if constexpr (wl >= 0) { SB(); w2f[wl] = gload<(((wl >> 1) % 7) & 3) * 1024>(voff, w2src + (wl & 1) * 114688 + (wl >= 14 ? 7168 - rot : rot) + (((wl >> 1) % 7) >> 2) * 4096); }
    });
    SB();
    STAMP(5);
    asm volatile("s_waitcnt vmcnt(%1)" : "+v"(sg) : "i"(g1_vm(g1_sg(3), 112)) : "memory");
    SG_WAIT(sg, sgp + 96);
    SG_APPLY(sg, acc[1]);
    static_for<0, 5>([&](auto sc) __attribute__((always_inline)) {
      static_for<0, 8>([&](auto vc) __attribute__((always_inline)) { EPI_STAGE(acc[1], decltype(sc)::value, decltype(vc)::value); });
    });
    EPI_STORE(3);
    SB();
  }
  {
    u32x4 gq[2];
    u32x4 hf[2][14];
    const char* hsrc[2] = {ws + WS_HF + ((size_t)te * 112 + 14 * w) * 1024, ws + WS_HF + ((size_t)tl * 112 + 14 * w) * 1024};
#define H_LOAD(j, lo, hi_) static_for<lo, hi_>([&](auto ic) __attribute__((always_inline)) { constexpr int i = decltype(ic)::value; hf[j][i] = gload<((i % 7) & 3) * 1024>(voff, hsrc[j] + (i >= 7 ? 7168 - rot : rot) + ((i % 7) >> 2) * 4096); })
    pl0 = pl1 = pl2 = 0;
#define ANNOUNCE23(n) asm volatile("s_waitcnt vmcnt(%3)\n\ts_mov_b64 exec, 3\n\tglobal_atomic_add %0, %1, %2 offset:2048\n\ts_mov_b64 exec, -1" :: "v"(lane << 10), "v"(1), "s"(c2p), "i"(n) : "memory")
    asm volatile("s_waitcnt vmcnt(1)\n\ts_barrier" : "+v"(pv) :: "memory");
    const bool slow = __builtin_amdgcn_readfirstlane(pv) < 14u;
    if (slow) {
      ANNOUNCE23(0);
      wait_cnt<1, 6>(c2e, 14u, pl0, pl1, pl2);
    }
    STAMP(6);
    { const char* gp = ws + WS_MODC + (64 * dg + 32 * dt + 8 * kq) * 8; gq[0] = gload<0>(hi * 32, gp); gq[1] = gload<16>(hi * 32, gp); }
    H_LOAD(0, 0, G2_B1);
    if (!slow) ANNOUNCE23(G2_B1 + 2);
    H_LOAD(0, G2_B1, 14);
    static_for<0, 2>([&](auto jc) __attribute__((always_inline)) {
      constexpr int j = decltype(jc)::value;
      f32x16 acc[2];
#pragma unroll
      for (int c = 0; c < 2; ++c)
#pragma unroll
        for (int r = 0; r < 16; ++r) acc[c][r] = 0.f;
      static_for<0, 14>([&](auto sc) __attribute__((always_inline)) {
        constexpr int s = decltype(sc)::value;
        asm volatile("s_waitcnt vmcnt(%1)" : "+v"(hf[j][s]) : "i"(13 - s + (j == 0 && s > G2_PL ? 1 : 0)) : "memory");
        if constexpr (s == 0) POLL_KEEP();
        if constexpr (j == 0 && s == 0) { PIN14(w2f, 0); PIN14(w2f, 14); pin(gq[0]); pin(gq[1]); }
        if constexpr (j == 0 && s == G2_PL) POLL_ISSUE(pv, c2l, 1);
        acc[0] = MFMA32(w2f[2 * s], hf[j][s], acc[0]);
        acc[1] = MFMA32(w2f[2 * s + 1], hf[j][s], acc[1]);
        SB();
      });
      if constexpr (j == 0) {
        asm volatile("s_waitcnt vmcnt(0)" : "+v"(pv) :: "memory");
        if (__builtin_amdgcn_readfirstlane(pv) < 14u) wait_cnt<1, 6>(c2l, 14u, pl0, pl1, pl2);
        H_LOAD(1, 0, 14);
      } else {
        STAMP(7);
      }
      char* red = smem + j * 65536;
#pragma unroll
      for (int P = 0; P < 8; ++P)
        *(f32x4*)(red + (P * 8 + w) * 1024 + lane * 16) = f32x4{acc[P & 1][4 * (P >> 1)], acc[P & 1][4 * (P >> 1) + 1], acc[P & 1][4 * (P >> 1) + 2], acc[P & 1][4 * (P >> 1) + 3]};
      if constexpr (j == 1) {
        const float gbv[4] = {rbf(U2F(gq[0].x) + U2F(gq[0].y)), rbf(U2F(gq[0].z) + U2F(gq[0].w)), rbf(U2F(gq[1].x) + U2F(gq[1].y)), rbf(U2F(gq[1].z) + U2F(gq[1].w))};
        asm volatile("s_waitcnt lgkmcnt(0)\n\ts_barrier" ::: "memory");
#pragma unroll
        for (int jj = 0; jj < 2; ++jj) {
          const char* rp = smem + jj * 65536 + w * 8192 + lane * 16;
          const f32x4 y = ((*(const f32x4*)rp + *(const f32x4*)(rp + 1024)) + (*(const f32x4*)(rp + 2048) + *(const f32x4*)(rp + 3072))) +
                          ((*(const f32x4*)(rp + 4096) + *(const f32x4*)(rp + 5120)) + (*(const f32x4*)(rp + 6144) + *(const f32x4*)(rp + 7168)));
          const u32x2 xr = trread(smem + G2_X1 + jj * 4096 + (32 * dt + 8 * kq + 4 * hi + qq) * 64 + (G & 1) * 32 + pp * 8);
          u32x2 o; o.x = pkbf(__builtin_fmaf(y[0], gbv[0], bflo(xr.x)), __builtin_fmaf(y[1], gbv[1], bfhi(xr.x)));
          o.y = pkbf(__builtin_fmaf(y[2], gbv[2], bflo(xr.y)), __builtin_fmaf(y[3], gbv[3], bfhi(xr.y)));
          ostore(o, out + (size_t)(32 * (jj ? tl : te) + (lane & 31)) * D_ + 64 * dg + 32 * dt + 8 * kq + 4 * hi);
        }
      }
      SB();
    });
  }
  STAMP(15);
  if (b == DIAG_WG) { const unsigned long long lim = stv[15] + 4 * (stv[DIAG_B] - stv[DIAG_A]); while (RT() < lim) __builtin_amdgcn_s_sleep(1); }
}

extern "C" void kernel_launch(void* const* d_in, const int* in_sizes, int n_in, void* d_out, int out_size, void* d_ws, size_t ws_size,
                              hipStream_t stream) {
  const u16* x = (const u16*)d_in[0]; const u16* logA = (const u16*)d_in[1]; const u16* Aim = (const u16*)d_in[2];
  const u16* Cr = (const u16*)d_in[3]; const u16* Ci = (const u16*)d_in[4]; const u16* logdt = (const u16*)d_in[5]; const u16* Dsk = (const u16*)d_in[6];
  const u16* w1 = (const u16*)d_in[8]; const u16* w3 = (const u16*)d_in[9]; const u16* w2 = (const u16*)d_in[10];
  const u16* c = (const u16*)d_in[11]; const u16* wm = (const u16*)d_in[12];
  char* ws = (char*)d_ws;
  unsigned long long* stamps = (unsigned long long*)(ws + (64u << 20));
  kf_main<<<256, 512, 0, stream>>>(x, logA, Aim, Cr, Ci, logdt, Dsk, w1, w3, w2, c, wm, ws, (u16*)d_out, stamps);
}
```

```cpp
#include <hip/hip_runtime.h>

typedef unsigned short u16;
typedef unsigned u32;
typedef u32 u32x4 __attribute__((ext_vector_type(4)));
typedef u32 u32x2 __attribute__((ext_vector_type(2)));
typedef short s16x4 __attribute__((ext_vector_type(4)));
typedef __bf16 bf16x8 __attribute__((ext_vector_type(8)));
typedef __bf16 bf16x2 __attribute__((ext_vector_type(2)));
typedef _Float16 f16x8 __attribute__((ext_vector_type(8)));
typedef _Float16 f16x2 __attribute__((ext_vector_type(2)));
typedef float f32x2 __attribute__((ext_vector_type(2)));
typedef float f32x4 __attribute__((ext_vector_type(4)));
typedef float f32x16 __attribute__((ext_vector_type(16)));
#define DI __device__ __forceinline__
#define U2F(x) __builtin_bit_cast(float, (u32)(x))
#define F2U(x) __builtin_bit_cast(u32, (float)(x))
#define I2F(x) __builtin_bit_cast(float, (int)(x))
#define F2I(x) __builtin_bit_cast(int, (float)(x))
#if defined(__HIP_DEVICE_COMPILE__)
#define NOPK __attribute__((target("no-packed-fp32-ops")))
#else
#define NOPK
#endif
#define LDSP(T, p) ((__attribute__((address_space(3))) T*)(p))
#define GAS __attribute__((address_space(1)))
#define RLX_AGENT __ATOMIC_RELAXED, __HIP_MEMORY_SCOPE_AGENT
#define SB() __builtin_amdgcn_sched_barrier(0)
#define LDIN(p) __builtin_nontemporal_load(p)
template <int V> struct IC { static constexpr int value = V; };
template <int I, int N, class F> __device__ __forceinline__ void static_for(F&& f) { if constexpr (I < N) { f(IC<I>{}); static_for<I + 1, N>(f); } }

constexpr int L_ = 2048, D_ = 448, N_ = 256, F_ = 1792;

constexpr int EP_E = 0;
constexpr int EP_R = 64;
constexpr int C0_CNT = 1024;
constexpr int C1_CNT = 9216;
constexpr int C2_CNT = 14336;
constexpr int ST2_OFF = 122880;
constexpr int ZERO_LO = 36864, ZERO_HI = 139264;
constexpr size_t WS_MODA = 139264;
constexpr size_t WS_MODB = WS_MODA + 448 * 32;
constexpr size_t WS_MODC = WS_MODB + 448 * 16;
constexpr size_t WS_ST1 = 167936;
constexpr size_t WS_XT = WS_ST1 + 16384;
constexpr size_t WS_X1T = WS_XT + 64 * 448 * 64;
constexpr size_t WS_XB = WS_X1T + 64 * 448 * 64;
constexpr size_t WS_W13 = WS_XB + 64 * 448 * 64;
constexpr size_t WS_W2F = WS_W13 + 112 * 28 * 1024;
constexpr size_t WS_HF = WS_W2F + 14 * 112 * 1024;
constexpr size_t WS_END = WS_HF + 64 * 112 * 1024;

DI float bflo(u32 v) { return U2F(v << 16); }
DI float bfhi(u32 v) { return U2F(v & 0xffff0000u); }
DI float bf2f(u16 v) { return U2F(((u32)v) << 16); }
DI u32 pkbf(float lo, float hi) { f32x2 p = {lo, hi}; return __builtin_bit_cast(u32, __builtin_convertvector(p, bf16x2)); }
DI u32 pkh(f32x2 p) { return __builtin_bit_cast(u32, __builtin_convertvector(p, f16x2)); }
DI float rbf(float f) { return bflo(pkbf(f, f)); }
DI float dot2(u32 a, u32 b, float c) { return __builtin_amdgcn_fdot2_f32_bf16(__builtin_bit_cast(bf16x2, a), __builtin_bit_cast(bf16x2, b), c, false); }
DI float fexp(float x) { return __builtin_amdgcn_exp2f(x * 1.4426950408889634f); }
DI float silu_bf(float c) { return rbf(c * __builtin_amdgcn_rcpf(1.f + fexp(-c))); }
template <int CTRL> DI float dppf(float x) { return I2F(__builtin_amdgcn_update_dpp(0, F2I(x), CTRL, 0xf, 0xf, false)); }
DI float sum16(float x) { x += dppf<0x128>(x); x += dppf<0x124>(x); x += dppf<0x122>(x); x += dppf<0x121>(x); return x; }
DI float max16(float x) { x = __builtin_fmaxf(x, dppf<0x128>(x)); x = __builtin_fmaxf(x, dppf<0x124>(x)); x = __builtin_fmaxf(x, dppf<0x122>(x)); x = __builtin_fmaxf(x, dppf<0x121>(x)); return x; }
DI float add32(float x) { auto r = __builtin_amdgcn_permlane32_swap(F2U(x), F2U(x), false, false); return U2F(r[0]) + U2F(r[1]); }
DI float add16(float x) { auto r = __builtin_amdgcn_permlane16_swap(F2U(x), F2U(x), false, false); return U2F(r[0]) + U2F(r[1]); }
DI float max64(float x) {
  x = max16(x);
  auto r = __builtin_amdgcn_permlane16_swap(F2U(x), F2U(x), false, false);
  x = __builtin_fmaxf(U2F(r[0]), U2F(r[1]));
  auto q = __builtin_amdgcn_permlane32_swap(F2U(x), F2U(x), false, false);
  return __builtin_fmaxf(U2F(q[0]), U2F(q[1]));
}
DI u32x2 trread(const char* p) { return __builtin_bit_cast(u32x2, __builtin_amdgcn_ds_read_tr16_b64_v4i16(LDSP(s16x4, p))); }
DI u32x2 trread3(const __attribute__((address_space(3))) char* p) { return __builtin_bit_cast(u32x2, __builtin_amdgcn_ds_read_tr16_b64_v4i16((__attribute__((address_space(3))) s16x4*)p)); }
DI void pinf(float& v) { asm volatile("" : "+v"(v)); }
DI void pin(u32x4& v) { asm volatile("" : "+v"(v)); }
DI void ostore(u32x2 v, u16* p) { *(u32x2*)p = v; }
DI f32x2 cmulp(f32x2 a, f32x2 z, f32x2 zs) { return __builtin_elementwise_fma(a.yx, zs, a * z.xx); }

#define STAMP_DECL
#define STAMP(i)
#define CSTAMP(i)
#define STAMP_OUT(buf, slot)
#define STAMP_OUT2(buf, slot)

#define MFMA32(a, b, c) __builtin_amdgcn_mfma_f32_32x32x16_bf16(__builtin_bit_cast(bf16x8, a), __builtin_bit_cast(bf16x8, b), c, 0, 0, 0)
#define ST16(v, off) __builtin_amdgcn_raw_buffer_store_b128(v, rs, (int)(off), 0, 16)
template <int IMM> DI u32x4 gload(u32 voff, const char* sbase) {
  u32x4 r; asm volatile("global_load_dwordx4 %0, %1, %2 offset:%3 sc1" : "=v"(r) : "v"(voff), "s"(sbase), "i"(IMM)); return r;
}
DI void glds16(u32 voff, const char* sbase, u32 ldsaddr) {
  asm volatile("s_mov_b32 m0, %2\n\ts_nop 0\n\tglobal_load_lds_dwordx4 %0, %1 sc1" :: "v"(voff), "s"(sbase), "s"(ldsaddr) : "memory");
}
constexpr unsigned SPIN_CAP = 1u << 16;
template <int OFF> DI u32 ldu16(const u16* p) { u32 r; asm volatile("global_load_ushort %0, %1, off offset:%2" : "=v"(r) : "v"(p), "i"(OFF) : "memory"); return r; }
template <int OFF> DI u32x4 ldnt(const u16* p) { u32x4 r; asm volatile("global_load_dwordx4 %0, %1, off offset:%2 nt" : "=v"(r) : "v"(p), "i"(OFF) : "memory"); return r; }
DI void sgload(unsigned long long& g, GAS unsigned long long* p) { asm volatile("global_load_dwordx2 %0, %1, off sc1" : "+v"(g) : "v"(p) : "memory"); }
DI void sgwait(unsigned long long& g) { asm volatile("s_waitcnt vmcnt(1)" : "+v"(g) :: "memory"); }
DI void sgreload(unsigned long long& g, GAS unsigned long long* p) { asm volatile("global_load_dwordx2 %0, %1, off sc1\n\ts_waitcnt vmcnt(0)" : "+v"(g) : "v"(p) : "memory"); }
#define POLL_ISSUE(v, p, m) asm volatile("s_mov_b64 exec, %3\n\tglobal_atomic_or %0, %1, %2, off sc0\n\ts_mov_b64 exec, -1" : "+v"(v) : "v"(p), "v"(0), "i"(m) : "memory")
template <int NL, int SLP> DI void wait_cnt(GAS u32* p, u32 want, u32& v0, u32& v1, u32& v2) {
  v0 = want; v1 = want; v2 = want;
  POLL_ISSUE(v0, p, (1 << NL) - 1); __builtin_amdgcn_s_sleep(SLP);
  POLL_ISSUE(v1, p, (1 << NL) - 1); __builtin_amdgcn_s_sleep(SLP);
#pragma unroll 1
  for (unsigned spins = 0; spins < SPIN_CAP; ++spins) {
    POLL_ISSUE(v2, p, (1 << NL) - 1);
    asm volatile("s_waitcnt vmcnt(2)" : "+v"(v0) :: "memory");
    if (__builtin_amdgcn_ballot_w64(v0 < want) == 0) break;
    POLL_ISSUE(v0, p, (1 << NL) - 1);
    asm volatile("s_waitcnt vmcnt(2)" : "+v"(v1) :: "memory");
    if (__builtin_amdgcn_ballot_w64(v1 < want) == 0) break;
    POLL_ISSUE(v1, p, (1 << NL) - 1);
    asm volatile("s_waitcnt vmcnt(2)" : "+v"(v2) :: "memory");
    if (__builtin_amdgcn_ballot_w64(v2 < want) == 0) break;
  }
}
#define POLL_KEEP() asm volatile("" :: "v"(pl0), "v"(pl1), "v"(pl2) : "memory")
#define WAIT_VM0() __builtin_amdgcn_s_waitcnt(0x0F70)
#define BAR() __builtin_amdgcn_s_barrier()

constexpr int CV_DSTR = 4 * 4160 + 4608;
constexpr int CV_RED = 2 * CV_DSTR;
constexpr int G1_ST = 4 * 28672;
constexpr int G2_RED = 4 * 28672;
constexpr int G2_X1 = 18 * 8192;
constexpr int TAB_GB = G2_X1 + 8192;
constexpr int TAB_SB = TAB_GB + 256;
constexpr int ARR_CNT = TAB_SB + 1792;
constexpr int KF_LDS = ARR_CNT + 16;
constexpr int KG_REG = 32 * 272;
constexpr int KG_IMG = 2 * CV_DSTR;
constexpr int TASK_LDS = KG_IMG + 8 * KG_REG;
constexpr int KG_TAB = TASK_LDS + 30464;
#define HSLOT stv[14]
#define WSLOT stv[0]
#define W2_N0 13
#define W13_SLP 1
#define G1MAP 0
#define CODEPF 29
#define X_N0 6
#define G2_B1 6
#define G2_PL 8
#define EPI0 7
#define W_N0 2
#define W_NM 18
#define W_ST 4
#define W_NB 2
#define W_N1 (W_N0 + W_NB)
#define W_N2 (W_N1 + W_NM)
#define W_ND 2
#define W_N3 (W_N2 + W_ND)
#define SC_SIG 6
#define SC_POLL 21
#define LAS __attribute__((address_space(3)))
#define PIN14(a, o) asm volatile("" : "+v"(a[o]), "+v"(a[o + 1]), "+v"(a[o + 2]), "+v"(a[o + 3]), "+v"(a[o + 4]), "+v"(a[o + 5]), "+v"(a[o + 6]), "+v"(a[o + 7]), \
    "+v"(a[o + 8]), "+v"(a[o + 9]), "+v"(a[o + 10]), "+v"(a[o + 11]), "+v"(a[o + 12]), "+v"(a[o + 13]) :: "memory")

constexpr int g1_e0(int t) { return t == 1 ? EPI0 : 3; }
constexpr int g1_st(int t) { return g1_e0(t) + 19; }
constexpr int g1_an(int t) { return t == 2 ? 18 : 14; }
constexpr int g1_w2(int n) {
  if (n >= 25 && n < 25 + 14 * 4 && (n - 25) % 4 == 0) return (n - 25) / 4;
  if (n >= 84 && n < 84 + 23) { const int q = n - 84; if (q % 3 != 2) return 14 + q - q / 3 < 28 ? 14 + q - q / 3 : -1; }
  return -1;
}
constexpr int g1_sg(int t) { return t == 0 ? 24 : 28 * t + 10; }
constexpr int g1_vm(int a, int b) {
  int c = 0;
  for (int n = a; n < b; ++n) {
    const int t = n / 28, q = n % 28;
    if (n > a) { if (t >= 2 && q == g1_an(t)) ++c; if (n == 108) ++c; if (n == g1_sg(t)) ++c; }
    if (t >= 1 && q == g1_st(t) && n != a) ++c;
    if (g1_w2(n) >= 0) ++c;
  }
  return c;
}
__global__ __launch_bounds__(512) void kf_main(const u16* __restrict__ x, const u16* __restrict__ logA, const u16* __restrict__ Aim,
                                               const u16* __restrict__ Cr, const u16* __restrict__ Ci, const u16* __restrict__ logdt,
                                               const u16* __restrict__ Dsk, const u16* __restrict__ w1, const u16* __restrict__ w3,
                                               const u16* __restrict__ w2, const u16* __restrict__ cvec, const u16* __restrict__ wmod,
                                               char* __restrict__ ws, u16* __restrict__ out, unsigned long long* stamps) {
  __shared__ __attribute__((aligned(16))) char smem[KF_LDS];
  const int tid = __builtin_amdgcn_workitem_id_x(), lane = tid & 63;
  const int w = __builtin_amdgcn_readfirstlane(tid >> 6);
  const int b = __builtin_amdgcn_workgroup_id_x(), xcd = b & 7, idx = b >> 3;
  const int hi = lane >> 5;
  const int G = lane >> 4, qq = (lane & 15) >> 2, pp = lane & 3;
  const u32 voff = lane * 16;
  GAS u32* ctl = (GAS u32*)ws;
  auto rs = __builtin_amdgcn_make_buffer_rsrc(ws, 0, (int)WS_END, 0x00020000);
  const u32 lds0 = (u32)(size_t)LDSP(char, smem);
  STAMP_DECL;
  STAMP(0);
  u32 pl0 = 0, pl1 = 0, pl2 = 0;
  u32x4 cpf;
  { const char* pc; asm volatile("s_getpc_b64 %0" : "=s"(pc));
    const u32 o = (idx < CODEPF ? idx : 0) * 1024 + lane * 16;
    if (w == 7) asm volatile("global_load_dwordx4 %0, %1, %2" : "=v"(cpf) : "v"(o), "s"(pc) : "memory"); }
  const bool mainwg = b < 224;
  const int dl = w >> 2, q4 = w & 3, t256 = tid & 255;
  auto arrive0 = [&]() __attribute__((always_inline)) {
    u32 old;
    asm volatile("s_waitcnt vmcnt(0)\n\ts_mov_b64 exec, 1\n\tds_add_rtn_u32 %0, %1, %2\n\ts_mov_b64 exec, -1\n\ts_waitcnt lgkmcnt(0)" : "=&v"(old) : "v"(lds0 + ARR_CNT), "v"(1) : "memory");
    if (__builtin_amdgcn_readfirstlane(old) == (mainwg ? 4u : (b < 249 ? 8u : 0u)) && lane < 16) __hip_atomic_fetch_add(ctl + C0_CNT + (lane * 8 + xcd) * 32, 1u, RLX_AGENT);
  };
  GAS u32* c0p = ctl + C0_CNT + ((b & 15) * 8 + lane) * 32;
  int kg_wb, kg_wst;
  u32 kga[12];
  u32 dskr;
  unsigned long long epoch;
  {
    u32 lar = 0, air = 0, crr = 0, cir = 0, ldt = 0;
    if (mainwg) {
      const int d = 2 * b + dl;
      asm volatile("global_load_ushort %0, %5, %6 nt\n\tglobal_load_ushort %1, %5, %7 nt\n\tglobal_load_ushort %4, %10, %11\n\tglobal_load_ushort %2, %5, %8 nt\n\tglobal_load_ushort %3, %5, %9 nt"
                   : "+v"(lar), "+v"(air), "+v"(crr), "+v"(cir), "+v"(ldt) : "v"((d * N_ + t256) * 2), "s"(logA), "s"(Aim), "s"(Cr), "s"(Ci), "v"(d * 2), "s"(logdt) : "memory");
    }
    unsigned long long ep = 0;
    u32 pe = 32u;
    const int task = b >= 249 ? 2 * b - 364 + dl : (dl || b >= 246 ? -1 : (b < 134 ? b : b + 14));
    char* stg = smem + (dl ? 0 : TASK_LDS);
    u32x4 v[7]; u32 cv[7];
    auto taskload = [&]() __attribute__((always_inline)) {
      const u16* src; u32 step;
      if (task < 64) { src = x + (size_t)(task * 32 + (t256 >> 3)) * D_ + (t256 & 7) * 8; step = 128; }
      else { const int m = task - 64; src = wmod + (size_t)(224 * (m & 1) + (t256 >> 3)) * 2688 + (m >> 1) * 64 + (t256 & 7) * 8; step = 64 * 2688; }
#pragma unroll
      for (int i = 0; i < 7; ++i) asm volatile("global_load_dwordx4 %0, %1, off nt" : "=v"(v[i]) : "v"((const char*)src + (size_t)i * step) : "memory");
      const u16* cp = cvec + 224 * (task & 1) + (t256 >> 3);
      static_for<0, 7>([&](auto ic) __attribute__((always_inline)) { constexpr int i = decltype(ic)::value; cv[i] = ldu16<64 * i>(cp); });
    };
    if (task >= 0 && task < 134) {
      taskload();
    } else if (task >= 148) {
      const int m = task - 148;
      const u16* src = ((t256 & 128) ? w3 : w1) + (size_t)(8 * (m >> 1) + ((t256 >> 4) & 7)) * F_ + (m & 1) * 896 + (t256 & 15) * 8;
      static_for<0, 7>([&](auto ic) __attribute__((always_inline)) { constexpr int i = decltype(ic)::value; v[i] = ldnt<256 * i>(src); });
    } else if (task < 0 && !mainwg) {
      if (dl) ST16((u32x4{0, 0, 0, 0}), ZERO_LO + (b - 224) * 4096 + t256 * 16);
      if (b == 248 && w == 0) {
        const unsigned long long e0 = __hip_atomic_load((GAS unsigned long long*)(ctl + EP_E), RLX_AGENT);
#pragma unroll
        for (int i = 0; i < 2; ++i) __hip_atomic_store(ctl + C0_CNT + i * 2048 + lane * 32, 0u, RLX_AGENT);
        WAIT_VM0();
        asm volatile("s_waitcnt vmcnt(0)" ::: "memory");
        if (lane < 16) __hip_atomic_store((GAS unsigned long long*)(ctl + EP_R + lane * 32), e0 + 1, RLX_AGENT);
      }
    }
    SB();
    dskr = *(const u32*)(Dsk + 2 * (mainwg ? b : 0));
    asm volatile("" :: "s"(dskr));
    if (tid < 4) *(u32*)(smem + ARR_CNT + tid * 4) = 0;
    asm volatile("s_waitcnt lgkmcnt(0)\n\ts_barrier" ::: "memory");
    SB();
    auto meet = [&](u32 want) __attribute__((always_inline)) {
      asm volatile("s_waitcnt lgkmcnt(0)\n\ts_mov_b64 exec, 1\n\tds_add_u32 %0, %1 offset:4\n\ts_mov_b64 exec, -1" :: "v"(lds0 + ARR_CNT + dl * 4), "v"(1) : "memory");
#pragma unroll 1
      for (unsigned spins = 0; spins < 16 * SPIN_CAP; ++spins) {
        if (*(volatile LAS u32*)(size_t)(lds0 + ARR_CNT + 4 + dl * 4) >= want) break;
        __builtin_amdgcn_s_sleep(1);
      }
      asm volatile("" ::: "memory");
    };
#pragma unroll
    for (int h = 0; h < 2; ++h) {
      const int lb = 2040 - 8 * t256 - 8 * h;
      const int kr = lb < 256 ? 0 : 24 - __builtin_clz(lb);
      const float mB = U2F((u32)(150 + kr) << 23) + (float)(lb < 0 ? 704 : lb);
      const u32 kpa = lds0 + (u32)(KG_TAB + dl * 1536) + 256u * kr - ((u32)(150 + kr) << 24);
#pragma unroll
      for (int e = 0; e < (h ? 4 : 8); ++e) { kga[8 * h + e] = (F2U(mB + (float)(7 - e)) << 1) + kpa; asm volatile("" : "+v"(kga[8 * h + e])); }
    }
    if (t256 == 0) *(u32x4*)(smem + KG_TAB + dl * 1536 + 1408) = u32x4{0, 0, 0, 0};
    {
      char* cp = smem + dl * CV_DSTR;
      if (mainwg) {
        if (t256 < 32) *(u32x4*)(cp + 4 * 4160 + t256 * 16) = u32x4{0, 0, 0, 0};
        else if (t256 < 40) *(u32x4*)(cp + ((t256 - 32) >> 1) * 4160 + 4096 + (t256 & 1) * 16) = u32x4{0, 0, 0, 0};
      }
    }
    const int s = lane & 31;
    {
      const int kw = s < 8 ? 0 : (s < 20 ? (s >> 2) - 1 : 3);
      int wb = s < 12 ? (s < 8 ? s : 248 + s) : (s < 16 ? 384 + ((s & 2) << 5) + (s & 1) : 32 * s);
      kg_wst = 16 >> kw;
      kg_wb = (wb > 672 ? 672 : wb) * 2 + KG_TAB + dl * 1536 + (8 * q4 + 4 * hi) * kg_wst;
    }
    char* reg = smem + KG_IMG + w * KG_REG;
    f16x8 bfr[8];
    f32x2 z8, z16; float scl = 0.f, fs = 0.f;
    if (mainwg) {
      if (task < 0) asm volatile("s_waitcnt vmcnt(0)" ::: "memory"); else if (task < 148) asm volatile("s_waitcnt vmcnt(14)" ::: "memory"); else asm volatile("s_waitcnt vmcnt(7)" ::: "memory");
      asm volatile("" : "+v"(lar), "+v"(air), "+v"(crr), "+v"(cir), "+v"(ldt) :: "memory");
    }
    if (mainwg) {
      const float dt = rbf(fexp(bflo(ldt)));
      const float ar = -rbf(fexp(bflo(lar))) * dt * 1.4426950408889634f;
      const float ai = bflo(air) * dt;
      const float I2PH = 0.15915494f, I2PL = 6.4206382e-9f;
      const float th = ai * I2PH;
      const float tl = __builtin_fmaf(ai, I2PL, __builtin_fmaf(ai, I2PH, -th));
      auto zpow = [&](float sg) __attribute__((always_inline)) {
        float e = __builtin_amdgcn_exp2f(ar * sg);
        float rev = __builtin_fmaf(tl, sg, __builtin_amdgcn_fractf(th * sg));
        return f32x2{e * __builtin_amdgcn_cosf(rev), e * __builtin_amdgcn_sinf(rev)};
      };
      const f32x2 z1 = zpow(1.f), z256 = zpow(256.f);
      z16 = zpow(16.f);
      const f32x2 s1 = {-z1.y, z1.y}, s256 = {-z256.y, z256.y};
      u32 eb = ((crr & 0x7f80u) > (cir & 0x7f80u) ? (crr & 0x7f80u) : (cir & 0x7f80u));
      asm("s_nop 1\n\tv_max_u32_dpp %0, %0, %0 row_ror:8 row_mask:0xf bank_mask:0xf\n\ts_nop 1\n\tv_max_u32_dpp %0, %0, %0 row_ror:4 row_mask:0xf bank_mask:0xf\n\ts_nop 1\n\t"
          "v_max_u32_dpp %0, %0, %0 row_ror:2 row_mask:0xf bank_mask:0xf\n\ts_nop 1\n\tv_max_u32_dpp %0, %0, %0 row_ror:1 row_mask:0xf bank_mask:0xf\n\ts_nop 1" : "+v"(eb));
      {
        const u32 e0 = __builtin_amdgcn_readlane(eb, 0), e1 = __builtin_amdgcn_readlane(eb, 16), e2 = __builtin_amdgcn_readlane(eb, 32), e3 = __builtin_amdgcn_readlane(eb, 48);
        const u32 m0 = e0 > e1 ? e0 : e1, m1 = e2 > e3 ? e2 : e3;
        eb = (m0 > m1 ? m0 : m1) >> 7;
      }
      eb = eb < 1u ? 1u : (eb > 252u ? 252u : eb);
      scl = U2F((253u - eb) << 23);
      fs = 2.f * dt * U2F((eb + 1u) << 23);
      f32x2 lo[9], hp[9];
      lo[1] = z1; hp[1] = z256;
#pragma unroll
      for (int i = 2; i <= 8; ++i) { lo[i] = cmulp(lo[i - 1], z1, s1); hp[i] = cmulp(hp[i - 1], z256, s256); }
      const f32x2 a2 = cmulp(lo[2], z256, s256), a4 = cmulp(lo[4], z256, s256), a6 = cmulp(lo[6], z256, s256);
      const f32x2 b4 = cmulp(a4, z256, s256), c4 = cmulp(b4, z256, s256);
      const u32 col[21] = {0x00003c00u, pkh(lo[1]), pkh(lo[2]), pkh(lo[3]), pkh(lo[4]), pkh(lo[5]), pkh(lo[6]), pkh(lo[7]),
                           pkh(hp[1]), pkh(a2), pkh(a4), pkh(a6), pkh(hp[2]), pkh(b4), pkh(hp[3]), pkh(c4),
                           pkh(hp[4]), pkh(hp[5]), pkh(hp[6]), pkh(hp[7]), pkh(hp[8])};
#pragma unroll
      for (int i = 0; i < 21; ++i) *(u32*)(reg + i * 272 + lane * 4) = col[i];
      z8 = lo[8];
#pragma unroll
      for (int i = 0; i < 8; ++i) bfr[i] = *(const f16x8*)(reg + s * 272 + i * 32 + hi * 16);
      asm volatile("s_waitcnt lgkmcnt(0)" : "+v"(bfr[0]), "+v"(bfr[1]), "+v"(bfr[2]), "+v"(bfr[3]), "+v"(bfr[4]), "+v"(bfr[5]), "+v"(bfr[6]), "+v"(bfr[7]) :: "memory");
    }
    if (!mainwg) asm volatile("s_waitcnt vmcnt(0)" ::: "memory");
    asm volatile("" :: "v"(cpf));
    STAMP(8);
    unsigned long long tagr = 0;
    GAS u32* tagp = ctl + EP_R + (b & 15) * 32;
#define EP_ISSUE() asm volatile("s_mov_b64 exec, 1\n\tglobal_load_dwordx2 %1, %4, off sc1\n\tglobal_atomic_or_x2 %0, %2, %3, off sc0\n\ts_mov_b64 exec, -1" \
                     : "+&v"(tagr), "+&v"(ep) : "v"(tagp), "v"(0ull), "v"(ctl + EP_E) : "memory")
    auto epcheck = [&]() __attribute__((always_inline)) {
#pragma unroll 1
      for (unsigned spins = 0; spins < SPIN_CAP; ++spins) {
        asm volatile("s_waitcnt vmcnt(0)" : "+v"(tagr), "+v"(ep) :: "memory");
        if ((__builtin_amdgcn_ballot_w64(tagr != ep + 1) & 1) == 0) break;
        EP_ISSUE();
      }
      arrive0();
    };
    if (w == 4) { EP_ISSUE(); if (b >= 249) epcheck(); }
    if (b >= 249) taskload();
#define PIN_V() asm volatile("s_waitcnt vmcnt(0)" : "+v"(v[0]), "+v"(v[1]), "+v"(v[2]), "+v"(v[3]), "+v"(v[4]), "+v"(v[5]), "+v"(v[6]) :: "memory")
    if (task >= 0 && task < 148) {
      PIN_V();
      if (task < 64) {
            const int row = t256 >> 3, p = t256 & 7;
            float sm = 0.f, sq = 0.f;
#pragma unroll
            for (int i = 0; i < 7; ++i) {
              pin(v[i]);
              *(u32x4*)(stg + row * 928 + (p + 8 * i) * 16) = v[i];
#pragma unroll
              for (int j = 0; j < 4; ++j) { sm = dot2(v[i][j], 0x3f803f80u, sm); sq = dot2(v[i][j], v[i][j], sq); }
            }
            sm += dppf<0xB1>(sm); sq += dppf<0xB1>(sq); sm += dppf<0x4E>(sm); sq += dppf<0x4E>(sq); sm += dppf<0x141>(sm); sq += dppf<0x141>(sq);
            const float m = sm * (1.f / D_);
            const float var = __builtin_fmaxf(sq * (1.f / D_) - m * m, 0.f);
            const float sd = rbf(__builtin_amdgcn_sqrtf(rbf(rbf(var) + 1e-5f)));
            const float rsd = __builtin_amdgcn_rcpf(sd);
            if (p < 2) __builtin_amdgcn_raw_buffer_store_b32(F2U(p ? -rbf(m) * rsd : rsd), rs, (int)WS_ST1 + p * 8192 + (task * 32 + row) * 4, 0, 16);
        meet(4u);
        const char* rb = stg + (8 * G + qq) * 928 + q4 * 224 + pp * 8;
#pragma unroll
        for (int i = 0; i < 7; ++i) {
          const u32x2 lo = trread(rb + i * 32), hh = trread(rb + i * 32 + 4 * 928);
          ST16((u32x4{lo.x, lo.y, hh.x, hh.y}), (int)WS_XT + task * 28672 + q4 * 7168 + (lane & 15) * 64 + G * 16 + i * 1024);
        }
      } else {
            float acc[8] = {0, 0, 0, 0, 0, 0, 0, 0};
#pragma unroll
            for (int i = 0; i < 7; ++i) {
              asm volatile("" : "+v"(cv[i]));
              const u32 sh = F2U(silu_bf(bflo(cv[i]))), sl = sh >> 16;
#pragma unroll
              for (int j = 0; j < 4; ++j) { acc[2 * j] = dot2(v[i][j], sl, acc[2 * j]); acc[2 * j + 1] = dot2(v[i][j], sh, acc[2 * j + 1]); }
            }
#pragma unroll
            for (int j = 0; j < 8; ++j) { acc[j] += dppf<0x128>(acc[j]); acc[j] = add32(acc[j]); }
            if ((lane & 40) == 0) {
              float* red = (float*)stg + (q4 * 2 + (lane >> 4)) * 64 + (lane & 7) * 8;
              *(f32x4*)red = f32x4{acc[0], acc[1], acc[2], acc[3]};
              *(f32x4*)(red + 4) = f32x4{acc[4], acc[5], acc[6], acc[7]};
            }
        meet(4u);
            if (q4 == 0) {
              const int m = task - 64, cg = m >> 1;
              float t = 0.f;
#pragma unroll
              for (int j = 0; j < 8; ++j) t += ((const float*)stg)[j * 64 + lane];
              const int sec = cg / 7, dd = (cg % 7) * 64 + lane;
              const int o1 = sec == 3 ? (int)WS_MODB + dd * 16 : (sec == 5 ? (int)WS_MODC + dd * 8 : (int)WS_MODA + dd * 32 + (sec == 4 ? 3 : sec) * 8);
              __builtin_amdgcn_raw_buffer_store_b32(F2U(t), rs, o1 + (m & 1) * 4, 0, 16);
              if (sec == 4) __builtin_amdgcn_raw_buffer_store_b32(F2U(t), rs, (int)WS_MODB + dd * 16 + 8 + (m & 1) * 4, 0, 16);
            }
      }
    }
    if (task >= 148) {
      PIN_V();
#pragma unroll
      for (int i = 0; i < 7; ++i) *(u32x4*)(stg + (t256 >> 4) * 1824 + ((t256 & 15) + 16 * i) * 16) = v[i];
      meet(4u);
      const int m = task - 148;
      const char* rb = stg + (8 * (G & 1) + qq) * 1824 + (14 * q4 + hi) * 32 + (8 * (pp & 1) + 4 * (pp >> 1)) * 2;
      const int dst = (int)WS_W13 + ((56 * (m & 1) + 14 * q4 + hi) * 28 + (m >> 2)) * 1024 + ((m >> 1) & 1) * 512 + (lane & 31) * 16;
#pragma unroll
      for (int i = 0; i < 7; ++i) {
        const u32x2 lo = trread(rb + i * 64), hh = trread(rb + i * 64 + 4 * 1824);
        ST16((u32x4{lo.x, lo.y, hh.x, hh.y}), dst + i * 57344);
      }
    }
    if (mainwg) {
        pinf(scl);
        f32x2 ra = {bflo(crr) * scl, -bflo(cir) * scl};
        f32x2 rb = cmulp(ra, z8, f32x2{z8.y, -z8.y});
        const f32x2 s16c = {z16.y, -z16.y};
#pragma unroll
        for (int r = 0; r < 32; r += 2) {
          *(u32*)(reg + r * 272 + lane * 4) = pkh(ra); *(u32*)(reg + (r + 1) * 272 + lane * 4) = pkh(rb);
          if (r < 30) { ra = cmulp(ra, z16, s16c); rb = cmulp(rb, z16, s16c); }
        }
        if (task >= 0) { WAIT_VM0(); arrive0(); STAMP(11); }
        if (w == 4) epcheck();
        f32x16 acc = {};
#pragma unroll
        for (int i = 0; i < 8; ++i) {
          const f16x8 af = *(const f16x8*)(reg + s * 272 + i * 32 + hi * 16);
          acc = __builtin_amdgcn_mfma_f32_32x32x16_f16(af, bfr[i], acc, 0, 0, 0);
        }
#pragma unroll
        for (int qd = 0; qd < 4; ++qd)
          *(f32x4*)(reg + qd * 1024 + lane * 16) = f32x4{acc[4 * qd] * fs, acc[4 * qd + 1] * fs, acc[4 * qd + 2] * fs, acc[4 * qd + 3] * fs};
    }
    if (mainwg) {
      if (w == 4) POLL_ISSUE(pe, c0p, 255);
      meet(dl ? 4u : 8u);
    {
      const char* rp = smem + KG_IMG + dl * 4 * KG_REG + q4 * 1024 + lane * 16;
      const f32x4 t = (*(const f32x4*)rp + *(const f32x4*)(rp + KG_REG)) + (*(const f32x4*)(rp + 2 * KG_REG) + *(const f32x4*)(rp + 3 * KG_REG));
      const u32 ta = pkbf(t[0], t[1]), tc = pkbf(t[2], t[3]);
      char* q = smem + kg_wb;
      *(u16*)q = (u16)ta; *(u16*)(q + kg_wst) = (u16)(ta >> 16); *(u16*)(q + 2 * kg_wst) = (u16)tc; *(u16*)(q + 3 * kg_wst) = (u16)(tc >> 16);
    }
    }
    if (!mainwg && b < 249) { WAIT_VM0(); arrive0(); }
    if (w == 4) {
      asm volatile("s_waitcnt vmcnt(0)" : "+v"(pe) :: "memory");
      if (mainwg) { if (__builtin_amdgcn_ballot_w64(pe < 32u) != 0) wait_cnt<8, 6>(c0p, 32u, pl0, pl1, pl2); }
      else if (b < 249) epcheck();
    }
    STAMP(14);
    epoch = ep + 1;
    asm volatile("s_waitcnt lgkmcnt(0)\n\ts_barrier" ::: "memory");
  }
  if (!mainwg) {
    const int fr = b - 224;
    if (fr < 28) {
      u32x4 v[7];
#pragma unroll
      for (int i = 0; i < 7; ++i) v[i] = LDIN((const u32x4*)(w2 + (size_t)(64 * fr + (tid >> 3)) * D_ + ((tid & 7) + 8 * i) * 8));
#pragma unroll
      for (int i = 0; i < 7; ++i) *(u32x4*)(smem + (tid >> 3) * 912 + ((tid & 7) + 8 * i) * 16) = v[i];
      asm volatile("s_waitcnt lgkmcnt(0)\n\ts_barrier" ::: "memory");
      const char* rbase = smem + (16 * (w & 3) + 8 * hi + qq) * 912 + (w >> 2) * 448 + (16 * (G & 1) + 4 * pp) * 2;
      const int dst = (int)WS_W2F + ((7 * (w >> 2)) * 112 + 4 * fr + (w & 3)) * 1024 + lane * 16;
#pragma unroll
      for (int s = 0; s < 7; ++s) {
        u32x2 lo = trread(rbase + s * 64), hh = trread(rbase + s * 64 + 4 * 912);
        __builtin_amdgcn_raw_buffer_store_b128(u32x4{lo.x, lo.y, hh.x, hh.y}, rs, dst + s * 112 * 1024, 0, 16);
      }
    }
    asm volatile("s_waitcnt vmcnt(0)\n\ts_barrier" ::: "memory");
    if (w == 4) {
      wait_cnt<8, 6>(c0p, 32u, pl0, pl1, pl2);
      asm volatile("s_waitcnt vmcnt(0)" ::: "memory");
      POLL_KEEP();
      if (b == 248 && lane == 0) __hip_atomic_store((GAS unsigned long long*)(ctl + EP_E), epoch, RLX_AGENT);
      if (lane < 16) __hip_atomic_fetch_add(ctl + C1_CNT + (lane * 9 + 8) * 32, 1u, RLX_AGENT);
      wait_cnt<8, 6>(ctl + C1_CNT + ((b & 15) * 9 + lane) * 32, 28u, pl0, pl1, pl2);
      asm volatile("s_waitcnt vmcnt(0)" ::: "memory");
      POLL_KEEP();
    }
    asm volatile("s_barrier" ::: "memory");
    {
      const char* xsrc = ws + WS_X1T + (size_t)(2 * fr + (w >> 2)) * 28672 + (w & 3) * 1024;
#pragma unroll
      for (int i = 0; i < 7; ++i) glds16(voff, xsrc + i * 4096, lds0 + w * 7168 + i * 1024);
      const char* sp = smem + w * 7168 + (8 * hi + qq) * 64 + (G & 1) * 32 + pp * 8;
      float s0 = 0.f, q0 = 0.f, s1 = 0.f, q1 = 0.f;
      static_for<0, 7>([&](auto ic) __attribute__((always_inline)) {
        constexpr int i = decltype(ic)::value;
        asm volatile("s_waitcnt vmcnt(%0)" :: "i"(6 - i) : "memory");
        const u32x2 lo = trread(sp + i * 1024), hh = trread(sp + i * 1024 + 256);
        s0 = dot2(lo.x, 0x3f803f80u, s0); q0 = dot2(lo.x, lo.x, q0); s1 = dot2(lo.y, 0x3f803f80u, s1); q1 = dot2(lo.y, lo.y, q1);
        s0 = dot2(hh.x, 0x3f803f80u, s0); q0 = dot2(hh.x, hh.x, q0); s1 = dot2(hh.y, 0x3f803f80u, s1); q1 = dot2(hh.y, hh.y, q1);
      });
      auto r = __builtin_amdgcn_permlane32_swap(F2U(s0 + s1), F2U(q0 + q1), false, false);
      *(float*)(smem + 65536 + w * 256 + (lane & 31) * 8 + hi * 4) = U2F(r[0]) + U2F(r[1]);
      asm volatile("s_waitcnt lgkmcnt(0)\n\ts_barrier" ::: "memory");
      if (w == 0) {
        const char* pt = smem + 65536 + hi * 1024 + (lane & 31) * 8;
        const f32x2 ts = (*(const f32x2*)pt + *(const f32x2*)(pt + 256)) + (*(const f32x2*)(pt + 512) + *(const f32x2*)(pt + 768));
        const float m = ts[0] * (1.f / D_);
        const float var = __builtin_fmaxf(ts[1] * (1.f / D_) - m * m, 0.f);
        const float sd = __builtin_amdgcn_sqrtf(rbf(rbf(var) + 1e-5f));
        __hip_atomic_store((GAS unsigned long long*)(ws + ST2_OFF) + 64 * fr + lane, (1ull << 32) | pkbf(-m, sd), RLX_AGENT);
      }
    }
    return;
  }
  const int rt = 4 * (xcd >> 1) + idx / 7, ct = 7 * (xcd & 1) + idx % 7;
  const int ft = 8 * ct + w;
  const int r2 = 4 * xcd + idx / 7, dg = idx % 7, kq = w >> 1, dt = w & 1;
  const int te = 4 * (r2 >> 1) + (r2 & 1), tl = 4 * (r2 >> 1) + 3 - (r2 & 1);
  const int wv = (int)WS_W13 + ft * 28672 + lane * 16;
#define WLOAD(p) wf[p] = __builtin_amdgcn_raw_buffer_load_b128(rs, wv + ((p) & 3) * 1024, ((p) & ~3) * 1024, 16)

  u32x4 wf[28];
  u32x4 wex;
  const int hbase = (int)WS_HF + (4 * rt * 112 + ft) * 1024 + lane * 16;

  {
    const int a = lane & 15;
    const int l0 = 256 * w + 16 * a + 4 * G;
    const int xoff = ((l0 >> 5) * 448 + 2 * b) * 64 + (l0 & 31) * 2;
    STAMP(9);
    u32x2 xx[2]; u32x4 st[2];
    float msv = U2F(__builtin_amdgcn_raw_buffer_load_b32(rs, (int)WS_MODA + (lane & 15) * 4, b * 64, 16));
#pragma unroll
    for (int i = 0; i < 2; ++i) {
      xx[i] = __builtin_amdgcn_raw_buffer_load_b64(rs, (int)WS_XT + xoff + i * 64, 0, 16);
      st[i] = __builtin_amdgcn_raw_buffer_load_b128(rs, (int)WS_ST1 + l0 * 4, i * 8192, 16);
    }
    SB();
    {
      u32 kw[6];
#pragma unroll
      for (int e = 0; e < 6; ++e) kw[e] = (u32)*(const LAS u16*)(size_t)kga[2 * e] | ((u32)*(const LAS u16*)(size_t)kga[2 * e + 1] << 16);
      char* cp = smem + dl * CV_DSTR;
      u32 al[5];
#pragma unroll
      for (int i = 0; i < 5; ++i) al[i] = __builtin_amdgcn_alignbit(kw[i + 1], kw[i], 16);
      char* q = cp + t256 * 16;
      *(u32x4*)(q) = u32x4{kw[0], kw[1], kw[2], kw[3]};
      *(u32x4*)(q + 4160) = u32x4{al[0], al[1], al[2], al[3]};
      *(u32x4*)(q + 8320) = u32x4{kw[1], kw[2], kw[3], kw[4]};
      *(u32x4*)(q + 12480) = u32x4{al[1], al[2], al[3], al[4]};
    }
    SB();
    const f32x4 tb = __builtin_bit_cast(f32x4, __builtin_amdgcn_raw_buffer_load_b128(rs, (int)WS_MODB + (tid < 448 ? tid : 447) * 16, 0, 16));
    SB();
#pragma unroll
    for (int i = 0; i < W_N1; ++i) WLOAD(i);
    SB();
    msv = rbf(msv + dppf<0xB1>(msv));
    if (lane & 2) msv = rbf(1.f + msv);
#define MSV(i) U2F(__builtin_amdgcn_readlane(F2U(msv), i))
    float ga[2], b2v[2];
    const int swl = (lane ^ ((lane >> 5) << 3)) * 16;
#pragma unroll
    for (int i = 0; i < 2; ++i) {
      const float sa = MSV(8 * i), beta = MSV(8 * i + 2);
      ga[i] = MSV(8 * i + 4); b2v[i] = MSV(8 * i + 6);
      const float gd = ga[i] * (i ? bfhi(dskr) : bflo(dskr));
      const float xv[4] = {bflo(xx[i].x), bfhi(xx[i].x), bflo(xx[i].y), bfhi(xx[i].y)};
      float hv[4];
#pragma unroll
      for (int e = 0; e < 4; ++e)
        hv[e] = __builtin_fmaf(__builtin_fmaf(xv[e], U2F(st[0][e]), U2F(st[1][e])), beta, sa);
      *(u32x2*)(smem + i * CV_DSTR + 4 * 4160 + 512 + l0 * 2) = u32x2{pkbf(hv[0], hv[1]), pkbf(hv[2], hv[3])};
      f32x4 base;
#pragma unroll
      for (int e = 0; e < 4; ++e) base[e] = __builtin_fmaf(hv[e], gd, xv[e]);
      *(f32x4*)(smem + CV_RED + (i * 40 + w * 5 + 4) * 1024 + swl) = base;
    }
    SB();
    POLL_KEEP();
    asm volatile("s_waitcnt lgkmcnt(0)\n\ts_barrier" ::: "memory");
    STAMP(1);
    f32x4 acc[8];
#pragma unroll
    for (int t = 0; t < 8; ++t) acc[t] = f32x4{0, 0, 0, 0};
    const int im = 15 - a;
    const u32 abase = lds0 + dl * CV_DSTR + (im & 3) * 4160 + (2016 + 8 * G + 4 * (im >> 2)) * 2 - q4 * 128 - 3648;
    const u32 bbase = lds0 + dl * CV_DSTR + 4 * 4160 + (256 - 16 + 16 * a + 8 * G) * 2 - q4 * 128 - 64;
    char* dump = smem + CV_RED + (dl * 40 + q4) * 1024 + swl;
#pragma unroll
    for (int cc = 0; cc < 2; ++cc) {
      u32x2 alo[8], ahi[8]; u32x4 bfr[8];
#pragma unroll
      for (int c1 = 0; c1 < 8; ++c1)
        asm volatile("ds_read_b64 %0, %3 offset:%5\n\tds_read_b64 %1, %3 offset:%6\n\tds_read_b128 %2, %4 offset:%7"
                     : "=&v"(alo[c1]), "=&v"(ahi[c1]), "=&v"(bfr[c1])
                     : "v"(abase), "v"(bbase), "i"(3648 - (8 * c1 + cc) * 64), "i"(3656 - (8 * c1 + cc) * 64), "i"(64 + (8 * c1 - cc) * 64) : "memory");
      SB();
      if (cc == 0) __builtin_amdgcn_s_setprio(1); else __builtin_amdgcn_s_setprio(0);
#pragma unroll
      for (int t = 0; t < 8; ++t) {
        if (t == 0) asm volatile("s_waitcnt lgkmcnt(15)" : "+v"(alo[0]), "+v"(ahi[0]), "+v"(bfr[0]), "+v"(alo[1]), "+v"(ahi[1]), "+v"(bfr[1]), "+v"(alo[2]), "+v"(ahi[2]), "+v"(bfr[2]));
        if (t == 3) asm volatile("s_waitcnt lgkmcnt(9)" : "+v"(alo[3]), "+v"(ahi[3]), "+v"(bfr[3]), "+v"(alo[4]), "+v"(ahi[4]), "+v"(bfr[4]));
        if (t == 5) asm volatile("s_waitcnt lgkmcnt(0)" : "+v"(alo[5]), "+v"(ahi[5]), "+v"(bfr[5]), "+v"(alo[6]), "+v"(ahi[6]), "+v"(bfr[6]), "+v"(alo[7]), "+v"(ahi[7]), "+v"(bfr[7]));
#pragma unroll
        for (int c1 = 0; c1 <= t; ++c1) {
          acc[t] = __builtin_amdgcn_mfma_f32_16x16x32_bf16(__builtin_bit_cast(bf16x8, u32x4{alo[c1].x, alo[c1].y, ahi[c1].x, ahi[c1].y}),
                                                           __builtin_bit_cast(bf16x8, bfr[t - c1]), acc[t], 0, 0, 0);
          const int n = t * (t + 1) / 2 + c1;
          if ((n % W_ST) == W_ST - 1 && W_N1 + (36 / W_ST) * cc + n / W_ST < W_N2) { SB(); WLOAD(W_N1 + (36 / W_ST) * cc + n / W_ST); SB(); }
          if (cc == 1 && t >= 5 && c1 >= 1) {
            const int u = (t == 5 ? c1 - 1 : (t == 6 ? (c1 == 1 ? 5 : -1) : (c1 == 1 ? 6 : -1)));
            if (u >= 0) { SB(); *(f32x4*)(dump + u * 5120) = acc[u]; SB(); }
          }
        }
        SB();
      }
    }
    *(f32x4*)(dump + 7 * 5120) = acc[7];
    STAMP(2);
    if (tid < 448) {
      const float be = rbf(1.f + rbf(tb[2] + tb[3]));
      *(u16*)(smem + TAB_SB + tid * 2) = (u16)(F2U(rbf(tb[0] + tb[1])) >> 16);
      *(u16*)(smem + TAB_SB + 896 + tid * 2) = (u16)(F2U(be) >> 16);
    }
    asm volatile("s_waitcnt lgkmcnt(0)\n\ts_barrier" ::: "memory");
    {
      const int ch = (lane >> 2) & 1;
      const float gac = ch ? ga[1] : ga[0];
      const float b2 = ch ? b2v[1] : b2v[0];
      const char* rp = smem + CV_RED + (ch * 40 + w * 5) * 1024 + (lane & 1) * 512 + ((2 * (lane >> 3) + ((lane >> 1) & 1)) ^ ((lane & 1) << 3)) * 16;
      u32x4 o, ob;
#pragma unroll
      for (int i = 0; i < 2; ++i) {
        const char* q = rp + i * 256;
        const f32x4 y = (*(const f32x4*)q + *(const f32x4*)(q + 1024)) + (*(const f32x4*)(q + 2048) + *(const f32x4*)(q + 3072));
        const f32x4 bs = *(const f32x4*)(q + 4096);
        o[2 * i] = pkbf(__builtin_fmaf(y[0], gac, bs[0]), __builtin_fmaf(y[1], gac, bs[1]));
        o[2 * i + 1] = pkbf(__builtin_fmaf(y[2], gac, bs[2]), __builtin_fmaf(y[3], gac, bs[3]));
      }
#pragma unroll
      for (int i = 0; i < 4; ++i) ob[i] = pkbf(bflo(o[i]) * b2, bfhi(o[i]) * b2);
      const int so = ((8 * w + (lane >> 3)) * 448 + 2 * b) * 64 + (lane & 7) * 16;
      __builtin_amdgcn_raw_buffer_store_b128(ob, rs, (int)WS_XB + so, 0, 16);
      __builtin_amdgcn_raw_buffer_store_b128(o, rs, (int)WS_X1T + so, 0, 16);
    }
    SB();
    STAMP(10);
  }
  u32x4 w2f[28];
  const int ke = w ^ ((dg & 1) << 2);
  const int rot = (r2 & 1) * 7168;
  const char* w2src = ws + WS_W2F + ((size_t)(2 * dg) * 112 + 14 * ke) * 1024;
  const char* wsrc = ws + WS_W13 + (size_t)ft * 28672;
#define WDEF(lo, hi_) static_for<lo, hi_>([&](auto ic) __attribute__((always_inline)) { constexpr int i = decltype(ic)::value; wf[i] = gload<(i & 3) * 1024>(voff, wsrc + (i & ~3) * 1024); })
  __builtin_amdgcn_s_waitcnt(0x0F72);
  WDEF(W_N2, W_N3);
  asm volatile("s_waitcnt vmcnt(%0)\n\ts_barrier" :: "i"(W_N3 - W_N2) : "memory");
  STAMP(3);
  if (w == 0 && lane < 16) __hip_atomic_fetch_add(ctl + C1_CNT + (lane * 9 + xcd) * 32, 1u, RLX_AGENT);
  SB();
  WDEF(W_N3, 28);
  f32x16 accw;
#pragma unroll
  for (int r = 0; r < 16; ++r) accw[r] = 0.f;
  const char* tsb = smem + TAB_SB + (G & 1) * 896 + hi * 16;
#pragma unroll
  for (int p = 0; p < W_N2; ++p) accw = MFMA32(*(const u32x4*)(tsb + p * 32), wf[p], accw);
  SB();
  if (w == 0) wait_cnt<9, 6>(ctl + C1_CNT + ((b & 15) * 9 + lane) * 32, lane == 8 ? 32u : 28u, pl0, pl1, pl2);
  asm volatile("s_barrier" ::: "memory");
  STAMP(4);
  const char* xsrc = ws + WS_XB + (size_t)(4 * rt) * 28672 + w * 1024;
#pragma unroll
  for (int i = 0; i < X_N0; ++i) glds16(voff, xsrc + i * 8192, lds0 + w * 1024 + i * 8192);
  {
    asm volatile("s_waitcnt vmcnt(%0)" :: "i"(X_N0) : "memory");
    PIN14(wf, 14);
#pragma unroll
    for (int p = W_N2; p < 28; ++p) accw = MFMA32(*(const u32x4*)(tsb + p * 32), wf[p], accw);
    const float us = accw[8], vs = accw[0];
    const float uh = rbf(us), vh = rbf(vs);
    wex = u32x4{hi ? 0u : pkbf(uh, us - uh), hi ? 0u : pkbf(vh, vs - vh), 0u, 0u};
    pin(wex);
  }
  GAS u32* c2p = ctl + C2_CNT + rt * 1024 + (ft / 14) * 32;
  GAS u32* c2e = ctl + C2_CNT + te * 256 + ke * 32;
  GAS u32* c2l = ctl + C2_CNT + tl * 256 + lane * 32;
  u32 pv = 14;
#define ANNOUNCE(n, tt) asm volatile("s_waitcnt vmcnt(%3)\n\ts_mov_b64 exec, 1\n\tglobal_atomic_add %0, %1, %2 offset:%4\n\ts_mov_b64 exec, -1" :: "v"(0), "v"(1), "s"(c2p), "i"(n), "i"((tt) * 1024) : "memory")
  {
    u32 rb = lds0 + (8 * hi + qq) * 64 + (G & 1) * 32 + pp * 8;
    GAS unsigned long long* sgp = (GAS unsigned long long*)(ws + ST2_OFF) + 128 * rt + (lane & 31);

    u32x4 xf[4];
    f32x16 acc[2];
    float nal, s2, ex[8], pr[8];
    unsigned long long sg = 0;
#define G1_READ(q) do { const LAS char* p_ = (const LAS char*)(size_t)rb + (q) * 1024; u32x2 lo = trread3(p_), hh = trread3(p_ + 256); xf[(q) & 3] = u32x4{lo.x, lo.y, hh.x, hh.y}; } while (0)
#define SG_WAIT(g, ptr) do { _Pragma("unroll 1") for (unsigned spins = 0; spins < SPIN_CAP; ++spins) { if (__builtin_amdgcn_ballot_w64((u32)((g) >> 32) != 1u) == 0) break; sgreload(g, ptr); } } while (0)
#define SG_APPLY(g, A) do { const u32 st_ = (u32)(g); const u32 mm_ = __builtin_amdgcn_perm(st_, st_, 0x01000100u), ss_ = __builtin_amdgcn_perm(st_, st_, 0x03020302u); \
      const float sd_ = bfhi(st_); nal = -1.4426950408889634f * __builtin_amdgcn_rcpf(sd_); s2 = sd_ * sd_; \
      pinf(nal); pinf(s2); A = MFMA32(wex, (u32x4{mm_, ss_, mm_, ss_}), A); } while (0)
#define EPI_STAGE(A, st, v) do { if constexpr ((st) == 0) { ex[v] = A[v] * nal; pr[v] = A[v] * A[8 + (v)]; pinf(pr[v]); } \
      if constexpr ((st) == 1) ex[v] = __builtin_amdgcn_exp2f(ex[v]); if constexpr ((st) == 2) ex[v] = __builtin_fmaf(ex[v], s2, s2); \
      if constexpr ((st) == 3) ex[v] = __builtin_amdgcn_rcpf(ex[v]); if constexpr ((st) == 4) { pr[v] *= ex[v]; pinf(pr[v]); } else pinf(ex[v]); } while (0)
#define EPI_STORE(tt) __builtin_amdgcn_raw_buffer_store_b128(u32x4{pkbf(pr[0], pr[1]), pkbf(pr[2], pr[3]), pkbf(pr[4], pr[5]), pkbf(pr[6], pr[7])}, rs, hbase, (tt) * 112 * 1024, 16)
    asm volatile("s_waitcnt vmcnt(%0)\n\ts_barrier" :: "i"(X_N0 - 4) : "memory");
    POLL_KEEP();
    STAMP(12);
    G1_READ(0); G1_READ(1);
    static_for<0, 112>([&](auto nc) __attribute__((always_inline)) {
      constexpr int n = decltype(nc)::value, t = n / 28, q = n % 28;
      constexpr int wl = g1_w2(n);
      if constexpr (t >= 1 && q == g1_e0(t) - 2) {
        asm volatile("s_waitcnt vmcnt(%1)" : "+v"(sg) : "i"(g1_vm(g1_sg(t - 1), n)) : "memory");
        SG_WAIT(sg, sgp + 32 * (t - 1));
      }
      if constexpr (n == 24) asm volatile("s_waitcnt vmcnt(0)\n\ts_barrier" ::: "memory");
      if constexpr (t >= 2 && q == g1_an(t)) ANNOUNCE(g1_vm(28 * (t - 1) + g1_st(t - 1), n), t - 2);
      if constexpr (n == 108) POLL_ISSUE(pv, c2e, 1);
      if constexpr (n == g1_sg(t)) sgload(sg, sgp + 32 * t);
      if constexpr (q == 26 && (t & 1)) { rb += 57344; asm volatile("" : "+v"(rb)); }
      SB();
      if constexpr (n < 110) G1_READ((n + 2) % 56);
      SB();
      if constexpr (q == 0) { _Pragma("unroll") for (int r = 0; r < 16; ++r) acc[t & 1][r] = 0.f; }
      acc[t & 1] = MFMA32(wf[q], xf[q & 3], acc[t & 1]);
      if constexpr (t >= 1) {
        if constexpr (q == g1_e0(t) - 2) SG_APPLY(sg, acc[(t - 1) & 1]);
        static_for<0, 5>([&](auto sc) __attribute__((always_inline)) {
          constexpr int st = decltype(sc)::value, g = q - g1_e0(t) - st, v = g / 2;
          if constexpr (g >= 0 && (g & 1) == 0 && v < 8) EPI_STAGE(acc[(t - 1) & 1], st, v);
        });
        if constexpr (q == g1_st(t)) { SB(); EPI_STORE(t - 1); }
      }
      if constexpr (n + X_N0 < 14) { SB(); glds16(voff, xsrc + (n + X_N0) * 8192, lds0 + w * 1024 + (n + X_N0) * 8192); }
      if constexpr (n + X_N0 == 14) { SB(); glds16(voff, ws + WS_X1T + ((size_t)(w >> 2 ? tl : te) * 448 + 64 * dg) * 64 + (w & 3) * 1024, lds0 + G2_X1 + w * 1024); }
      if constexpr (wl >= 0) { SB(); w2f[wl] = gload<(((wl >> 1) % 7) & 3) * 1024>(voff, w2src + (wl & 1) * 114688 + (wl >= 14 ? 7168 - rot : rot) + (((wl >> 1) % 7) >> 2) * 4096); }
    });
    SB();
    STAMP(5);
    asm volatile("s_waitcnt vmcnt(%1)" : "+v"(sg) : "i"(g1_vm(g1_sg(3), 112)) : "memory");
    SG_WAIT(sg, sgp + 96);
    SG_APPLY(sg, acc[1]);
    static_for<0, 5>([&](auto sc) __attribute__((always_inline)) {
      static_for<0, 8>([&](auto vc) __attribute__((always_inline)) { EPI_STAGE(acc[1], decltype(sc)::value, decltype(vc)::value); });
    });
    EPI_STORE(3);
    SB();
  }
  {
    u32x4 gq[2];
    u32x4 hf[2][14];
    const char* hsrc[2] = {ws + WS_HF + ((size_t)te * 112 + 14 * ke) * 1024, ws + WS_HF + ((size_t)tl * 112 + 14 * ke) * 1024};
#define H_LOAD(j, lo, hi_) static_for<lo, hi_>([&](auto ic) __attribute__((always_inline)) { constexpr int i = decltype(ic)::value; hf[j][i] = gload<((i % 7) & 3) * 1024>(voff, hsrc[j] + (i >= 7 ? 7168 - rot : rot) + ((i % 7) >> 2) * 4096); })
    pl0 = pl1 = pl2 = 0;
#define ANNOUNCE23(n) asm volatile("s_waitcnt vmcnt(%3)\n\ts_mov_b64 exec, 3\n\tglobal_atomic_add %0, %1, %2 offset:2048\n\ts_mov_b64 exec, -1" :: "v"(lane << 10), "v"(1), "s"(c2p), "i"(n) : "memory")
    asm volatile("s_waitcnt vmcnt(1)\n\ts_barrier" : "+v"(pv) :: "memory");
    const bool slow = __builtin_amdgcn_readfirstlane(pv) < 14u;
    if (slow) {
      ANNOUNCE23(0);
      wait_cnt<1, 6>(c2e, 14u, pl0, pl1, pl2);
    }
    STAMP(6);
    { const char* gp = ws + WS_MODC + (64 * dg + 32 * dt + 8 * kq) * 8; gq[0] = gload<0>(hi * 32, gp); gq[1] = gload<16>(hi * 32, gp); }
    H_LOAD(0, 0, G2_B1);
    if (!slow) ANNOUNCE23(G2_B1 + 2);
    static_for<0, 2>([&](auto jc) __attribute__((always_inline)) {
      constexpr int j = decltype(jc)::value;
      f32x16 acc[2];
#pragma unroll
      for (int c = 0; c < 2; ++c)
#pragma unroll
        for (int r = 0; r < 16; ++r) acc[c][r] = 0.f;
      static_for<0, 14>([&](auto sc) __attribute__((always_inline)) {
        constexpr int s = decltype(sc)::value;
        asm volatile("s_waitcnt vmcnt(%1)" : "+v"(hf[j][s]) : "i"((s + G2_B1 - 1 < 13 ? G2_B1 - 1 : 13 - s) + (j == 1 && s < G2_B1 ? 1 : 0)) : "memory");
        if constexpr (s == 0) POLL_KEEP();
        if constexpr (j == 0 && s == 0) { PIN14(w2f, 0); PIN14(w2f, 14); pin(gq[0]); pin(gq[1]); }
        acc[0] = MFMA32(w2f[2 * s], hf[j][s], acc[0]);
        acc[1] = MFMA32(w2f[2 * s + 1], hf[j][s], acc[1]);
        SB();
        if constexpr (s + G2_B1 < 14) H_LOAD(j, s + G2_B1, s + G2_B1 + 1);
      });
      if constexpr (j == 0) {
      } else {
        STAMP(7);
      }
      char* red = smem + j * 65536;
#pragma unroll
      for (int P = 0; P < 8; ++P)
        *(f32x4*)(red + (P * 8 + w) * 1024 + lane * 16) = f32x4{acc[P & 1][4 * (P >> 1)], acc[P & 1][4 * (P >> 1) + 1], acc[P & 1][4 * (P >> 1) + 2], acc[P & 1][4 * (P >> 1) + 3]};
      if constexpr (j == 0) {
        if (w == 0) wait_cnt<8, 5>(c2l, 14u, pl0, pl1, pl2);
        asm volatile("s_waitcnt lgkmcnt(0)\n\ts_barrier" ::: "memory");
        STAMP(13);
        H_LOAD(1, 0, G2_B1);
      }
      {
        const float gbv[4] = {rbf(U2F(gq[0].x) + U2F(gq[0].y)), rbf(U2F(gq[0].z) + U2F(gq[0].w)), rbf(U2F(gq[1].x) + U2F(gq[1].y)), rbf(U2F(gq[1].z) + U2F(gq[1].w))};
        if constexpr (j == 1) asm volatile("s_waitcnt lgkmcnt(0)\n\ts_barrier" ::: "memory");
        {
          constexpr int jj = j;
          const char* rp = smem + jj * 65536 + w * 8192 + lane * 16;
          const f32x4 y = ((*(const f32x4*)rp + *(const f32x4*)(rp + 1024)) + (*(const f32x4*)(rp + 2048) + *(const f32x4*)(rp + 3072))) +
                          ((*(const f32x4*)(rp + 4096) + *(const f32x4*)(rp + 5120)) + (*(const f32x4*)(rp + 6144) + *(const f32x4*)(rp + 7168)));
          const u32x2 xr = trread(smem + G2_X1 + jj * 4096 + (32 * dt + 8 * kq + 4 * hi + qq) * 64 + (G & 1) * 32 + pp * 8);
          u32x2 o; o.x = pkbf(__builtin_fmaf(y[0], gbv[0], bflo(xr.x)), __builtin_fmaf(y[1], gbv[1], bfhi(xr.x)));
          o.y = pkbf(__builtin_fmaf(y[2], gbv[2], bflo(xr.y)), __builtin_fmaf(y[3], gbv[3], bfhi(xr.y)));
          ostore(o, out + (size_t)(32 * (jj ? tl : te) + (lane & 31)) * D_ + 64 * dg + 32 * dt + 8 * kq + 4 * hi);
        }
      }
      SB();
    });
  }
  STAMP(15);
  STAMP_OUT(stamps, 1024 + b);
  STAMP_OUT2(stamps, 2048 + b);
}

extern "C" void kernel_launch(void* const* d_in, const int* in_sizes, int n_in, void* d_out, int out_size, void* d_ws, size_t ws_size,
                              hipStream_t stream) {
  const u16* x = (const u16*)d_in[0]; const u16* logA = (const u16*)d_in[1]; const u16* Aim = (const u16*)d_in[2];
  const u16* Cr = (const u16*)d_in[3]; const u16* Ci = (const u16*)d_in[4]; const u16* logdt = (const u16*)d_in[5]; const u16* Dsk = (const u16*)d_in[6];
  const u16* w1 = (const u16*)d_in[8]; const u16* w3 = (const u16*)d_in[9]; const u16* w2 = (const u16*)d_in[10];
  const u16* c = (const u16*)d_in[11]; const u16* wm = (const u16*)d_in[12];
  char* ws = (char*)d_ws;
  unsigned long long* stamps = (unsigned long long*)(ws + (64u << 20));
  kf_main<<<256, 512, 0, stream>>>(x, logA, Aim, Cr, Ci, logdt, Dsk, w1, w3, w2, c, wm, ws, (u16*)d_out, stamps);
}
```

```cpp
#include <hip/hip_runtime.h>

typedef unsigned short u16;
typedef unsigned u32;
typedef u32 u32x4 __attribute__((ext_vector_type(4)));
typedef u32 u32x2 __attribute__((ext_vector_type(2)));
typedef short s16x4 __attribute__((ext_vector_type(4)));
typedef __bf16 bf16x8 __attribute__((ext_vector_type(8)));
typedef __bf16 bf16x2 __attribute__((ext_vector_type(2)));
typedef _Float16 f16x8 __attribute__((ext_vector_type(8)));
typedef _Float16 f16x2 __attribute__((ext_vector_type(2)));
typedef float f32x2 __attribute__((ext_vector_type(2)));
typedef float f32x4 __attribute__((ext_vector_type(4)));
typedef float f32x16 __attribute__((ext_vector_type(16)));
#define DI __device__ __forceinline__
#define U2F(x) __builtin_bit_cast(float, (u32)(x))
#define F2U(x) __builtin_bit_cast(u32, (float)(x))
#define I2F(x) __builtin_bit_cast(float, (int)(x))
#define F2I(x) __builtin_bit_cast(int, (float)(x))
#if defined(__HIP_DEVICE_COMPILE__)
#define NOPK __attribute__((target("no-packed-fp32-ops")))
#else
#define NOPK
#endif
#define LDSP(T, p) ((__attribute__((address_space(3))) T*)(p))
#define GAS __attribute__((address_space(1)))
#define RLX_AGENT __ATOMIC_RELAXED, __HIP_MEMORY_SCOPE_AGENT
#define SB() __builtin_amdgcn_sched_barrier(0)
#define LDIN(p) __builtin_nontemporal_load(p)
template <int V> struct IC { static constexpr int value = V; };
template <int I, int N, class F> __device__ __forceinline__ void static_for(F&& f) { if constexpr (I < N) { f(IC<I>{}); static_for<I + 1, N>(f); } }

constexpr int L_ = 2048, D_ = 448, N_ = 256, F_ = 1792;

constexpr int EP_E = 0;
constexpr int EP_R = 64;
constexpr int C0_CNT = 1024;
constexpr int C1_CNT = 9216;
constexpr int C2_CNT = 14336;
constexpr int ST2_OFF = 122880;
constexpr int ZERO_LO = 36864, ZERO_HI = 139264;
constexpr size_t WS_MODA = 139264;
constexpr size_t WS_MODB = WS_MODA + 448 * 32;
constexpr size_t WS_MODC = WS_MODB + 448 * 16;
constexpr size_t WS_ST1 = 167936;
constexpr size_t WS_XT = WS_ST1 + 16384;
constexpr size_t WS_X1T = WS_XT + 64 * 448 * 64;
constexpr size_t WS_XB = WS_X1T + 64 * 448 * 64;
constexpr size_t WS_W13 = WS_XB + 64 * 448 * 64;
constexpr size_t WS_W2F = WS_W13 + 112 * 28 * 1024;
constexpr size_t WS_HF = WS_W2F + 14 * 112 * 1024;
constexpr size_t WS_END = WS_HF + 64 * 112 * 1024;

DI float bflo(u32 v) { return U2F(v << 16); }
DI float bfhi(u32 v) { return U2F(v & 0xffff0000u); }
DI float bf2f(u16 v) { return U2F(((u32)v) << 16); }
DI u32 pkbf(float lo, float hi) { f32x2 p = {lo, hi}; return __builtin_bit_cast(u32, __builtin_convertvector(p, bf16x2)); }
DI u32 pkh(f32x2 p) { return __builtin_bit_cast(u32, __builtin_convertvector(p, f16x2)); }
DI float rbf(float f) { return bflo(pkbf(f, f)); }
DI float dot2(u32 a, u32 b, float c) { return __builtin_amdgcn_fdot2_f32_bf16(__builtin_bit_cast(bf16x2, a), __builtin_bit_cast(bf16x2, b), c, false); }
DI float fexp(float x) { return __builtin_amdgcn_exp2f(x * 1.4426950408889634f); }
DI float silu_bf(float c) { return rbf(c * __builtin_amdgcn_rcpf(1.f + fexp(-c))); }
template <int CTRL> DI float dppf(float x) { return I2F(__builtin_amdgcn_update_dpp(0, F2I(x), CTRL, 0xf, 0xf, false)); }
DI float sum16(float x) { x += dppf<0x128>(x); x += dppf<0x124>(x); x += dppf<0x122>(x); x += dppf<0x121>(x); return x; }
DI float max16(float x) { x = __builtin_fmaxf(x, dppf<0x128>(x)); x = __builtin_fmaxf(x, dppf<0x124>(x)); x = __builtin_fmaxf(x, dppf<0x122>(x)); x = __builtin_fmaxf(x, dppf<0x121>(x)); return x; }
DI float add32(float x) { auto r = __builtin_amdgcn_permlane32_swap(F2U(x), F2U(x), false, false); return U2F(r[0]) + U2F(r[1]); }
DI float add16(float x) { auto r = __builtin_amdgcn_permlane16_swap(F2U(x), F2U(x), false, false); return U2F(r[0]) + U2F(r[1]); }
DI float max64(float x) {
  x = max16(x);
  auto r = __builtin_amdgcn_permlane16_swap(F2U(x), F2U(x), false, false);
  x = __builtin_fmaxf(U2F(r[0]), U2F(r[1]));
  auto q = __builtin_amdgcn_permlane32_swap(F2U(x), F2U(x), false, false);
  return __builtin_fmaxf(U2F(q[0]), U2F(q[1]));
}
DI u32x2 trread(const char* p) { return __builtin_bit_cast(u32x2, __builtin_amdgcn_ds_read_tr16_b64_v4i16(LDSP(s16x4, p))); }
DI u32x2 trread3(const __attribute__((address_space(3))) char* p) { return __builtin_bit_cast(u32x2, __builtin_amdgcn_ds_read_tr16_b64_v4i16((__attribute__((address_space(3))) s16x4*)p)); }
DI void pinf(float& v) { asm volatile("" : "+v"(v)); }
DI void pin(u32x4& v) { asm volatile("" : "+v"(v)); }
DI void ostore(u32x2 v, u16* p) { *(u32x2*)p = v; }
DI f32x2 cmulp(f32x2 a, f32x2 z, f32x2 zs) { return __builtin_elementwise_fma(a.yx, zs, a * z.xx); }

#define STAMP_DECL
#define STAMP(i)
#define CSTAMP(i)
#define STAMP_OUT(buf, slot)
#define STAMP_OUT2(buf, slot)

#define MFMA32(a, b, c) __builtin_amdgcn_mfma_f32_32x32x16_bf16(__builtin_bit_cast(bf16x8, a), __builtin_bit_cast(bf16x8, b), c, 0, 0, 0)
#define ST16(v, off) __builtin_amdgcn_raw_buffer_store_b128(v, rs, (int)(off), 0, 16)
template <int IMM> DI u32x4 gload(u32 voff, const char* sbase) {
  u32x4 r; asm volatile("global_load_dwordx4 %0, %1, %2 offset:%3 sc1" : "=v"(r) : "v"(voff), "s"(sbase), "i"(IMM)); return r;
}
DI void glds16(u32 voff, const char* sbase, u32 ldsaddr) {
  asm volatile("s_mov_b32 m0, %2\n\ts_nop 0\n\tglobal_load_lds_dwordx4 %0, %1 sc1" :: "v"(voff), "s"(sbase), "s"(ldsaddr) : "memory");
}
constexpr unsigned SPIN_CAP = 1u << 16;
template <int OFF> DI u32 ldu16(const u16* p) { u32 r; asm volatile("global_load_ushort %0, %1, off offset:%2" : "=v"(r) : "v"(p), "i"(OFF) : "memory"); return r; }
template <int OFF> DI u32x4 ldnt(const u16* p) { u32x4 r; asm volatile("global_load_dwordx4 %0, %1, off offset:%2 nt" : "=v"(r) : "v"(p), "i"(OFF) : "memory"); return r; }
DI void sgload(unsigned long long& g, GAS unsigned long long* p) { asm volatile("global_load_dwordx2 %0, %1, off sc1" : "+v"(g) : "v"(p) : "memory"); }
DI void sgwait(unsigned long long& g) { asm volatile("s_waitcnt vmcnt(1)" : "+v"(g) :: "memory"); }
DI void sgreload(unsigned long long& g, GAS unsigned long long* p) { asm volatile("global_load_dwordx2 %0, %1, off sc1\n\ts_waitcnt vmcnt(0)" : "+v"(g) : "v"(p) : "memory"); }
#define POLL_ISSUE(v, p, m) asm volatile("s_mov_b64 exec, %3\n\tglobal_atomic_or %0, %1, %2, off sc0\n\ts_mov_b64 exec, -1" : "+v"(v) : "v"(p), "v"(0), "i"(m) : "memory")
template <int NL> DI void poll_loop(GAS u32* p, u32 want, u32& v0, u32& v1, u32& v2) {
#pragma unroll 1
  for (unsigned spins = 0; spins < SPIN_CAP; ++spins) {
    POLL_ISSUE(v2, p, (1 << NL) - 1);
    asm volatile("s_waitcnt vmcnt(2)" : "+v"(v0) :: "memory");
    if (__builtin_amdgcn_ballot_w64(v0 < want) == 0) break;
    POLL_ISSUE(v0, p, (1 << NL) - 1);
    asm volatile("s_waitcnt vmcnt(2)" : "+v"(v1) :: "memory");
    if (__builtin_amdgcn_ballot_w64(v1 < want) == 0) break;
    POLL_ISSUE(v1, p, (1 << NL) - 1);
    asm volatile("s_waitcnt vmcnt(2)" : "+v"(v2) :: "memory");
    if (__builtin_amdgcn_ballot_w64(v2 < want) == 0) break;
  }
}
template <int NL, int SLP> DI void wait_cnt(GAS u32* p, u32 want, u32& v0, u32& v1, u32& v2) {
  v0 = want; v1 = want; v2 = want;
  POLL_ISSUE(v0, p, (1 << NL) - 1); __builtin_amdgcn_s_sleep(SLP);
  POLL_ISSUE(v1, p, (1 << NL) - 1); __builtin_amdgcn_s_sleep(SLP);
  poll_loop<NL>(p, want, v0, v1, v2);
}
#define POLL_KEEP() asm volatile("" :: "v"(pl0), "v"(pl1), "v"(pl2) : "memory")
#define WAIT_VM0() __builtin_amdgcn_s_waitcnt(0x0F70)
#define BAR() __builtin_amdgcn_s_barrier()

constexpr int CV_DSTR = 4 * 4160 + 4608;
constexpr int CV_RED = 2 * CV_DSTR;
constexpr int G1_ST = 4 * 28672;
constexpr int G2_RED = 4 * 28672;
constexpr int G2_X1 = 18 * 8192;
constexpr int TAB_GB = G2_X1 + 8192;
constexpr int TAB_SB = TAB_GB + 256;
constexpr int ARR_CNT = TAB_SB + 1792;
constexpr int KF_LDS = ARR_CNT + 16;
constexpr int KG_REG = 32 * 272;
constexpr int KG_IMG = 2 * CV_DSTR;
constexpr int TASK_LDS = KG_IMG + 8 * KG_REG;
constexpr int KG_TAB = TASK_LDS + 30464;
#define HSLOT stv[14]
#define WSLOT stv[0]
#define W2_N0 13
#define W13_SLP 1
#define G1MAP 0
#define CODEPF 29
#define X_N0 6
#define P0S(i)
#define NP0S(i) STAMP(i)
#define G2_B1 6
#define G2_PL 10
#define EPI0 7
#define W_N0 2
#define W_NM 18
#define W_ST 4
#define W_NB 2
#define W_N1 (W_N0 + W_NB)
#define W_N2 (W_N1 + W_NM)
#define W_ND 2
#define W_N3 (W_N2 + W_ND)
#define SC_SIG 6
#define SC_POLL 21
#define LAS __attribute__((address_space(3)))
#define PIN14(a, o) asm volatile("" : "+v"(a[o]), "+v"(a[o + 1]), "+v"(a[o + 2]), "+v"(a[o + 3]), "+v"(a[o + 4]), "+v"(a[o + 5]), "+v"(a[o + 6]), "+v"(a[o + 7]), \
    "+v"(a[o + 8]), "+v"(a[o + 9]), "+v"(a[o + 10]), "+v"(a[o + 11]), "+v"(a[o + 12]), "+v"(a[o + 13]) :: "memory")

constexpr int g1_e0(int t) { return t == 1 ? EPI0 : 3; }
constexpr int g1_st(int t) { return g1_e0(t) + 19; }
constexpr int g1_an(int t) { return t == 2 ? 18 : 14; }
constexpr int g1_w2(int n) {
  if (n >= 25 && n < 25 + 14 * 4 && (n - 25) % 4 == 0) return (n - 25) / 4;
  if (n >= 84 && n < 84 + 23) { const int q = n - 84; if (q % 3 != 2) return 14 + q - q / 3 < 28 ? 14 + q - q / 3 : -1; }
  return -1;
}
constexpr int g1_sg(int t) { return t == 0 ? 24 : 28 * t + 10; }
constexpr int g1_vm(int a, int b) {
  int c = 0;
  for (int n = a; n < b; ++n) {
    const int t = n / 28, q = n % 28;
    if (n > a) { if (t >= 2 && q == g1_an(t)) ++c; if (n == 108) ++c; if (n == g1_sg(t)) ++c; }
    if (t >= 1 && q == g1_st(t) && n != a) ++c;
    if (g1_w2(n) >= 0) ++c;
  }
  return c;
}
__global__ __launch_bounds__(512) void kf_main(const u16* __restrict__ x, const u16* __restrict__ logA, const u16* __restrict__ Aim,
                                               const u16* __restrict__ Cr, const u16* __restrict__ Ci, const u16* __restrict__ logdt,
                                               const u16* __restrict__ Dsk, const u16* __restrict__ w1, const u16* __restrict__ w3,
                                               const u16* __restrict__ w2, const u16* __restrict__ cvec, const u16* __restrict__ wmod,
                                               char* __restrict__ ws, u16* __restrict__ out, unsigned long long* stamps) {
  __shared__ __attribute__((aligned(16))) char smem[KF_LDS];
  const int tid = __builtin_amdgcn_workitem_id_x(), lane = tid & 63;
  const int w = __builtin_amdgcn_readfirstlane(tid >> 6);
  const int b = __builtin_amdgcn_workgroup_id_x(), xcd = b & 7, idx = b >> 3;
  const int hi = lane >> 5;
  const int G = lane >> 4, qq = (lane & 15) >> 2, pp = lane & 3;
  const u32 voff = lane * 16;
  GAS u32* ctl = (GAS u32*)ws;
  auto rs = __builtin_amdgcn_make_buffer_rsrc(ws, 0, (int)WS_END, 0x00020000);
  const u32 lds0 = (u32)(size_t)LDSP(char, smem);
  STAMP_DECL;
  STAMP(0);
  u32 pl0 = 0, pl1 = 0, pl2 = 0;
  u32x4 cpf;
  { const char* pc; asm volatile("s_getpc_b64 %0" : "=s"(pc));
    const u32 o = (idx < CODEPF ? idx : 0) * 1024 + lane * 16;
    if (w == 7) asm volatile("global_load_dwordx4 %0, %1, %2" : "=v"(cpf) : "v"(o), "s"(pc) : "memory"); }
  const bool mainwg = b < 224;
  const int dl = w >> 2, q4 = w & 3, t256 = tid & 255;
  auto arrive0 = [&]() __attribute__((always_inline)) {
    u32 old;
    asm volatile("s_waitcnt vmcnt(0)\n\ts_mov_b64 exec, 1\n\tds_add_rtn_u32 %0, %1, %2\n\ts_mov_b64 exec, -1\n\ts_waitcnt lgkmcnt(0)" : "=&v"(old) : "v"(lds0 + ARR_CNT), "v"(1) : "memory");
    if (__builtin_amdgcn_readfirstlane(old) == (mainwg ? 4u : (b < 249 ? 8u : 0u)) && lane < 16) __hip_atomic_fetch_add(ctl + C0_CNT + (lane * 8 + xcd) * 32, 1u, RLX_AGENT);
  };
  GAS u32* c0p = ctl + C0_CNT + ((b & 15) * 8 + lane) * 32;
  int kg_wb, kg_wst;
  u32 kga[12];
  u32 dskr = 0;
  unsigned long long epoch;
  {
    asm volatile("" :: "s"(x), "s"(logA), "s"(Aim), "s"(Cr), "s"(Ci), "s"(logdt), "s"(Dsk), "s"(w1), "s"(w3), "s"(cvec), "s"(wmod), "s"(ws));
    u32 lar = 0, air = 0, crr = 0, cir = 0, ldt = 0;
    if (mainwg) {
      const int d = 2 * b + dl;
      asm volatile("global_load_ushort %0, %6, %7 nt\n\tglobal_load_ushort %1, %6, %8 nt\n\tglobal_load_ushort %4, %11, %12\n\tglobal_load_ushort %2, %6, %9 nt\n\tglobal_load_ushort %3, %6, %10 nt\n\tglobal_load_dword %5, %13, %14"
                   : "+v"(lar), "+v"(air), "+v"(crr), "+v"(cir), "+v"(ldt), "+v"(dskr) : "v"((d * N_ + t256) * 2), "s"(logA), "s"(Aim), "s"(Cr), "s"(Ci), "v"(d * 2), "s"(logdt), "v"(b * 4), "s"(Dsk) : "memory");
    }
    SB();
    unsigned long long ep = 0;
    u32 pe = 32u;
    const int task = b >= 249 ? 2 * b - 364 + dl : (dl || b >= 246 ? -1 : (b < 134 ? b : b + 14));
    char* stg = smem + (dl ? 0 : TASK_LDS);
    u32x4 v[7]; u32 cv[7];
    const bool isx = task < 64, isw = task >= 148;
    const char* tsb; u32 tstep, tvo;
    {
      const int m = task - (isw ? 148 : 64);
      const size_t px = (size_t)x, pw = q4 >= 2 ? (size_t)w3 : (size_t)w1, pm = (size_t)wmod;
      const u32 k1 = isw ? 28672u : 128u, k0 = isw ? 1792u : 1204224u;
      const u32 off = isx ? (u32)task * 28672u : (u32)(m >> 1) * k1 + (u32)(m & 1) * k0;
      tsb = (const char*)((isx ? px : (isw ? pw : pm)) + off);
      tstep = isx ? 128u : (isw ? 256u : 172032u);
      tvo = ((t256 >> (isw ? 4 : 3)) & (isw ? 7 : 31)) * (isx ? 896u : (isw ? 3584u : 5376u)) + (t256 & (isw ? 15 : 7)) * 16;
    }
#define taskload() do { _Pragma("unroll") for (int i = 0; i < 7; ++i) asm volatile("global_load_dwordx4 %0, %1, %2 nt" : "=v"(v[i]) : "v"(tvo), "s"(tsb + i * tstep) : "memory"); } while (0)
    auto cload = [&]() __attribute__((always_inline)) {
      const u16* cp = cvec + 224 * (task & 1) + (t256 >> 3);
      static_for<0, 7>([&](auto ic) __attribute__((always_inline)) { constexpr int i = decltype(ic)::value; cv[i] = ldu16<64 * i>(cp); });
    };
    if (task >= 0 && b < 249) taskload();
    if (task >= 64 && task < 134) {
      cload();
    } else if (task < 0 && !mainwg) {
      if (dl) ST16((u32x4{0, 0, 0, 0}), ZERO_LO + (b - 224) * 4096 + t256 * 16);
      if (b == 248 && w == 0) {
        const unsigned long long e0 = __hip_atomic_load((GAS unsigned long long*)(ctl + EP_E), RLX_AGENT);
#pragma unroll
        for (int i = 0; i < 2; ++i) __hip_atomic_store(ctl + C0_CNT + i * 2048 + lane * 32, 0u, RLX_AGENT);
        WAIT_VM0();
        asm volatile("s_waitcnt vmcnt(0)" ::: "memory");
        if (lane < 16) __hip_atomic_store((GAS unsigned long long*)(ctl + EP_R + lane * 32), e0 + 1, RLX_AGENT);
      }
    }
    SB();
    P0S(15);
    if (tid < 4) *(u32*)(smem + ARR_CNT + tid * 4) = 0;
    asm volatile("s_waitcnt lgkmcnt(0)\n\ts_barrier" ::: "memory");
    SB();
    auto meet = [&](u32 want) __attribute__((always_inline)) {
      asm volatile("s_waitcnt lgkmcnt(0)\n\ts_mov_b64 exec, 1\n\tds_add_u32 %0, %1 offset:4\n\ts_mov_b64 exec, -1" :: "v"(lds0 + ARR_CNT + dl * 4), "v"(1) : "memory");
#pragma unroll 1
      for (unsigned spins = 0; spins < 16 * SPIN_CAP; ++spins) {
        if (*(volatile LAS u32*)(size_t)(lds0 + ARR_CNT + 4 + dl * 4) >= want) break;
        __builtin_amdgcn_s_sleep(1);
      }
      asm volatile("" ::: "memory");
    };
#pragma unroll
    for (int h = 0; h < 2; ++h) {
      const int lb = 2040 - 8 * t256 - 8 * h;
      const int kr = lb < 256 ? 0 : 24 - __builtin_clz(lb);
      const float mB = U2F((u32)(150 + kr) << 23) + (float)(lb < 0 ? 704 : lb);
      const u32 kpa = lds0 + (u32)(KG_TAB + dl * 1536) + 256u * kr - ((u32)(150 + kr) << 24);
#pragma unroll
      for (int e = 0; e < (h ? 4 : 8); ++e) { kga[8 * h + e] = (F2U(mB + (float)(7 - e)) << 1) + kpa; asm volatile("" : "+v"(kga[8 * h + e])); }
    }
    if (t256 == 0) *(u32x4*)(smem + KG_TAB + dl * 1536 + 1408) = u32x4{0, 0, 0, 0};
    {
      char* cp = smem + dl * CV_DSTR;
      if (mainwg) {
        if (t256 < 32) *(u32x4*)(cp + 4 * 4160 + t256 * 16) = u32x4{0, 0, 0, 0};
        else if (t256 < 40) *(u32x4*)(cp + ((t256 - 32) >> 1) * 4160 + 4096 + (t256 & 1) * 16) = u32x4{0, 0, 0, 0};
      }
    }
    const int s = lane & 31;
    {
      const int kw = s < 8 ? 0 : (s < 20 ? (s >> 2) - 1 : 3);
      int wb = s < 12 ? (s < 8 ? s : 248 + s) : (s < 16 ? 384 + ((s & 2) << 5) + (s & 1) : 32 * s);
      kg_wst = 16 >> kw;
      kg_wb = (wb > 672 ? 672 : wb) * 2 + KG_TAB + dl * 1536 + (8 * q4 + 4 * hi) * kg_wst;
    }
    char* reg = smem + KG_IMG + w * KG_REG;
    f16x8 bfr[8];
    f32x2 z8, z16; float scl = 0.f, fs = 0.f;
    unsigned long long tagr = 0;
    GAS u32* tagp = ctl + EP_R + (b & 15) * 32;
#define EP_ISSUE() asm volatile("s_mov_b64 exec, 1\n\tglobal_load_dwordx2 %1, %4, off sc1\n\tglobal_atomic_or_x2 %0, %2, %3, off sc0\n\ts_mov_b64 exec, -1" \
                     : "+&v"(tagr), "+&v"(ep) : "v"(tagp), "v"(0ull), "v"(ctl + EP_E) : "memory")
    auto epcheck = [&]() __attribute__((always_inline)) {
#pragma unroll 1
      for (unsigned spins = 0; spins < SPIN_CAP; ++spins) {
        asm volatile("s_waitcnt vmcnt(0)" : "+v"(tagr), "+v"(ep) :: "memory");
        if ((__builtin_amdgcn_ballot_w64(tagr != ep + 1) & 1) == 0) break;
        EP_ISSUE();
      }
      arrive0();
    };
    if (mainwg) {
      if (task < 0) asm volatile("s_waitcnt vmcnt(0)" ::: "memory"); else if (isx || isw) asm volatile("s_waitcnt vmcnt(7)" ::: "memory"); else asm volatile("s_waitcnt vmcnt(14)" ::: "memory");
      asm volatile("" : "+v"(lar), "+v"(air), "+v"(crr), "+v"(cir), "+v"(ldt), "+v"(dskr) :: "memory");
      P0S(12);
      if (w == 4) EP_ISSUE();
    }
    if (mainwg) {
      const float dt = rbf(fexp(bflo(ldt)));
      const float ar = -rbf(fexp(bflo(lar))) * dt * 1.4426950408889634f;
      const float ai = bflo(air) * dt;
      const float I2PH = 0.15915494f, I2PL = 6.4206382e-9f;
      const float th = ai * I2PH;
      const float tl = __builtin_fmaf(ai, I2PL, __builtin_fmaf(ai, I2PH, -th));
      auto zpow = [&](float sg) __attribute__((always_inline)) {
        float e = __builtin_amdgcn_exp2f(ar * sg);
        float rev = __builtin_fmaf(tl, sg, __builtin_amdgcn_fractf(th * sg));
        return f32x2{e * __builtin_amdgcn_cosf(rev), e * __builtin_amdgcn_sinf(rev)};
      };
      const f32x2 z1 = zpow(1.f), z256 = zpow(256.f);
      z16 = zpow(16.f);
      const f32x2 s1 = {-z1.y, z1.y}, s256 = {-z256.y, z256.y};
      u32 eb = ((crr & 0x7f80u) > (cir & 0x7f80u) ? (crr & 0x7f80u) : (cir & 0x7f80u));
      asm("s_nop 1\n\tv_max_u32_dpp %0, %0, %0 row_ror:8 row_mask:0xf bank_mask:0xf\n\ts_nop 1\n\tv_max_u32_dpp %0, %0, %0 row_ror:4 row_mask:0xf bank_mask:0xf\n\ts_nop 1\n\t"
          "v_max_u32_dpp %0, %0, %0 row_ror:2 row_mask:0xf bank_mask:0xf\n\ts_nop 1\n\tv_max_u32_dpp %0, %0, %0 row_ror:1 row_mask:0xf bank_mask:0xf\n\ts_nop 1" : "+v"(eb));
      {
        const u32 e0 = __builtin_amdgcn_readlane(eb, 0), e1 = __builtin_amdgcn_readlane(eb, 16), e2 = __builtin_amdgcn_readlane(eb, 32), e3 = __builtin_amdgcn_readlane(eb, 48);
        const u32 m0 = e0 > e1 ? e0 : e1, m1 = e2 > e3 ? e2 : e3;
        eb = (m0 > m1 ? m0 : m1) >> 7;
      }
      eb = eb < 1u ? 1u : (eb > 252u ? 252u : eb);
      scl = U2F((253u - eb) << 23);
      fs = 2.f * dt * U2F((eb + 1u) << 23);
      f32x2 lo[9], hp[9];
      lo[1] = z1; hp[1] = z256;
#pragma unroll
      for (int i = 2; i <= 8; ++i) { lo[i] = cmulp(lo[i - 1], z1, s1); hp[i] = cmulp(hp[i - 1], z256, s256); }
      const f32x2 a2 = cmulp(lo[2], z256, s256), a4 = cmulp(lo[4], z256, s256), a6 = cmulp(lo[6], z256, s256);
      const f32x2 b4 = cmulp(a4, z256, s256), c4 = cmulp(b4, z256, s256);
      const u32 col[21] = {0x00003c00u, pkh(lo[1]), pkh(lo[2]), pkh(lo[3]), pkh(lo[4]), pkh(lo[5]), pkh(lo[6]), pkh(lo[7]),
                           pkh(hp[1]), pkh(a2), pkh(a4), pkh(a6), pkh(hp[2]), pkh(b4), pkh(hp[3]), pkh(c4),
                           pkh(hp[4]), pkh(hp[5]), pkh(hp[6]), pkh(hp[7]), pkh(hp[8])};
#pragma unroll
      for (int i = 0; i < 21; ++i) *(u32*)(reg + i * 272 + lane * 4) = col[i];
      z8 = lo[8];
#pragma unroll
      for (int i = 0; i < 8; ++i) bfr[i] = *(const f16x8*)(reg + s * 272 + i * 32 + hi * 16);
      asm volatile("s_waitcnt lgkmcnt(0)" : "+v"(bfr[0]), "+v"(bfr[1]), "+v"(bfr[2]), "+v"(bfr[3]), "+v"(bfr[4]), "+v"(bfr[5]), "+v"(bfr[6]), "+v"(bfr[7]) :: "memory");
    }
    if (!mainwg) asm volatile("s_waitcnt vmcnt(0)" ::: "memory");
    asm volatile("" :: "v"(cpf));
    STAMP(8);
    if (w == 4) { if (!mainwg) EP_ISSUE(); if (mainwg || b >= 249) epcheck(); }
    if (b >= 249) { taskload(); cload(); }
#define PIN_V() asm volatile("s_waitcnt vmcnt(0)" : "+v"(v[0]), "+v"(v[1]), "+v"(v[2]), "+v"(v[3]), "+v"(v[4]), "+v"(v[5]), "+v"(v[6]) :: "memory")
    if (task >= 0 && task < 148) {
      PIN_V();
      P0S(13);
      if (task < 64) {
            const int row = t256 >> 3, p = t256 & 7;
            float sm = 0.f, sq = 0.f;
#pragma unroll
            for (int i = 0; i < 7; ++i) {
              pin(v[i]);
              *(u32x4*)(stg + row * 928 + (p + 8 * i) * 16) = v[i];
#pragma unroll
              for (int j = 0; j < 4; ++j) { sm = dot2(v[i][j], 0x3f803f80u, sm); sq = dot2(v[i][j], v[i][j], sq); }
            }
            sm += dppf<0xB1>(sm); sq += dppf<0xB1>(sq); sm += dppf<0x4E>(sm); sq += dppf<0x4E>(sq); sm += dppf<0x141>(sm); sq += dppf<0x141>(sq);
            const float m = sm * (1.f / D_);
            const float var = __builtin_fmaxf(sq * (1.f / D_) - m * m, 0.f);
            const float sd = rbf(__builtin_amdgcn_sqrtf(rbf(rbf(var) + 1e-5f)));
            const float rsd = __builtin_amdgcn_rcpf(sd);
            if (p < 2) __builtin_amdgcn_raw_buffer_store_b32(F2U(p ? -rbf(m) * rsd : rsd), rs, (int)WS_ST1 + p * 8192 + (task * 32 + row) * 4, 0, 16);
        meet(4u);
        const char* rb = stg + (8 * G + qq) * 928 + q4 * 224 + pp * 8;
#pragma unroll
        for (int i = 0; i < 7; ++i) {
          const u32x2 lo = trread(rb + i * 32), hh = trread(rb + i * 32 + 4 * 928);
          ST16((u32x4{lo.x, lo.y, hh.x, hh.y}), (int)WS_XT + task * 28672 + q4 * 7168 + (lane & 15) * 64 + G * 16 + i * 1024);
        }
      } else {
            float acc[8] = {0, 0, 0, 0, 0, 0, 0, 0};
#pragma unroll
            for (int i = 0; i < 7; ++i) {
              asm volatile("" : "+v"(cv[i]));
              const u32 sh = F2U(silu_bf(bflo(cv[i]))), sl = sh >> 16;
#pragma unroll
              for (int j = 0; j < 4; ++j) { acc[2 * j] = dot2(v[i][j], sl, acc[2 * j]); acc[2 * j + 1] = dot2(v[i][j], sh, acc[2 * j + 1]); }
            }
#pragma unroll
            for (int j = 0; j < 8; ++j) { acc[j] += dppf<0x128>(acc[j]); acc[j] = add32(acc[j]); }
            if ((lane & 40) == 0) {
              float* red = (float*)stg + (q4 * 2 + (lane >> 4)) * 64 + (lane & 7) * 8;
              *(f32x4*)red = f32x4{acc[0], acc[1], acc[2], acc[3]};
              *(f32x4*)(red + 4) = f32x4{acc[4], acc[5], acc[6], acc[7]};
            }
        meet(4u);
            if (q4 == 0) {
              const int m = task - 64, cg = m >> 1;
              float t = 0.f;
#pragma unroll
              for (int j = 0; j < 8; ++j) t += ((const float*)stg)[j * 64 + lane];
              const int sec = cg / 7, dd = (cg % 7) * 64 + lane;
              const int o1 = sec == 3 ? (int)WS_MODB + dd * 16 : (sec == 5 ? (int)WS_MODC + dd * 8 : (int)WS_MODA + dd * 32 + (sec == 4 ? 3 : sec) * 8);
              __builtin_amdgcn_raw_buffer_store_b32(F2U(t), rs, o1 + (m & 1) * 4, 0, 16);
              if (sec == 4) __builtin_amdgcn_raw_buffer_store_b32(F2U(t), rs, (int)WS_MODB + dd * 16 + 8 + (m & 1) * 4, 0, 16);
            }
      }
    }
    if (task >= 148) {
      PIN_V();
      P0S(13);
#pragma unroll
      for (int i = 0; i < 7; ++i) *(u32x4*)(stg + (t256 >> 4) * 1824 + ((t256 & 15) + 16 * i) * 16) = v[i];
      meet(4u);
      const int m = task - 148;
      const char* rb = stg + (8 * (G & 1) + qq) * 1824 + (14 * q4 + hi) * 32 + (8 * (pp & 1) + 4 * (pp >> 1)) * 2;
      const int dst = (int)WS_W13 + ((56 * (m & 1) + 14 * q4 + hi) * 28 + (m >> 2)) * 1024 + ((m >> 1) & 1) * 512 + (lane & 31) * 16;
#pragma unroll
      for (int i = 0; i < 7; ++i) {
        const u32x2 lo = trread(rb + i * 64), hh = trread(rb + i * 64 + 4 * 1824);
        ST16((u32x4{lo.x, lo.y, hh.x, hh.y}), dst + i * 57344);
      }
    }
    P0S(10);
    if (mainwg) {
        pinf(scl);
        f32x2 ra = {bflo(crr) * scl, -bflo(cir) * scl};
        f32x2 rb = cmulp(ra, z8, f32x2{z8.y, -z8.y});
        const f32x2 s16c = {z16.y, -z16.y};
#pragma unroll
        for (int r = 0; r < 32; r += 2) {
          *(u32*)(reg + r * 272 + lane * 4) = pkh(ra); *(u32*)(reg + (r + 1) * 272 + lane * 4) = pkh(rb);
          if (r < 30) { ra = cmulp(ra, z16, s16c); rb = cmulp(rb, z16, s16c); }
        }
        if (task >= 0) { WAIT_VM0(); arrive0(); STAMP(11); }
        f32x16 acc = {};
#pragma unroll
        for (int i = 0; i < 8; ++i) {
          const f16x8 af = *(const f16x8*)(reg + s * 272 + i * 32 + hi * 16);
          acc = __builtin_amdgcn_mfma_f32_32x32x16_f16(af, bfr[i], acc, 0, 0, 0);
        }
#pragma unroll
        for (int qd = 0; qd < 4; ++qd)
          *(f32x4*)(reg + qd * 1024 + lane * 16) = f32x4{acc[4 * qd] * fs, acc[4 * qd + 1] * fs, acc[4 * qd + 2] * fs, acc[4 * qd + 3] * fs};
    }
    if (mainwg) {
      if (w == 4) POLL_ISSUE(pe, c0p, 255);
      meet(dl ? 4u : 8u);
    {
      const char* rp = smem + KG_IMG + dl * 4 * KG_REG + q4 * 1024 + lane * 16;
      const f32x4 t = (*(const f32x4*)rp + *(const f32x4*)(rp + KG_REG)) + (*(const f32x4*)(rp + 2 * KG_REG) + *(const f32x4*)(rp + 3 * KG_REG));
      const u32 ta = pkbf(t[0], t[1]), tc = pkbf(t[2], t[3]);
      char* q = smem + kg_wb;
      *(u16*)q = (u16)ta; *(u16*)(q + kg_wst) = (u16)(ta >> 16); *(u16*)(q + 2 * kg_wst) = (u16)tc; *(u16*)(q + 3 * kg_wst) = (u16)(tc >> 16);
    }
    }
    if (!mainwg && b < 249) { WAIT_VM0(); arrive0(); }
    if (w == 4) {
      asm volatile("s_waitcnt vmcnt(0)" : "+v"(pe) :: "memory");
      if (mainwg) { if (__builtin_amdgcn_ballot_w64(pe < 32u) != 0) wait_cnt<8, 6>(c0p, 32u, pl0, pl1, pl2); }
      else if (b < 249) epcheck();
    }
    STAMP(14);
    epoch = ep + 1;
    asm volatile("s_waitcnt lgkmcnt(0)\n\ts_barrier" ::: "memory");
  }
  if (!mainwg) {
    const int fr = b - 224;
    if (fr < 28) {
      u32x4 v[7];
#pragma unroll
      for (int i = 0; i < 7; ++i) v[i] = LDIN((const u32x4*)(w2 + (size_t)(64 * fr + (tid >> 3)) * D_ + ((tid & 7) + 8 * i) * 8));
#pragma unroll
      for (int i = 0; i < 7; ++i) *(u32x4*)(smem + (tid >> 3) * 912 + ((tid & 7) + 8 * i) * 16) = v[i];
      asm volatile("s_waitcnt lgkmcnt(0)\n\ts_barrier" ::: "memory");
      const char* rbase = smem + (16 * (w & 3) + 8 * hi + qq) * 912 + (w >> 2) * 448 + (16 * (G & 1) + 4 * pp) * 2;
      const int dst = (int)WS_W2F + ((7 * (w >> 2)) * 112 + 4 * fr + (w & 3)) * 1024 + lane * 16;
#pragma unroll
      for (int s = 0; s < 7; ++s) {
        u32x2 lo = trread(rbase + s * 64), hh = trread(rbase + s * 64 + 4 * 912);
        __builtin_amdgcn_raw_buffer_store_b128(u32x4{lo.x, lo.y, hh.x, hh.y}, rs, dst + s * 112 * 1024, 0, 16);
      }
    }
    asm volatile("s_waitcnt vmcnt(0)\n\ts_barrier" ::: "memory");
    if (w == 4) {
      wait_cnt<8, 6>(c0p, 32u, pl0, pl1, pl2);
      asm volatile("s_waitcnt vmcnt(0)" ::: "memory");
      POLL_KEEP();
      if (b == 248 && lane == 0) __hip_atomic_store((GAS unsigned long long*)(ctl + EP_E), epoch, RLX_AGENT);
      STAMP(3);
      if (lane < 16) __hip_atomic_fetch_add(ctl + C1_CNT + (lane * 9 + 8) * 32, 1u, RLX_AGENT);
      wait_cnt<8, 6>(ctl + C1_CNT + ((b & 15) * 9 + lane) * 32, 28u, pl0, pl1, pl2);
      asm volatile("s_waitcnt vmcnt(0)" ::: "memory");
      POLL_KEEP();
    }
    asm volatile("s_barrier" ::: "memory");
    STAMP(4);
    {
      const char* xsrc = ws + WS_X1T + (size_t)(2 * fr + (w >> 2)) * 28672 + (w & 3) * 1024;
#pragma unroll
      for (int i = 0; i < 7; ++i) glds16(voff, xsrc + i * 4096, lds0 + w * 7168 + i * 1024);
      const char* sp = smem + w * 7168 + (8 * hi + qq) * 64 + (G & 1) * 32 + pp * 8;
      float s0 = 0.f, q0 = 0.f, s1 = 0.f, q1 = 0.f;
      static_for<0, 7>([&](auto ic) __attribute__((always_inline)) {
        constexpr int i = decltype(ic)::value;
        asm volatile("s_waitcnt vmcnt(%0)" :: "i"(6 - i) : "memory");
        const u32x2 lo = trread(sp + i * 1024), hh = trread(sp + i * 1024 + 256);
        s0 = dot2(lo.x, 0x3f803f80u, s0); q0 = dot2(lo.x, lo.x, q0); s1 = dot2(lo.y, 0x3f803f80u, s1); q1 = dot2(lo.y, lo.y, q1);
        s0 = dot2(hh.x, 0x3f803f80u, s0); q0 = dot2(hh.x, hh.x, q0); s1 = dot2(hh.y, 0x3f803f80u, s1); q1 = dot2(hh.y, hh.y, q1);
      });
      auto r = __builtin_amdgcn_permlane32_swap(F2U(s0 + s1), F2U(q0 + q1), false, false);
      *(float*)(smem + 65536 + w * 256 + (lane & 31) * 8 + hi * 4) = U2F(r[0]) + U2F(r[1]);
      asm volatile("s_waitcnt lgkmcnt(0)\n\ts_barrier" ::: "memory");
      if (w == 0) {
        const char* pt = smem + 65536 + hi * 1024 + (lane & 31) * 8;
        const f32x2 ts = (*(const f32x2*)pt + *(const f32x2*)(pt + 256)) + (*(const f32x2*)(pt + 512) + *(const f32x2*)(pt + 768));
        const float m = ts[0] * (1.f / D_);
        const float var = __builtin_fmaxf(ts[1] * (1.f / D_) - m * m, 0.f);
        const float sd = __builtin_amdgcn_sqrtf(rbf(rbf(var) + 1e-5f));
        __hip_atomic_store((GAS unsigned long long*)(ws + ST2_OFF) + 64 * fr + lane, (1ull << 32) | pkbf(-m, sd), RLX_AGENT);
      }
    }
    STAMP(5);
    return;
  }
  const int rt = 4 * (xcd >> 1) + idx / 7, ct = 7 * (xcd & 1) + idx % 7;
  const int ft = 8 * ct + w;
  const int r2 = 4 * xcd + idx / 7, dg = idx % 7, kq = w >> 1, dt = w & 1;
  const int te = 4 * (r2 >> 1) + (r2 & 1), tl = 4 * (r2 >> 1) + 3 - (r2 & 1);
  const int wv = (int)WS_W13 + ft * 28672 + lane * 16;
#define WLOAD(p) wf[p] = __builtin_amdgcn_raw_buffer_load_b128(rs, wv + ((p) & 3) * 1024, ((p) & ~3) * 1024, 16)

  u32x4 wf[28];
  u32x4 wex;
  const int hbase = (int)WS_HF + (4 * rt * 112 + ft) * 1024 + lane * 16;

  {
    const int a = lane & 15;
    const int l0 = 256 * w + 16 * a + 4 * G;
    const int xoff = ((l0 >> 5) * 448 + 2 * b) * 64 + (l0 & 31) * 2;
    STAMP(9);
    u32x2 xx[2]; u32x4 st[2];
    float msv = U2F(__builtin_amdgcn_raw_buffer_load_b32(rs, (int)WS_MODA + (lane & 15) * 4, b * 64, 16));
#pragma unroll
    for (int i = 0; i < 2; ++i) {
      xx[i] = __builtin_amdgcn_raw_buffer_load_b64(rs, (int)WS_XT + xoff + i * 64, 0, 16);
      st[i] = __builtin_amdgcn_raw_buffer_load_b128(rs, (int)WS_ST1 + l0 * 4, i * 8192, 16);
    }
    SB();
    {
      u32 kw[6];
#pragma unroll
      for (int e = 0; e < 6; ++e) kw[e] = (u32)*(const LAS u16*)(size_t)kga[2 * e] | ((u32)*(const LAS u16*)(size_t)kga[2 * e + 1] << 16);
      char* cp = smem + dl * CV_DSTR;
      u32 al[5];
#pragma unroll
      for (int i = 0; i < 5; ++i) al[i] = __builtin_amdgcn_alignbit(kw[i + 1], kw[i], 16);
      char* q = cp + t256 * 16;
      *(u32x4*)(q) = u32x4{kw[0], kw[1], kw[2], kw[3]};
      *(u32x4*)(q + 4160) = u32x4{al[0], al[1], al[2], al[3]};
      *(u32x4*)(q + 8320) = u32x4{kw[1], kw[2], kw[3], kw[4]};
      *(u32x4*)(q + 12480) = u32x4{al[1], al[2], al[3], al[4]};
    }
    SB();
    const f32x4 tb = __builtin_bit_cast(f32x4, __builtin_amdgcn_raw_buffer_load_b128(rs, (int)WS_MODB + (tid < 448 ? tid : 447) * 16, 0, 16));
    SB();
#pragma unroll
    for (int i = 0; i < W_N1; ++i) WLOAD(i);
    SB();
    msv = rbf(msv + dppf<0xB1>(msv));
    if (lane & 2) msv = rbf(1.f + msv);
#define MSV(i) U2F(__builtin_amdgcn_readlane(F2U(msv), i))
    float ga[2], b2v[2];
    const int swl = (lane ^ ((lane >> 5) << 3)) * 16;
#pragma unroll
    for (int i = 0; i < 2; ++i) {
      const float sa = MSV(8 * i), beta = MSV(8 * i + 2);
      ga[i] = MSV(8 * i + 4); b2v[i] = MSV(8 * i + 6);
      const float gd = ga[i] * (i ? bfhi(dskr) : bflo(dskr));
      const float xv[4] = {bflo(xx[i].x), bfhi(xx[i].x), bflo(xx[i].y), bfhi(xx[i].y)};
      float hv[4];
#pragma unroll
      for (int e = 0; e < 4; ++e)
        hv[e] = __builtin_fmaf(__builtin_fmaf(xv[e], U2F(st[0][e]), U2F(st[1][e])), beta, sa);
      *(u32x2*)(smem + i * CV_DSTR + 4 * 4160 + 512 + l0 * 2) = u32x2{pkbf(hv[0], hv[1]), pkbf(hv[2], hv[3])};
      f32x4 base;
#pragma unroll
      for (int e = 0; e < 4; ++e) base[e] = __builtin_fmaf(hv[e], gd, xv[e]);
      *(f32x4*)(smem + CV_RED + (i * 40 + w * 5 + 4) * 1024 + swl) = base;
    }
    SB();
    POLL_KEEP();
    asm volatile("s_waitcnt lgkmcnt(0)\n\ts_barrier" ::: "memory");
    STAMP(1);
    f32x4 acc[8];
#pragma unroll
    for (int t = 0; t < 8; ++t) acc[t] = f32x4{0, 0, 0, 0};
    const int im = 15 - a;
    const u32 abase = lds0 + dl * CV_DSTR + (im & 3) * 4160 + (2016 + 8 * G + 4 * (im >> 2)) * 2 - q4 * 128 - 3648;
    const u32 bbase = lds0 + dl * CV_DSTR + 4 * 4160 + (256 - 16 + 16 * a + 8 * G) * 2 - q4 * 128 - 64;
    char* dump = smem + CV_RED + (dl * 40 + q4) * 1024 + swl;
#pragma unroll
    for (int cc = 0; cc < 2; ++cc) {
      u32x2 alo[8], ahi[8]; u32x4 bfr[8];
#pragma unroll
      for (int c1 = 0; c1 < 8; ++c1)
        asm volatile("ds_read_b64 %0, %3 offset:%5\n\tds_read_b64 %1, %3 offset:%6\n\tds_read_b128 %2, %4 offset:%7"
                     : "=&v"(alo[c1]), "=&v"(ahi[c1]), "=&v"(bfr[c1])
                     : "v"(abase), "v"(bbase), "i"(3648 - (8 * c1 + cc) * 64), "i"(3656 - (8 * c1 + cc) * 64), "i"(64 + (8 * c1 - cc) * 64) : "memory");
      SB();
      if (cc == 0) __builtin_amdgcn_s_setprio(1); else __builtin_amdgcn_s_setprio(0);
#pragma unroll
      for (int t = 0; t < 8; ++t) {
        if (t == 0) asm volatile("s_waitcnt lgkmcnt(15)" : "+v"(alo[0]), "+v"(ahi[0]), "+v"(bfr[0]), "+v"(alo[1]), "+v"(ahi[1]), "+v"(bfr[1]), "+v"(alo[2]), "+v"(ahi[2]), "+v"(bfr[2]));
        if (t == 3) asm volatile("s_waitcnt lgkmcnt(9)" : "+v"(alo[3]), "+v"(ahi[3]), "+v"(bfr[3]), "+v"(alo[4]), "+v"(ahi[4]), "+v"(bfr[4]));
        if (t == 5) asm volatile("s_waitcnt lgkmcnt(0)" : "+v"(alo[5]), "+v"(ahi[5]), "+v"(bfr[5]), "+v"(alo[6]), "+v"(ahi[6]), "+v"(bfr[6]), "+v"(alo[7]), "+v"(ahi[7]), "+v"(bfr[7]));
#pragma unroll
        for (int c1 = 0; c1 <= t; ++c1) {
          acc[t] = __builtin_amdgcn_mfma_f32_16x16x32_bf16(__builtin_bit_cast(bf16x8, u32x4{alo[c1].x, alo[c1].y, ahi[c1].x, ahi[c1].y}),
                                                           __builtin_bit_cast(bf16x8, bfr[t - c1]), acc[t], 0, 0, 0);
          const int n = t * (t + 1) / 2 + c1;
          if ((n % W_ST) == W_ST - 1 && W_N1 + (36 / W_ST) * cc + n / W_ST < W_N2) { SB(); WLOAD(W_N1 + (36 / W_ST) * cc + n / W_ST); SB(); }
          if (cc == 1 && t >= 5 && c1 >= 1) {
            const int u = (t == 5 ? c1 - 1 : (t == 6 ? (c1 == 1 ? 5 : -1) : (c1 == 1 ? 6 : -1)));
            if (u >= 0) { SB(); *(f32x4*)(dump + u * 5120) = acc[u]; SB(); }
          }
        }
        SB();
      }
    }
    *(f32x4*)(dump + 7 * 5120) = acc[7];
    STAMP(2);
    if (tid < 448) {
      const float be = rbf(1.f + rbf(tb[2] + tb[3]));
      *(u16*)(smem + TAB_SB + tid * 2) = (u16)(F2U(rbf(tb[0] + tb[1])) >> 16);
      *(u16*)(smem + TAB_SB + 896 + tid * 2) = (u16)(F2U(be) >> 16);
    }
    asm volatile("s_waitcnt lgkmcnt(0)\n\ts_barrier" ::: "memory");
    {
      const int ch = (lane >> 2) & 1;
      const float gac = ch ? ga[1] : ga[0];
      const float b2 = ch ? b2v[1] : b2v[0];
      const char* rp = smem + CV_RED + (ch * 40 + w * 5) * 1024 + (lane & 1) * 512 + ((2 * (lane >> 3) + ((lane >> 1) & 1)) ^ ((lane & 1) << 3)) * 16;
      u32x4 o, ob;
#pragma unroll
      for (int i = 0; i < 2; ++i) {
        const char* q = rp + i * 256;
        const f32x4 y = (*(const f32x4*)q + *(const f32x4*)(q + 1024)) + (*(const f32x4*)(q + 2048) + *(const f32x4*)(q + 3072));
        const f32x4 bs = *(const f32x4*)(q + 4096);
        o[2 * i] = pkbf(__builtin_fmaf(y[0], gac, bs[0]), __builtin_fmaf(y[1], gac, bs[1]));
        o[2 * i + 1] = pkbf(__builtin_fmaf(y[2], gac, bs[2]), __builtin_fmaf(y[3], gac, bs[3]));
      }
#pragma unroll
      for (int i = 0; i < 4; ++i) ob[i] = pkbf(bflo(o[i]) * b2, bfhi(o[i]) * b2);
      const int so = ((8 * w + (lane >> 3)) * 448 + 2 * b) * 64 + (lane & 7) * 16;
      __builtin_amdgcn_raw_buffer_store_b128(ob, rs, (int)WS_XB + so, 0, 16);
      __builtin_amdgcn_raw_buffer_store_b128(o, rs, (int)WS_X1T + so, 0, 16);
    }
    SB();
    NP0S(10);
  }
  u32x4 w2f[28];
  const int ke = w ^ ((dg & 1) << 2);
  const int rot = (r2 & 1) * 7168;
  const char* w2src = ws + WS_W2F + ((size_t)(2 * dg) * 112 + 14 * ke) * 1024;
  const char* wsrc = ws + WS_W13 + (size_t)ft * 28672;
#define WDEF(lo, hi_) static_for<lo, hi_>([&](auto ic) __attribute__((always_inline)) { constexpr int i = decltype(ic)::value; wf[i] = gload<(i & 3) * 1024>(voff, wsrc + (i & ~3) * 1024); })
  __builtin_amdgcn_s_waitcnt(0x0F72);
  WDEF(W_N2, W_N3);
  asm volatile("s_waitcnt vmcnt(%0)\n\ts_barrier" :: "i"(W_N3 - W_N2) : "memory");
  STAMP(3);
  if (w == 0 && lane < 16) __hip_atomic_fetch_add(ctl + C1_CNT + (lane * 9 + xcd) * 32, 1u, RLX_AGENT);
  SB();
  WDEF(W_N3, 28);
  f32x16 accw;
#pragma unroll
  for (int r = 0; r < 16; ++r) accw[r] = 0.f;
  const char* tsb = smem + TAB_SB + (G & 1) * 896 + hi * 16;
#pragma unroll
  for (int p = 0; p < W_N2; ++p) accw = MFMA32(*(const u32x4*)(tsb + p * 32), wf[p], accw);
  SB();
  if (w == 0) wait_cnt<9, 6>(ctl + C1_CNT + ((b & 15) * 9 + lane) * 32, lane == 8 ? 32u : 28u, pl0, pl1, pl2);
  asm volatile("s_barrier" ::: "memory");
  STAMP(4);
  const char* xsrc = ws + WS_XB + (size_t)(4 * rt) * 28672 + w * 1024;
#pragma unroll
  for (int i = 0; i < X_N0; ++i) glds16(voff, xsrc + i * 8192, lds0 + w * 1024 + i * 8192);
  {
    asm volatile("s_waitcnt vmcnt(%0)" :: "i"(X_N0) : "memory");
    PIN14(wf, 14);
#pragma unroll
    for (int p = W_N2; p < 28; ++p) accw = MFMA32(*(const u32x4*)(tsb + p * 32), wf[p], accw);
    const float us = accw[8], vs = accw[0];
    const float uh = rbf(us), vh = rbf(vs);
    wex = u32x4{hi ? 0u : pkbf(uh, us - uh), hi ? 0u : pkbf(vh, vs - vh), 0u, 0u};
    pin(wex);
  }
  GAS u32* c2p = ctl + C2_CNT + rt * 1024 + (ft / 14) * 32;
  GAS u32* c2e = ctl + C2_CNT + te * 256 + ke * 32;
  GAS u32* c2l = ctl + C2_CNT + tl * 256 + lane * 32;
  u32 pv = 14;
#define ANNOUNCE(n, tt) asm volatile("s_waitcnt vmcnt(%3)\n\ts_mov_b64 exec, 1\n\tglobal_atomic_add %0, %1, %2 offset:%4\n\ts_mov_b64 exec, -1" :: "v"(0), "v"(1), "s"(c2p), "i"(n), "i"((tt) * 1024) : "memory")
  const char* w2b[4] = {w2src + rot + 3072, w2src + 114688 + rot + 3072, w2src + 7168 - rot + 3072, w2src + 114688 + 7168 - rot + 3072};
  asm volatile("" : "+s"(w2b[0]), "+s"(w2b[1]), "+s"(w2b[2]), "+s"(w2b[3]));
  {
    u32 rb = lds0 + (8 * hi + qq) * 64 + (G & 1) * 32 + pp * 8;
    GAS unsigned long long* sgp = (GAS unsigned long long*)(ws + ST2_OFF) + 128 * rt + (lane & 31);

    u32x4 xf[4];
    f32x16 acc[2];
    float nal, s2, ex[8], pr[8];
    unsigned long long sg = 0;
#define G1_READ(q) do { const LAS char* p_ = (const LAS char*)(size_t)rb + (q) * 1024; u32x2 lo = trread3(p_), hh = trread3(p_ + 256); xf[(q) & 3] = u32x4{lo.x, lo.y, hh.x, hh.y}; } while (0)
#define SG_WAIT(g, ptr) do { _Pragma("unroll 1") for (unsigned spins = 0; spins < SPIN_CAP; ++spins) { if (__builtin_amdgcn_ballot_w64((u32)((g) >> 32) != 1u) == 0) break; sgreload(g, ptr); } } while (0)
#define SG_APPLY(g, A) do { const u32 st_ = (u32)(g); const u32 mm_ = __builtin_amdgcn_perm(st_, st_, 0x01000100u), ss_ = __builtin_amdgcn_perm(st_, st_, 0x03020302u); \
      const float sd_ = bfhi(st_); nal = -1.4426950408889634f * __builtin_amdgcn_rcpf(sd_); s2 = sd_ * sd_; \
      pinf(nal); pinf(s2); A = MFMA32(wex, (u32x4{mm_, ss_, mm_, ss_}), A); } while (0)
#define EPI_STAGE(A, st, v) do { if constexpr ((st) == 0) { ex[v] = A[v] * nal; pr[v] = A[v] * A[8 + (v)]; pinf(pr[v]); } \
      if constexpr ((st) == 1) ex[v] = __builtin_amdgcn_exp2f(ex[v]); if constexpr ((st) == 2) ex[v] = __builtin_fmaf(ex[v], s2, s2); \
      if constexpr ((st) == 3) ex[v] = __builtin_amdgcn_rcpf(ex[v]); if constexpr ((st) == 4) { pr[v] *= ex[v]; pinf(pr[v]); } else pinf(ex[v]); } while (0)
#define EPI_STORE(tt) __builtin_amdgcn_raw_buffer_store_b128(u32x4{pkbf(pr[0], pr[1]), pkbf(pr[2], pr[3]), pkbf(pr[4], pr[5]), pkbf(pr[6], pr[7])}, rs, hbase, (tt) * 112 * 1024, 16)
    asm volatile("s_waitcnt vmcnt(%0)\n\ts_barrier" :: "i"(X_N0 - 4) : "memory");
    POLL_KEEP();
    NP0S(12);
    G1_READ(0); G1_READ(1);
    static_for<0, 112>([&](auto nc) __attribute__((always_inline)) {
      constexpr int n = decltype(nc)::value, t = n / 28, q = n % 28;
      constexpr int wl = g1_w2(n);
      if constexpr (t >= 1 && q == g1_e0(t) - 2) {
        asm volatile("s_waitcnt vmcnt(%1)" : "+v"(sg) : "i"(g1_vm(g1_sg(t - 1), n)) : "memory");
        SG_WAIT(sg, sgp + 32 * (t - 1));
      }
      if constexpr (n == 24) asm volatile("s_waitcnt vmcnt(0)\n\ts_barrier" ::: "memory");
      if constexpr (t >= 2 && q == g1_an(t)) ANNOUNCE(g1_vm(28 * (t - 1) + g1_st(t - 1), n), t - 2);
      if constexpr (n == 108) POLL_ISSUE(pv, c2e, 1);
      if constexpr (n == g1_sg(t)) sgload(sg, sgp + 32 * t);
      if constexpr (q == 26 && (t & 1)) { rb += 57344; asm volatile("" : "+v"(rb)); }
      SB();
      if constexpr ((n & 1) == 0) {
        if constexpr (n < 110) { G1_READ((n + 2) % 56); G1_READ((n + 3) % 56); SB(); __builtin_amdgcn_s_waitcnt(0xC47F); }
        else __builtin_amdgcn_s_waitcnt(0xC07F);
      }
      SB();
      if constexpr (q == 0) { _Pragma("unroll") for (int r = 0; r < 16; ++r) acc[t & 1][r] = 0.f; }
      acc[t & 1] = MFMA32(wf[q], xf[q & 3], acc[t & 1]);
      if constexpr (t >= 1) {
        if constexpr (q == g1_e0(t) - 2) SG_APPLY(sg, acc[(t - 1) & 1]);
        static_for<0, 5>([&](auto sc) __attribute__((always_inline)) {
          constexpr int st = decltype(sc)::value, g = q - g1_e0(t) - st, v = g / 2;
          if constexpr (g >= 0 && (g & 1) == 0 && v < 8) EPI_STAGE(acc[(t - 1) & 1], st, v);
        });
        if constexpr (q == g1_st(t)) { SB(); EPI_STORE(t - 1); }
      }
      if constexpr (n + X_N0 < 14) { SB(); glds16(voff, xsrc + (n + X_N0) * 8192, lds0 + w * 1024 + (n + X_N0) * 8192); }
      if constexpr (n + X_N0 == 14) { SB(); glds16(voff, ws + WS_X1T + ((size_t)(w >> 2 ? tl : te) * 448 + 64 * dg) * 64 + (w & 3) * 1024, lds0 + G2_X1 + w * 1024); }
      if constexpr (wl >= 0) { SB(); w2f[wl] = gload<((wl >> 1) % 7) * 1024 - 3072>(voff, w2b[(wl & 1) + (wl >= 14 ? 2 : 0)]); }
    });
    SB();
    STAMP(5);
    asm volatile("s_waitcnt vmcnt(%1)" : "+v"(sg) : "i"(g1_vm(g1_sg(3), 112)) : "memory");
    SG_WAIT(sg, sgp + 96);
    SG_APPLY(sg, acc[1]);
    static_for<0, 5>([&](auto sc) __attribute__((always_inline)) {
      static_for<0, 8>([&](auto vc) __attribute__((always_inline)) { EPI_STAGE(acc[1], decltype(sc)::value, decltype(vc)::value); });
    });
    EPI_STORE(3);
    SB();
  }
  {
    u32x4 gq[2];
    u32x4 hf[2][14];
    const char* hsrc[2] = {ws + WS_HF + ((size_t)te * 112 + 14 * ke) * 1024, ws + WS_HF + ((size_t)tl * 112 + 14 * ke) * 1024};
#define H_LOAD(j, lo, hi_) static_for<lo, hi_>([&](auto ic) __attribute__((always_inline)) { constexpr int i = decltype(ic)::value; hf[j][i] = gload<((i % 7) & 3) * 1024>(voff, hsrc[j] + (i >= 7 ? 7168 - rot : rot) + ((i % 7) >> 2) * 4096); })
    pl0 = pl1 = pl2 = 0;
#define ANNOUNCE23(n) asm volatile("s_waitcnt vmcnt(%3)\n\ts_mov_b64 exec, 3\n\tglobal_atomic_add %0, %1, %2 offset:2048\n\ts_mov_b64 exec, -1" :: "v"(lane << 10), "v"(1), "s"(c2p), "i"(n) : "memory")
    asm volatile("s_waitcnt vmcnt(1)\n\ts_barrier" : "+v"(pv) :: "memory");
    const bool slow = __builtin_amdgcn_readfirstlane(pv) < 14u;
    if (slow) {
      ANNOUNCE23(0);
      wait_cnt<1, 6>(c2e, 14u, pl0, pl1, pl2);
    }
    STAMP(6);
    { const char* gp = ws + WS_MODC + (64 * dg + 32 * dt + 8 * kq) * 8; gq[0] = gload<0>(hi * 32, gp); gq[1] = gload<16>(hi * 32, gp); }
    H_LOAD(0, 0, G2_B1);
    if (!slow) ANNOUNCE23(G2_B1 + 2);
    static_for<0, 2>([&](auto jc) __attribute__((always_inline)) {
      constexpr int j = decltype(jc)::value;
      f32x16 acc[2];
#pragma unroll
      for (int c = 0; c < 2; ++c)
#pragma unroll
        for (int r = 0; r < 16; ++r) acc[c][r] = 0.f;
      static_for<0, 14>([&](auto sc) __attribute__((always_inline)) {
        constexpr int s = decltype(sc)::value;
        asm volatile("s_waitcnt vmcnt(%1)" : "+v"(hf[j][s]) : "i"((s + G2_B1 - 1 < 13 ? G2_B1 - 1 : 13 - s) + (j == 1 && s < G2_B1 ? 1 : 0)) : "memory");
        if constexpr (s == 0) POLL_KEEP();
        if constexpr (j == 0 && s == 0) { PIN14(w2f, 0); PIN14(w2f, 14); pin(gq[0]); pin(gq[1]); pl0 = pl1 = pl2 = 14u; }
        if constexpr (j == 0 && s == G2_PL) { if (w == 0) POLL_ISSUE(pl0, c2l, 255); }
        if constexpr (j == 0 && s == 13) { if (w == 0) POLL_ISSUE(pl1, c2l, 255); }
        acc[0] = MFMA32(w2f[2 * s], hf[j][s], acc[0]);
        acc[1] = MFMA32(w2f[2 * s + 1], hf[j][s], acc[1]);
        SB();
        if constexpr (s + G2_B1 < 14) H_LOAD(j, s + G2_B1, s + G2_B1 + 1);
      });
      if constexpr (j == 0) {
      } else {
        STAMP(7);
      }
      char* red = smem + j * 65536;
#pragma unroll
      for (int P = 0; P < 8; ++P)
        *(f32x4*)(red + (P * 8 + w) * 1024 + lane * 16) = f32x4{acc[P & 1][4 * (P >> 1)], acc[P & 1][4 * (P >> 1) + 1], acc[P & 1][4 * (P >> 1) + 2], acc[P & 1][4 * (P >> 1) + 3]};
      if constexpr (j == 0) {
        if (w == 0) poll_loop<8>(c2l, 14u, pl0, pl1, pl2);
        asm volatile("s_waitcnt lgkmcnt(0)\n\ts_barrier" ::: "memory");
        NP0S(13);
        H_LOAD(1, 0, G2_B1);
      }
      {
        const float gbv[4] = {rbf(U2F(gq[0].x) + U2F(gq[0].y)), rbf(U2F(gq[0].z) + U2F(gq[0].w)), rbf(U2F(gq[1].x) + U2F(gq[1].y)), rbf(U2F(gq[1].z) + U2F(gq[1].w))};
        if constexpr (j == 1) asm volatile("s_waitcnt lgkmcnt(0)\n\ts_barrier" ::: "memory");
        {
          constexpr int jj = j;
          const char* rp = smem + jj * 65536 + w * 8192 + lane * 16;
          const f32x4 y = ((*(const f32x4*)rp + *(const f32x4*)(rp + 1024)) + (*(const f32x4*)(rp + 2048) + *(const f32x4*)(rp + 3072))) +
                          ((*(const f32x4*)(rp + 4096) + *(const f32x4*)(rp + 5120)) + (*(const f32x4*)(rp + 6144) + *(const f32x4*)(rp + 7168)));
          const u32x2 xr = trread(smem + G2_X1 + jj * 4096 + (32 * dt + 8 * kq + 4 * hi + qq) * 64 + (G & 1) * 32 + pp * 8);
          u32x2 o; o.x = pkbf(__builtin_fmaf(y[0], gbv[0], bflo(xr.x)), __builtin_fmaf(y[1], gbv[1], bfhi(xr.x)));
          o.y = pkbf(__builtin_fmaf(y[2], gbv[2], bflo(xr.y)), __builtin_fmaf(y[3], gbv[3], bfhi(xr.y)));
          ostore(o, out + (size_t)(32 * (jj ? tl : te) + (lane & 31)) * D_ + 64 * dg + 32 * dt + 8 * kq + 4 * hi);
        }
      }
      SB();
    });
  }
  NP0S(15);
  STAMP_OUT(stamps, 1024 + b);
  STAMP_OUT2(stamps, 2048 + b);
}

extern "C" void kernel_launch(void* const* d_in, const int* in_sizes, int n_in, void* d_out, int out_size, void* d_ws, size_t ws_size,
                              hipStream_t stream) {
  const u16* x = (const u16*)d_in[0]; const u16* logA = (const u16*)d_in[1]; const u16* Aim = (const u16*)d_in[2];
  const u16* Cr = (const u16*)d_in[3]; const u16* Ci = (const u16*)d_in[4]; const u16* logdt = (const u16*)d_in[5]; const u16* Dsk = (const u16*)d_in[6];
  const u16* w1 = (const u16*)d_in[8]; const u16* w3 = (const u16*)d_in[9]; const u16* w2 = (const u16*)d_in[10];
  const u16* c = (const u16*)d_in[11]; const u16* wm = (const u16*)d_in[12];
  char* ws = (char*)d_ws;
  unsigned long long* stamps = (unsigned long long*)(ws + (64u << 20));
  kf_main<<<256, 512, 0, stream>>>(x, logA, Aim, Cr, Ci, logdt, Dsk, w1, w3, w2, c, wm, ws, (u16*)d_out, stamps);
}
```

```cpp
#define DIAG_A 0
#define DIAG_B 0
#define DIAG_WG 100
#include <hip/hip_runtime.h>

typedef unsigned short u16;
typedef unsigned u32;
typedef u32 u32x4 __attribute__((ext_vector_type(4)));
typedef u32 u32x2 __attribute__((ext_vector_type(2)));
typedef short s16x4 __attribute__((ext_vector_type(4)));
typedef __bf16 bf16x8 __attribute__((ext_vector_type(8)));
typedef __bf16 bf16x2 __attribute__((ext_vector_type(2)));
typedef _Float16 f16x8 __attribute__((ext_vector_type(8)));
typedef _Float16 f16x2 __attribute__((ext_vector_type(2)));
typedef float f32x2 __attribute__((ext_vector_type(2)));
typedef float f32x4 __attribute__((ext_vector_type(4)));
typedef float f32x16 __attribute__((ext_vector_type(16)));
#define DI __device__ __forceinline__
#define U2F(x) __builtin_bit_cast(float, (u32)(x))
#define F2U(x) __builtin_bit_cast(u32, (float)(x))
#define I2F(x) __builtin_bit_cast(float, (int)(x))
#define F2I(x) __builtin_bit_cast(int, (float)(x))
#if defined(__HIP_DEVICE_COMPILE__)
#define NOPK __attribute__((target("no-packed-fp32-ops")))
#else
#define NOPK
#endif
#define LDSP(T, p) ((__attribute__((address_space(3))) T*)(p))
#define GAS __attribute__((address_space(1)))
#define RLX_AGENT __ATOMIC_RELAXED, __HIP_MEMORY_SCOPE_AGENT
#define SB() __builtin_amdgcn_sched_barrier(0)
#define LDIN(p) __builtin_nontemporal_load(p)
template <int V> struct IC { static constexpr int value = V; };
template <int I, int N, class F> __device__ __forceinline__ void static_for(F&& f) { if constexpr (I < N) { f(IC<I>{}); static_for<I + 1, N>(f); } }

constexpr int L_ = 2048, D_ = 448, N_ = 256, F_ = 1792;

constexpr int EP_E = 0;
constexpr int EP_R = 64;
constexpr int C0_CNT = 1024;
constexpr int C1_CNT = 9216;
constexpr int C2_CNT = 14336;
constexpr int ST2_OFF = 122880;
constexpr int ZERO_LO = 36864, ZERO_HI = 139264;
constexpr size_t WS_MODA = 139264;
constexpr size_t WS_MODB = WS_MODA + 448 * 32;
constexpr size_t WS_MODC = WS_MODB + 448 * 16;
constexpr size_t WS_ST1 = 167936;
constexpr size_t WS_XT = WS_ST1 + 16384;
constexpr size_t WS_X1T = WS_XT + 64 * 448 * 64;
constexpr size_t WS_XB = WS_X1T + 64 * 448 * 64;
constexpr size_t WS_W13 = WS_XB + 64 * 448 * 64;
constexpr size_t WS_W2F = WS_W13 + 112 * 28 * 1024;
constexpr size_t WS_HF = WS_W2F + 14 * 112 * 1024;
constexpr size_t WS_END = WS_HF + 64 * 112 * 1024;

DI float bflo(u32 v) { return U2F(v << 16); }
DI float bfhi(u32 v) { return U2F(v & 0xffff0000u); }
DI float bf2f(u16 v) { return U2F(((u32)v) << 16); }
DI u32 pkbf(float lo, float hi) { f32x2 p = {lo, hi}; return __builtin_bit_cast(u32, __builtin_convertvector(p, bf16x2)); }
DI u32 pkh(f32x2 p) { return __builtin_bit_cast(u32, __builtin_convertvector(p, f16x2)); }
DI float rbf(float f) { return bflo(pkbf(f, f)); }
DI float dot2(u32 a, u32 b, float c) { return __builtin_amdgcn_fdot2_f32_bf16(__builtin_bit_cast(bf16x2, a), __builtin_bit_cast(bf16x2, b), c, false); }
DI float fexp(float x) { return __builtin_amdgcn_exp2f(x * 1.4426950408889634f); }
DI float silu_bf(float c) { return rbf(c * __builtin_amdgcn_rcpf(1.f + fexp(-c))); }
template <int CTRL> DI float dppf(float x) { return I2F(__builtin_amdgcn_update_dpp(0, F2I(x), CTRL, 0xf, 0xf, false)); }
DI float sum16(float x) { x += dppf<0x128>(x); x += dppf<0x124>(x); x += dppf<0x122>(x); x += dppf<0x121>(x); return x; }
DI float max16(float x) { x = __builtin_fmaxf(x, dppf<0x128>(x)); x = __builtin_fmaxf(x, dppf<0x124>(x)); x = __builtin_fmaxf(x, dppf<0x122>(x)); x = __builtin_fmaxf(x, dppf<0x121>(x)); return x; }
DI float add32(float x) { auto r = __builtin_amdgcn_permlane32_swap(F2U(x), F2U(x), false, false); return U2F(r[0]) + U2F(r[1]); }
DI float add16(float x) { auto r = __builtin_amdgcn_permlane16_swap(F2U(x), F2U(x), false, false); return U2F(r[0]) + U2F(r[1]); }
DI float max64(float x) {
  x = max16(x);
  auto r = __builtin_amdgcn_permlane16_swap(F2U(x), F2U(x), false, false);
  x = __builtin_fmaxf(U2F(r[0]), U2F(r[1]));
  auto q = __builtin_amdgcn_permlane32_swap(F2U(x), F2U(x), false, false);
  return __builtin_fmaxf(U2F(q[0]), U2F(q[1]));
}
DI u32x2 trread(const char* p) { return __builtin_bit_cast(u32x2, __builtin_amdgcn_ds_read_tr16_b64_v4i16(LDSP(s16x4, p))); }
DI u32x2 trread3(const __attribute__((address_space(3))) char* p) { return __builtin_bit_cast(u32x2, __builtin_amdgcn_ds_read_tr16_b64_v4i16((__attribute__((address_space(3))) s16x4*)p)); }
DI void pinf(float& v) { asm volatile("" : "+v"(v)); }
DI void pin(u32x4& v) { asm volatile("" : "+v"(v)); }
DI void ostore(u32x2 v, u16* p) { *(u32x2*)p = v; }
DI f32x2 cmulp(f32x2 a, f32x2 z, f32x2 zs) { return __builtin_elementwise_fma(a.yx, zs, a * z.xx); }

#define STAMPS
DI unsigned long long RT() { unsigned long long t; SB(); asm volatile("s_memrealtime %0\n\ts_waitcnt lgkmcnt(0)" : "=s"(t) :: "memory"); SB(); return t; }
#define STAMP_DECL unsigned long long stv[16] = {0, 0, 0, 0, 0, 0, 0, 0, 0, 0, 0, 0, 0, 0, 0, 0}
#define STAMP(i) stv[i] = RT()
DI unsigned long long CT() { unsigned long long t; SB(); asm volatile("s_memtime %0\n\ts_waitcnt lgkmcnt(0)" : "=s"(t) :: "memory"); SB(); return t; }
#define CSTAMP(i) ctv[i] = CT()
#define STAMP_OUT(buf, slot) do { if (__builtin_amdgcn_workitem_id_x() == 0) { _Pragma("unroll") for (int i_ = 0; i_ < 8; ++i_) (buf)[(slot) * 8 + i_] = stv[i_]; } } while (0)
#define STAMP_OUT2(buf, slot) do { if (__builtin_amdgcn_workitem_id_x() == 0) { _Pragma("unroll") for (int i_ = 0; i_ < 8; ++i_) (buf)[(slot) * 8 + i_] = stv[8 + i_]; } } while (0)

#define MFMA32(a, b, c) __builtin_amdgcn_mfma_f32_32x32x16_bf16(__builtin_bit_cast(bf16x8, a), __builtin_bit_cast(bf16x8, b), c, 0, 0, 0)
#define ST16(v, off) __builtin_amdgcn_raw_buffer_store_b128(v, rs, (int)(off), 0, 16)
template <int IMM> DI u32x4 gload(u32 voff, const char* sbase) {
  u32x4 r; asm volatile("global_load_dwordx4 %0, %1, %2 offset:%3 sc1" : "=v"(r) : "v"(voff), "s"(sbase), "i"(IMM)); return r;
}
DI void glds16(u32 voff, const char* sbase, u32 ldsaddr) {
  asm volatile("s_mov_b32 m0, %2\n\ts_nop 0\n\tglobal_load_lds_dwordx4 %0, %1 sc1" :: "v"(voff), "s"(sbase), "s"(ldsaddr) : "memory");
}
constexpr unsigned SPIN_CAP = 1u << 16;
template <int OFF> DI u32 ldu16(const u16* p) { u32 r; asm volatile("global_load_ushort %0, %1, off offset:%2" : "=v"(r) : "v"(p), "i"(OFF) : "memory"); return r; }
template <int OFF> DI u32x4 ldnt(const u16* p) { u32x4 r; asm volatile("global_load_dwordx4 %0, %1, off offset:%2 nt" : "=v"(r) : "v"(p), "i"(OFF) : "memory"); return r; }
DI void sgload(unsigned long long& g, GAS unsigned long long* p) { asm volatile("global_load_dwordx2 %0, %1, off sc1" : "+v"(g) : "v"(p) : "memory"); }
DI void sgwait(unsigned long long& g) { asm volatile("s_waitcnt vmcnt(1)" : "+v"(g) :: "memory"); }
DI void sgreload(unsigned long long& g, GAS unsigned long long* p) { asm volatile("global_load_dwordx2 %0, %1, off sc1\n\ts_waitcnt vmcnt(0)" : "+v"(g) : "v"(p) : "memory"); }
#define POLL_ISSUE(v, p, m) asm volatile("s_mov_b64 exec, %3\n\tglobal_atomic_or %0, %1, %2, off sc0\n\ts_mov_b64 exec, -1" : "+v"(v) : "v"(p), "v"(0), "i"(m) : "memory")
template <int NL> DI void poll_loop(GAS u32* p, u32 want, u32& v0, u32& v1, u32& v2) {
#pragma unroll 1
  for (unsigned spins = 0; spins < SPIN_CAP; ++spins) {
    POLL_ISSUE(v2, p, (1 << NL) - 1);
    asm volatile("s_waitcnt vmcnt(2)" : "+v"(v0) :: "memory");
    if (__builtin_amdgcn_ballot_w64(v0 < want) == 0) break;
    POLL_ISSUE(v0, p, (1 << NL) - 1);
    asm volatile("s_waitcnt vmcnt(2)" : "+v"(v1) :: "memory");
    if (__builtin_amdgcn_ballot_w64(v1 < want) == 0) break;
    POLL_ISSUE(v1, p, (1 << NL) - 1);
    asm volatile("s_waitcnt vmcnt(2)" : "+v"(v2) :: "memory");
    if (__builtin_amdgcn_ballot_w64(v2 < want) == 0) break;
  }
}
template <int NL, int SLP> DI void wait_cnt(GAS u32* p, u32 want, u32& v0, u32& v1, u32& v2) {
  v0 = want; v1 = want; v2 = want;
  POLL_ISSUE(v0, p, (1 << NL) - 1); __builtin_amdgcn_s_sleep(SLP);
  POLL_ISSUE(v1, p, (1 << NL) - 1); __builtin_amdgcn_s_sleep(SLP);
  poll_loop<NL>(p, want, v0, v1, v2);
}
#define POLL_KEEP() asm volatile("" :: "v"(pl0), "v"(pl1), "v"(pl2) : "memory")
#define WAIT_VM0() __builtin_amdgcn_s_waitcnt(0x0F70)
#define BAR() __builtin_amdgcn_s_barrier()

constexpr int CV_DSTR = 4 * 4160 + 4608;
constexpr int CV_RED = 2 * CV_DSTR;
constexpr int G1_ST = 4 * 28672;
constexpr int G2_RED = 4 * 28672;
constexpr int G2_X1 = 18 * 8192;
constexpr int TAB_GB = G2_X1 + 8192;
constexpr int TAB_SB = TAB_GB + 256;
constexpr int ARR_CNT = TAB_SB + 1792;
constexpr int KF_LDS = ARR_CNT + 16;
constexpr int KG_REG = 32 * 272;
constexpr int KG_IMG = 2 * CV_DSTR;
constexpr int TASK_LDS = KG_IMG + 8 * KG_REG;
constexpr int KG_TAB = TASK_LDS + 30464;
#define HSLOT stv[14]
#define WSLOT stv[0]
#define W2_N0 13
#define W13_SLP 1
#define G1MAP 0
#define CODEPF 29
#define X_N0 6
#define P0S(i)
#define NP0S(i) STAMP(i)
#define G2_B1 6
#define G2_PL 10
#define EPI0 7
#define W_N0 2
#define W_NM 18
#define W_ST 4
#define W_NB 2
#define W_N1 (W_N0 + W_NB)
#define W_N2 (W_N1 + W_NM)
#define W_ND 2
#define W_N3 (W_N2 + W_ND)
#define SC_SIG 6
#define SC_POLL 21
#define LAS __attribute__((address_space(3)))
#define PIN14(a, o) asm volatile("" : "+v"(a[o]), "+v"(a[o + 1]), "+v"(a[o + 2]), "+v"(a[o + 3]), "+v"(a[o + 4]), "+v"(a[o + 5]), "+v"(a[o + 6]), "+v"(a[o + 7]), \
    "+v"(a[o + 8]), "+v"(a[o + 9]), "+v"(a[o + 10]), "+v"(a[o + 11]), "+v"(a[o + 12]), "+v"(a[o + 13]) :: "memory")

constexpr int g1_e0(int t) { return t == 1 ? EPI0 : 3; }
constexpr int g1_st(int t) { return g1_e0(t) + 19; }
constexpr int g1_an(int t) { return t == 2 ? 18 : 14; }
constexpr int g1_w2(int n) {
  if (n >= 25 && n < 25 + 14 * 4 && (n - 25) % 4 == 0) return (n - 25) / 4;
  if (n >= 84 && n < 84 + 23) { const int q = n - 84; if (q % 3 != 2) return 14 + q - q / 3 < 28 ? 14 + q - q / 3 : -1; }
  return -1;
}
constexpr int g1_sg(int t) { return t == 0 ? 24 : 28 * t + 10; }
constexpr int g1_vm(int a, int b) {
  int c = 0;
  for (int n = a; n < b; ++n) {
    const int t = n / 28, q = n % 28;
    if (n > a) { if (t >= 2 && q == g1_an(t)) ++c; if (n == 108) ++c; if (n == g1_sg(t)) ++c; }
    if (t >= 1 && q == g1_st(t) && n != a) ++c;
    if (g1_w2(n) >= 0) ++c;
  }
  return c;
}
__global__ __launch_bounds__(512) void kf_main(const u16* __restrict__ x, const u16* __restrict__ logA, const u16* __restrict__ Aim,
                                               const u16* __restrict__ Cr, const u16* __restrict__ Ci, const u16* __restrict__ logdt,
                                               const u16* __restrict__ Dsk, const u16* __restrict__ w1, const u16* __restrict__ w3,
                                               const u16* __restrict__ w2, const u16* __restrict__ cvec, const u16* __restrict__ wmod,
                                               char* __restrict__ ws, u16* __restrict__ out, unsigned long long* stamps) {
  __shared__ __attribute__((aligned(16))) char smem[KF_LDS];
  const int tid = __builtin_amdgcn_workitem_id_x(), lane = tid & 63;
  const int w = __builtin_amdgcn_readfirstlane(tid >> 6);
  const int b = __builtin_amdgcn_workgroup_id_x(), xcd = b & 7, idx = b >> 3;
  const int hi = lane >> 5;
  const int G = lane >> 4, qq = (lane & 15) >> 2, pp = lane & 3;
  const u32 voff = lane * 16;
  GAS u32* ctl = (GAS u32*)ws;
  auto rs = __builtin_amdgcn_make_buffer_rsrc(ws, 0, (int)WS_END, 0x00020000);
  const u32 lds0 = (u32)(size_t)LDSP(char, smem);
  STAMP_DECL;
  STAMP(0);
  u32 pl0 = 0, pl1 = 0, pl2 = 0;
  u32x4 cpf;
  { const char* pc; asm volatile("s_getpc_b64 %0" : "=s"(pc));
    const u32 o = (idx < CODEPF ? idx : 0) * 1024 + lane * 16;
    if (w == 7) asm volatile("global_load_dwordx4 %0, %1, %2" : "=v"(cpf) : "v"(o), "s"(pc) : "memory"); }
  const bool mainwg = b < 224;
  const int dl = w >> 2, q4 = w & 3, t256 = tid & 255;
  auto arrive0 = [&]() __attribute__((always_inline)) {
    u32 old;
    asm volatile("s_waitcnt vmcnt(0)\n\ts_mov_b64 exec, 1\n\tds_add_rtn_u32 %0, %1, %2\n\ts_mov_b64 exec, -1\n\ts_waitcnt lgkmcnt(0)" : "=&v"(old) : "v"(lds0 + ARR_CNT), "v"(1) : "memory");
    if (__builtin_amdgcn_readfirstlane(old) == (mainwg ? 4u : (b < 249 ? 8u : 0u)) && lane < 16) __hip_atomic_fetch_add(ctl + C0_CNT + (lane * 8 + xcd) * 32, 1u, RLX_AGENT);
  };
  GAS u32* c0p = ctl + C0_CNT + ((b & 15) * 8 + lane) * 32;
  int kg_wb, kg_wst;
  u32 kga[12];
  u32 dskr = 0;
  unsigned long long epoch;
  {
    asm volatile("" :: "s"(x), "s"(logA), "s"(Aim), "s"(Cr), "s"(Ci), "s"(logdt), "s"(Dsk), "s"(w1), "s"(w3), "s"(cvec), "s"(wmod), "s"(ws));
    u32 lar = 0, air = 0, crr = 0, cir = 0, ldt = 0;
    if (mainwg) {
      const int d = 2 * b + dl;
      asm volatile("global_load_ushort %0, %6, %7 nt\n\tglobal_load_ushort %1, %6, %8 nt\n\tglobal_load_ushort %4, %11, %12\n\tglobal_load_ushort %2, %6, %9 nt\n\tglobal_load_ushort %3, %6, %10 nt\n\tglobal_load_dword %5, %13, %14"
                   : "+v"(lar), "+v"(air), "+v"(crr), "+v"(cir), "+v"(ldt), "+v"(dskr) : "v"((d * N_ + t256) * 2), "s"(logA), "s"(Aim), "s"(Cr), "s"(Ci), "v"(d * 2), "s"(logdt), "v"(b * 4), "s"(Dsk) : "memory");
    }
    SB();
    unsigned long long ep = 0;
    u32 pe = 32u;
    const int task = b >= 249 ? 2 * b - 364 + dl : (dl || b >= 246 ? -1 : (b < 134 ? b : b + 14));
    char* stg = smem + (dl ? 0 : TASK_LDS);
    u32x4 v[7]; u32 cv[7];
    const bool isx = task < 64, isw = task >= 148;
    const char* tsb; u32 tstep, tvo;
    {
      const int m = task - (isw ? 148 : 64);
      const size_t px = (size_t)x, pw = q4 >= 2 ? (size_t)w3 : (size_t)w1, pm = (size_t)wmod;
      const u32 k1 = isw ? 28672u : 128u, k0 = isw ? 1792u : 1204224u;
      const u32 off = isx ? (u32)task * 28672u : (u32)(m >> 1) * k1 + (u32)(m & 1) * k0;
      tsb = (const char*)((isx ? px : (isw ? pw : pm)) + off);
      tstep = isx ? 128u : (isw ? 256u : 172032u);
      tvo = ((t256 >> (isw ? 4 : 3)) & (isw ? 7 : 31)) * (isx ? 896u : (isw ? 3584u : 5376u)) + (t256 & (isw ? 15 : 7)) * 16;
    }
#define taskload() do { _Pragma("unroll") for (int i = 0; i < 7; ++i) asm volatile("global_load_dwordx4 %0, %1, %2 nt" : "=v"(v[i]) : "v"(tvo), "s"(tsb + i * tstep) : "memory"); } while (0)
    auto cload = [&]() __attribute__((always_inline)) {
      const u16* cp = cvec + 224 * (task & 1) + (t256 >> 3);
      static_for<0, 7>([&](auto ic) __attribute__((always_inline)) { constexpr int i = decltype(ic)::value; cv[i] = ldu16<64 * i>(cp); });
    };
    if (task >= 0 && b < 249) taskload();
    if (task >= 64 && task < 134) {
      cload();
    } else if (task < 0 && !mainwg) {
      if (dl) ST16((u32x4{0, 0, 0, 0}), ZERO_LO + (b - 224) * 4096 + t256 * 16);
      if (b == 248 && w == 0) {
        const unsigned long long e0 = __hip_atomic_load((GAS unsigned long long*)(ctl + EP_E), RLX_AGENT);
#pragma unroll
        for (int i = 0; i < 2; ++i) __hip_atomic_store(ctl + C0_CNT + i * 2048 + lane * 32, 0u, RLX_AGENT);
        WAIT_VM0();
        asm volatile("s_waitcnt vmcnt(0)" ::: "memory");
        if (lane < 16) __hip_atomic_store((GAS unsigned long long*)(ctl + EP_R + lane * 32), e0 + 1, RLX_AGENT);
      }
    }
    SB();
    P0S(15);
    if (tid < 4) *(u32*)(smem + ARR_CNT + tid * 4) = 0;
    asm volatile("s_waitcnt lgkmcnt(0)\n\ts_barrier" ::: "memory");
    SB();
    auto meet = [&](u32 want) __attribute__((always_inline)) {
      asm volatile("s_waitcnt lgkmcnt(0)\n\ts_mov_b64 exec, 1\n\tds_add_u32 %0, %1 offset:4\n\ts_mov_b64 exec, -1" :: "v"(lds0 + ARR_CNT + dl * 4), "v"(1) : "memory");
#pragma unroll 1
      for (unsigned spins = 0; spins < 16 * SPIN_CAP; ++spins) {
        if (*(volatile LAS u32*)(size_t)(lds0 + ARR_CNT + 4 + dl * 4) >= want) break;
        __builtin_amdgcn_s_sleep(1);
      }
      asm volatile("" ::: "memory");
    };
#pragma unroll
    for (int h = 0; h < 2; ++h) {
      const int lb = 2040 - 8 * t256 - 8 * h;
      const int kr = lb < 256 ? 0 : 24 - __builtin_clz(lb);
      const float mB = U2F((u32)(150 + kr) << 23) + (float)(lb < 0 ? 704 : lb);
      const u32 kpa = lds0 + (u32)(KG_TAB + dl * 1536) + 256u * kr - ((u32)(150 + kr) << 24);
#pragma unroll
      for (int e = 0; e < (h ? 4 : 8); ++e) { kga[8 * h + e] = (F2U(mB + (float)(7 - e)) << 1) + kpa; asm volatile("" : "+v"(kga[8 * h + e])); }
    }
    if (t256 == 0) *(u32x4*)(smem + KG_TAB + dl * 1536 + 1408) = u32x4{0, 0, 0, 0};
    {
      char* cp = smem + dl * CV_DSTR;
      if (mainwg) {
        if (t256 < 32) *(u32x4*)(cp + 4 * 4160 + t256 * 16) = u32x4{0, 0, 0, 0};
        else if (t256 < 40) *(u32x4*)(cp + ((t256 - 32) >> 1) * 4160 + 4096 + (t256 & 1) * 16) = u32x4{0, 0, 0, 0};
      }
    }
    const int s = lane & 31;
    {
      const int kw = s < 8 ? 0 : (s < 20 ? (s >> 2) - 1 : 3);
      int wb = s < 12 ? (s < 8 ? s : 248 + s) : (s < 16 ? 384 + ((s & 2) << 5) + (s & 1) : 32 * s);
      kg_wst = 16 >> kw;
      kg_wb = (wb > 672 ? 672 : wb) * 2 + KG_TAB + dl * 1536 + (8 * q4 + 4 * hi) * kg_wst;
    }
    char* reg = smem + KG_IMG + w * KG_REG;
    f16x8 bfr[8];
    f32x2 z8, z16; float scl = 0.f, fs = 0.f;
    unsigned long long tagr = 0;
    GAS u32* tagp = ctl + EP_R + (b & 15) * 32;
#define EP_ISSUE() asm volatile("s_mov_b64 exec, 1\n\tglobal_load_dwordx2 %1, %4, off sc1\n\tglobal_atomic_or_x2 %0, %2, %3, off sc0\n\ts_mov_b64 exec, -1" \
                     : "+&v"(tagr), "+&v"(ep) : "v"(tagp), "v"(0ull), "v"(ctl + EP_E) : "memory")
    auto epcheck = [&]() __attribute__((always_inline)) {
#pragma unroll 1
      for (unsigned spins = 0; spins < SPIN_CAP; ++spins) {
        asm volatile("s_waitcnt vmcnt(0)" : "+v"(tagr), "+v"(ep) :: "memory");
        if ((__builtin_amdgcn_ballot_w64(tagr != ep + 1) & 1) == 0) break;
        EP_ISSUE();
      }
      arrive0();
    };
    if (mainwg) {
      if (task < 0) asm volatile("s_waitcnt vmcnt(0)" ::: "memory"); else if (isx || isw) asm volatile("s_waitcnt vmcnt(7)" ::: "memory"); else asm volatile("s_waitcnt vmcnt(14)" ::: "memory");
      asm volatile("" : "+v"(lar), "+v"(air), "+v"(crr), "+v"(cir), "+v"(ldt), "+v"(dskr) :: "memory");
      P0S(12);
      if (w == 4) EP_ISSUE();
    }
    if (mainwg) {
      const float dt = rbf(fexp(bflo(ldt)));
      const float ar = -rbf(fexp(bflo(lar))) * dt * 1.4426950408889634f;
      const float ai = bflo(air) * dt;
      const float I2PH = 0.15915494f, I2PL = 6.4206382e-9f;
      const float th = ai * I2PH;
      const float tl = __builtin_fmaf(ai, I2PL, __builtin_fmaf(ai, I2PH, -th));
      auto zpow = [&](float sg) __attribute__((always_inline)) {
        float e = __builtin_amdgcn_exp2f(ar * sg);
        float rev = __builtin_fmaf(tl, sg, __builtin_amdgcn_fractf(th * sg));
        return f32x2{e * __builtin_amdgcn_cosf(rev), e * __builtin_amdgcn_sinf(rev)};
      };
      const f32x2 z1 = zpow(1.f), z256 = zpow(256.f);
      z16 = zpow(16.f);
      const f32x2 s1 = {-z1.y, z1.y}, s256 = {-z256.y, z256.y};
      u32 eb = ((crr & 0x7f80u) > (cir & 0x7f80u) ? (crr & 0x7f80u) : (cir & 0x7f80u));
      asm("s_nop 1\n\tv_max_u32_dpp %0, %0, %0 row_ror:8 row_mask:0xf bank_mask:0xf\n\ts_nop 1\n\tv_max_u32_dpp %0, %0, %0 row_ror:4 row_mask:0xf bank_mask:0xf\n\ts_nop 1\n\t"
          "v_max_u32_dpp %0, %0, %0 row_ror:2 row_mask:0xf bank_mask:0xf\n\ts_nop 1\n\tv_max_u32_dpp %0, %0, %0 row_ror:1 row_mask:0xf bank_mask:0xf\n\ts_nop 1" : "+v"(eb));
      {
        const u32 e0 = __builtin_amdgcn_readlane(eb, 0), e1 = __builtin_amdgcn_readlane(eb, 16), e2 = __builtin_amdgcn_readlane(eb, 32), e3 = __builtin_amdgcn_readlane(eb, 48);
        const u32 m0 = e0 > e1 ? e0 : e1, m1 = e2 > e3 ? e2 : e3;
        eb = (m0 > m1 ? m0 : m1) >> 7;
      }
      eb = eb < 1u ? 1u : (eb > 252u ? 252u : eb);
      scl = U2F((253u - eb) << 23);
      fs = 2.f * dt * U2F((eb + 1u) << 23);
      f32x2 lo[9], hp[9];
      lo[1] = z1; hp[1] = z256;
#pragma unroll
      for (int i = 2; i <= 8; ++i) { lo[i] = cmulp(lo[i - 1], z1, s1); hp[i] = cmulp(hp[i - 1], z256, s256); }
      const f32x2 a2 = cmulp(lo[2], z256, s256), a4 = cmulp(lo[4], z256, s256), a6 = cmulp(lo[6], z256, s256);
      const f32x2 b4 = cmulp(a4, z256, s256), c4 = cmulp(b4, z256, s256);
      const u32 col[21] = {0x00003c00u, pkh(lo[1]), pkh(lo[2]), pkh(lo[3]), pkh(lo[4]), pkh(lo[5]), pkh(lo[6]), pkh(lo[7]),
                           pkh(hp[1]), pkh(a2), pkh(a4), pkh(a6), pkh(hp[2]), pkh(b4), pkh(hp[3]), pkh(c4),
                           pkh(hp[4]), pkh(hp[5]), pkh(hp[6]), pkh(hp[7]), pkh(hp[8])};
#pragma unroll
      for (int i = 0; i < 21; ++i) *(u32*)(reg + i * 272 + lane * 4) = col[i];
      z8 = lo[8];
#pragma unroll
      for (int i = 0; i < 8; ++i) bfr[i] = *(const f16x8*)(reg + s * 272 + i * 32 + hi * 16);
      asm volatile("s_waitcnt lgkmcnt(0)" : "+v"(bfr[0]), "+v"(bfr[1]), "+v"(bfr[2]), "+v"(bfr[3]), "+v"(bfr[4]), "+v"(bfr[5]), "+v"(bfr[6]), "+v"(bfr[7]) :: "memory");
    }
    if (!mainwg) asm volatile("s_waitcnt vmcnt(0)" ::: "memory");
    asm volatile("" :: "v"(cpf));
    STAMP(8);
    if (w == 4) { if (!mainwg) EP_ISSUE(); if (mainwg || b >= 249) epcheck(); }
    if (b >= 249) { taskload(); cload(); }
#define PIN_V() asm volatile("s_waitcnt vmcnt(0)" : "+v"(v[0]), "+v"(v[1]), "+v"(v[2]), "+v"(v[3]), "+v"(v[4]), "+v"(v[5]), "+v"(v[6]) :: "memory")
    if (task >= 0 && task < 148) {
      PIN_V();
      P0S(13);
      if (task < 64) {
            const int row = t256 >> 3, p = t256 & 7;
            float sm = 0.f, sq = 0.f;
#pragma unroll
            for (int i = 0; i < 7; ++i) {
              pin(v[i]);
              *(u32x4*)(stg + row * 928 + (p + 8 * i) * 16) = v[i];
#pragma unroll
              for (int j = 0; j < 4; ++j) { sm = dot2(v[i][j], 0x3f803f80u, sm); sq = dot2(v[i][j], v[i][j], sq); }
            }
            sm += dppf<0xB1>(sm); sq += dppf<0xB1>(sq); sm += dppf<0x4E>(sm); sq += dppf<0x4E>(sq); sm += dppf<0x141>(sm); sq += dppf<0x141>(sq);
            const float m = sm * (1.f / D_);
            const float var = __builtin_fmaxf(sq * (1.f / D_) - m * m, 0.f);
            const float sd = rbf(__builtin_amdgcn_sqrtf(rbf(rbf(var) + 1e-5f)));
            const float rsd = __builtin_amdgcn_rcpf(sd);
            if (p < 2) __builtin_amdgcn_raw_buffer_store_b32(F2U(p ? -rbf(m) * rsd : rsd), rs, (int)WS_ST1 + p * 8192 + (task * 32 + row) * 4, 0, 16);
        meet(4u);
        const char* rb = stg + (8 * G + qq) * 928 + q4 * 224 + pp * 8;
#pragma unroll
        for (int i = 0; i < 7; ++i) {
          const u32x2 lo = trread(rb + i * 32), hh = trread(rb + i * 32 + 4 * 928);
          ST16((u32x4{lo.x, lo.y, hh.x, hh.y}), (int)WS_XT + task * 28672 + q4 * 7168 + (lane & 15) * 64 + G * 16 + i * 1024);
        }
      } else {
            float acc[8] = {0, 0, 0, 0, 0, 0, 0, 0};
#pragma unroll
            for (int i = 0; i < 7; ++i) {
              asm volatile("" : "+v"(cv[i]));
              const u32 sh = F2U(silu_bf(bflo(cv[i]))), sl = sh >> 16;
#pragma unroll
              for (int j = 0; j < 4; ++j) { acc[2 * j] = dot2(v[i][j], sl, acc[2 * j]); acc[2 * j + 1] = dot2(v[i][j], sh, acc[2 * j + 1]); }
            }
#pragma unroll
            for (int j = 0; j < 8; ++j) { acc[j] += dppf<0x128>(acc[j]); acc[j] = add32(acc[j]); }
            if ((lane & 40) == 0) {
              float* red = (float*)stg + (q4 * 2 + (lane >> 4)) * 64 + (lane & 7) * 8;
              *(f32x4*)red = f32x4{acc[0], acc[1], acc[2], acc[3]};
              *(f32x4*)(red + 4) = f32x4{acc[4], acc[5], acc[6], acc[7]};
            }
        meet(4u);
            if (q4 == 0) {
              const int m = task - 64, cg = m >> 1;
              float t = 0.f;
#pragma unroll
              for (int j = 0; j < 8; ++j) t += ((const float*)stg)[j * 64 + lane];
              const int sec = cg / 7, dd = (cg % 7) * 64 + lane;
              const int o1 = sec == 3 ? (int)WS_MODB + dd * 16 : (sec == 5 ? (int)WS_MODC + dd * 8 : (int)WS_MODA + dd * 32 + (sec == 4 ? 3 : sec) * 8);
              __builtin_amdgcn_raw_buffer_store_b32(F2U(t), rs, o1 + (m & 1) * 4, 0, 16);
              if (sec == 4) __builtin_amdgcn_raw_buffer_store_b32(F2U(t), rs, (int)WS_MODB + dd * 16 + 8 + (m & 1) * 4, 0, 16);
            }
      }
    }
    if (task >= 148) {
      PIN_V();
      P0S(13);
#pragma unroll
      for (int i = 0; i < 7; ++i) *(u32x4*)(stg + (t256 >> 4) * 1824 + ((t256 & 15) + 16 * i) * 16) = v[i];
      meet(4u);
      const int m = task - 148;
      const char* rb = stg + (8 * (G & 1) + qq) * 1824 + (14 * q4 + hi) * 32 + (8 * (pp & 1) + 4 * (pp >> 1)) * 2;
      const int dst = (int)WS_W13 + ((56 * (m & 1) + 14 * q4 + hi) * 28 + (m >> 2)) * 1024 + ((m >> 1) & 1) * 512 + (lane & 31) * 16;
#pragma unroll
      for (int i = 0; i < 7; ++i) {
        const u32x2 lo = trread(rb + i * 64), hh = trread(rb + i * 64 + 4 * 1824);
        ST16((u32x4{lo.x, lo.y, hh.x, hh.y}), dst + i * 57344);
      }
    }
    P0S(10);
    if (mainwg) {
        pinf(scl);
        f32x2 ra = {bflo(crr) * scl, -bflo(cir) * scl};
        f32x2 rb = cmulp(ra, z8, f32x2{z8.y, -z8.y});
        const f32x2 s16c = {z16.y, -z16.y};
#pragma unroll
        for (int r = 0; r < 32; r += 2) {
          *(u32*)(reg + r * 272 + lane * 4) = pkh(ra); *(u32*)(reg + (r + 1) * 272 + lane * 4) = pkh(rb);
          if (r < 30) { ra = cmulp(ra, z16, s16c); rb = cmulp(rb, z16, s16c); }
        }
        if (task >= 0) { WAIT_VM0(); arrive0(); STAMP(11); }
        f32x16 acc = {};
#pragma unroll
        for (int i = 0; i < 8; ++i) {
          const f16x8 af = *(const f16x8*)(reg + s * 272 + i * 32 + hi * 16);
          acc = __builtin_amdgcn_mfma_f32_32x32x16_f16(af, bfr[i], acc, 0, 0, 0);
        }
#pragma unroll
        for (int qd = 0; qd < 4; ++qd)
          *(f32x4*)(reg + qd * 1024 + lane * 16) = f32x4{acc[4 * qd] * fs, acc[4 * qd + 1] * fs, acc[4 * qd + 2] * fs, acc[4 * qd + 3] * fs};
    }
    if (mainwg) {
      if (w == 4) POLL_ISSUE(pe, c0p, 255);
      meet(dl ? 4u : 8u);
    {
      const char* rp = smem + KG_IMG + dl * 4 * KG_REG + q4 * 1024 + lane * 16;
      const f32x4 t = (*(const f32x4*)rp + *(const f32x4*)(rp + KG_REG)) + (*(const f32x4*)(rp + 2 * KG_REG) + *(const f32x4*)(rp + 3 * KG_REG));
      const u32 ta = pkbf(t[0], t[1]), tc = pkbf(t[2], t[3]);
      char* q = smem + kg_wb;
      *(u16*)q = (u16)ta; *(u16*)(q + kg_wst) = (u16)(ta >> 16); *(u16*)(q + 2 * kg_wst) = (u16)tc; *(u16*)(q + 3 * kg_wst) = (u16)(tc >> 16);
    }
    }
    if (!mainwg && b < 249) { WAIT_VM0(); arrive0(); }
    if (w == 4) {
      asm volatile("s_waitcnt vmcnt(0)" : "+v"(pe) :: "memory");
      if (mainwg) { if (__builtin_amdgcn_ballot_w64(pe < 32u) != 0) wait_cnt<8, 6>(c0p, 32u, pl0, pl1, pl2); }
      else if (b < 249) epcheck();
    }
    STAMP(14);
    epoch = ep + 1;
    asm volatile("s_waitcnt lgkmcnt(0)\n\ts_barrier" ::: "memory");
  }
  if (!mainwg) {
    const int fr = b - 224;
    if (fr < 28) {
      u32x4 v[7];
#pragma unroll
      for (int i = 0; i < 7; ++i) v[i] = LDIN((const u32x4*)(w2 + (size_t)(64 * fr + (tid >> 3)) * D_ + ((tid & 7) + 8 * i) * 8));
#pragma unroll
      for (int i = 0; i < 7; ++i) *(u32x4*)(smem + (tid >> 3) * 912 + ((tid & 7) + 8 * i) * 16) = v[i];
      asm volatile("s_waitcnt lgkmcnt(0)\n\ts_barrier" ::: "memory");
      const char* rbase = smem + (16 * (w & 3) + 8 * hi + qq) * 912 + (w >> 2) * 448 + (16 * (G & 1) + 4 * pp) * 2;
      const int dst = (int)WS_W2F + ((7 * (w >> 2)) * 112 + 4 * fr + (w & 3)) * 1024 + lane * 16;
#pragma unroll
      for (int s = 0; s < 7; ++s) {
        u32x2 lo = trread(rbase + s * 64), hh = trread(rbase + s * 64 + 4 * 912);
        __builtin_amdgcn_raw_buffer_store_b128(u32x4{lo.x, lo.y, hh.x, hh.y}, rs, dst + s * 112 * 1024, 0, 16);
      }
    }
    asm volatile("s_waitcnt vmcnt(0)\n\ts_barrier" ::: "memory");
    if (w == 4) {
      wait_cnt<8, 6>(c0p, 32u, pl0, pl1, pl2);
      asm volatile("s_waitcnt vmcnt(0)" ::: "memory");
      POLL_KEEP();
      if (b == 248 && lane == 0) __hip_atomic_store((GAS unsigned long long*)(ctl + EP_E), epoch, RLX_AGENT);
      STAMP(3);
      if (lane < 16) __hip_atomic_fetch_add(ctl + C1_CNT + (lane * 9 + 8) * 32, 1u, RLX_AGENT);
      wait_cnt<8, 6>(ctl + C1_CNT + ((b & 15) * 9 + lane) * 32, 28u, pl0, pl1, pl2);
      asm volatile("s_waitcnt vmcnt(0)" ::: "memory");
      POLL_KEEP();
    }
    asm volatile("s_barrier" ::: "memory");
    STAMP(4);
    {
      const char* xsrc = ws + WS_X1T + (size_t)(2 * fr + (w >> 2)) * 28672 + (w & 3) * 1024;
#pragma unroll
      for (int i = 0; i < 7; ++i) glds16(voff, xsrc + i * 4096, lds0 + w * 7168 + i * 1024);
      const char* sp = smem + w * 7168 + (8 * hi + qq) * 64 + (G & 1) * 32 + pp * 8;
      float s0 = 0.f, q0 = 0.f, s1 = 0.f, q1 = 0.f;
      static_for<0, 7>([&](auto ic) __attribute__((always_inline)) {
        constexpr int i = decltype(ic)::value;
        asm volatile("s_waitcnt vmcnt(%0)" :: "i"(6 - i) : "memory");
        const u32x2 lo = trread(sp + i * 1024), hh = trread(sp + i * 1024 + 256);
        s0 = dot2(lo.x, 0x3f803f80u, s0); q0 = dot2(lo.x, lo.x, q0); s1 = dot2(lo.y, 0x3f803f80u, s1); q1 = dot2(lo.y, lo.y, q1);
        s0 = dot2(hh.x, 0x3f803f80u, s0); q0 = dot2(hh.x, hh.x, q0); s1 = dot2(hh.y, 0x3f803f80u, s1); q1 = dot2(hh.y, hh.y, q1);
      });
      auto r = __builtin_amdgcn_permlane32_swap(F2U(s0 + s1), F2U(q0 + q1), false, false);
      *(float*)(smem + 65536 + w * 256 + (lane & 31) * 8 + hi * 4) = U2F(r[0]) + U2F(r[1]);
      asm volatile("s_waitcnt lgkmcnt(0)\n\ts_barrier" ::: "memory");
      if (w == 0) {
        const char* pt = smem + 65536 + hi * 1024 + (lane & 31) * 8;
        const f32x2 ts = (*(const f32x2*)pt + *(const f32x2*)(pt + 256)) + (*(const f32x2*)(pt + 512) + *(const f32x2*)(pt + 768));
        const float m = ts[0] * (1.f / D_);
        const float var = __builtin_fmaxf(ts[1] * (1.f / D_) - m * m, 0.f);
        const float sd = __builtin_amdgcn_sqrtf(rbf(rbf(var) + 1e-5f));
        __hip_atomic_store((GAS unsigned long long*)(ws + ST2_OFF) + 64 * fr + lane, (1ull << 32) | pkbf(-m, sd), RLX_AGENT);
      }
    }
    STAMP(5);
    return;
  }
  const int rt = 4 * (xcd >> 1) + idx / 7, ct = 7 * (xcd & 1) + idx % 7;
  const int ft = 8 * ct + w;
  const int r2 = 4 * xcd + idx / 7, dg = idx % 7, kq = w >> 1, dt = w & 1;
  const int te = 4 * (r2 >> 1) + (r2 & 1), tl = 4 * (r2 >> 1) + 3 - (r2 & 1);
  const int wv = (int)WS_W13 + ft * 28672 + lane * 16;
#define WLOAD(p) wf[p] = __builtin_amdgcn_raw_buffer_load_b128(rs, wv + ((p) & 3) * 1024, ((p) & ~3) * 1024, 16)

  u32x4 wf[28];
  u32x4 wex;
  const int hbase = (int)WS_HF + (4 * rt * 112 + ft) * 1024 + lane * 16;

  {
    const int a = lane & 15;
    const int l0 = 256 * w + 16 * a + 4 * G;
    const int xoff = ((l0 >> 5) * 448 + 2 * b) * 64 + (l0 & 31) * 2;
    STAMP(9);
    u32x2 xx[2]; u32x4 st[2];
    float msv = U2F(__builtin_amdgcn_raw_buffer_load_b32(rs, (int)WS_MODA + (lane & 15) * 4, b * 64, 16));
#pragma unroll
    for (int i = 0; i < 2; ++i) {
      xx[i] = __builtin_amdgcn_raw_buffer_load_b64(rs, (int)WS_XT + xoff + i * 64, 0, 16);
      st[i] = __builtin_amdgcn_raw_buffer_load_b128(rs, (int)WS_ST1 + l0 * 4, i * 8192, 16);
    }
    SB();
    {
      u32 kw[6];
#pragma unroll
      for (int e = 0; e < 6; ++e) kw[e] = (u32)*(const LAS u16*)(size_t)kga[2 * e] | ((u32)*(const LAS u16*)(size_t)kga[2 * e + 1] << 16);
      char* cp = smem + dl * CV_DSTR;
      u32 al[5];
#pragma unroll
      for (int i = 0; i < 5; ++i) al[i] = __builtin_amdgcn_alignbit(kw[i + 1], kw[i], 16);
      char* q = cp + t256 * 16;
      *(u32x4*)(q) = u32x4{kw[0], kw[1], kw[2], kw[3]};
      *(u32x4*)(q + 4160) = u32x4{al[0], al[1], al[2], al[3]};
      *(u32x4*)(q + 8320) = u32x4{kw[1], kw[2], kw[3], kw[4]};
      *(u32x4*)(q + 12480) = u32x4{al[1], al[2], al[3], al[4]};
    }
    SB();
    const f32x4 tb = __builtin_bit_cast(f32x4, __builtin_amdgcn_raw_buffer_load_b128(rs, (int)WS_MODB + (tid < 448 ? tid : 447) * 16, 0, 16));
    SB();
#pragma unroll
    for (int i = 0; i < W_N1; ++i) WLOAD(i);
    SB();
    msv = rbf(msv + dppf<0xB1>(msv));
    if (lane & 2) msv = rbf(1.f + msv);
#define MSV(i) U2F(__builtin_amdgcn_readlane(F2U(msv), i))
    float ga[2], b2v[2];
    const int swl = (lane ^ ((lane >> 5) << 3)) * 16;
#pragma unroll
    for (int i = 0; i < 2; ++i) {
      const float sa = MSV(8 * i), beta = MSV(8 * i + 2);
      ga[i] = MSV(8 * i + 4); b2v[i] = MSV(8 * i + 6);
      const float gd = ga[i] * (i ? bfhi(dskr) : bflo(dskr));
      const float xv[4] = {bflo(xx[i].x), bfhi(xx[i].x), bflo(xx[i].y), bfhi(xx[i].y)};
      float hv[4];
#pragma unroll
      for (int e = 0; e < 4; ++e)
        hv[e] = __builtin_fmaf(__builtin_fmaf(xv[e], U2F(st[0][e]), U2F(st[1][e])), beta, sa);
      *(u32x2*)(smem + i * CV_DSTR + 4 * 4160 + 512 + l0 * 2) = u32x2{pkbf(hv[0], hv[1]), pkbf(hv[2], hv[3])};
      f32x4 base;
#pragma unroll
      for (int e = 0; e < 4; ++e) base[e] = __builtin_fmaf(hv[e], gd, xv[e]);
      *(f32x4*)(smem + CV_RED + (i * 40 + w * 5 + 4) * 1024 + swl) = base;
    }
    SB();
    POLL_KEEP();
    asm volatile("s_waitcnt lgkmcnt(0)\n\ts_barrier" ::: "memory");
    STAMP(1);
    f32x4 acc[8];
#pragma unroll
    for (int t = 0; t < 8; ++t) acc[t] = f32x4{0, 0, 0, 0};
    const int im = 15 - a;
    const u32 abase = lds0 + dl * CV_DSTR + (im & 3) * 4160 + (2016 + 8 * G + 4 * (im >> 2)) * 2 - q4 * 128 - 3648;
    const u32 bbase = lds0 + dl * CV_DSTR + 4 * 4160 + (256 - 16 + 16 * a + 8 * G) * 2 - q4 * 128 - 64;
    char* dump = smem + CV_RED + (dl * 40 + q4) * 1024 + swl;
#pragma unroll
    for (int cc = 0; cc < 2; ++cc) {
      u32x2 alo[8], ahi[8]; u32x4 bfr[8];
#pragma unroll
      for (int c1 = 0; c1 < 8; ++c1)
        asm volatile("ds_read_b64 %0, %3 offset:%5\n\tds_read_b64 %1, %3 offset:%6\n\tds_read_b128 %2, %4 offset:%7"
                     : "=&v"(alo[c1]), "=&v"(ahi[c1]), "=&v"(bfr[c1])
                     : "v"(abase), "v"(bbase), "i"(3648 - (8 * c1 + cc) * 64), "i"(3656 - (8 * c1 + cc) * 64), "i"(64 + (8 * c1 - cc) * 64) : "memory");
      SB();
      if (cc == 0) __builtin_amdgcn_s_setprio(1); else __builtin_amdgcn_s_setprio(0);
#pragma unroll
      for (int t = 0; t < 8; ++t) {
        if (t == 0) asm volatile("s_waitcnt lgkmcnt(15)" : "+v"(alo[0]), "+v"(ahi[0]), "+v"(bfr[0]), "+v"(alo[1]), "+v"(ahi[1]), "+v"(bfr[1]), "+v"(alo[2]), "+v"(ahi[2]), "+v"(bfr[2]));
        if (t == 3) asm volatile("s_waitcnt lgkmcnt(9)" : "+v"(alo[3]), "+v"(ahi[3]), "+v"(bfr[3]), "+v"(alo[4]), "+v"(ahi[4]), "+v"(bfr[4]));
        if (t == 5) asm volatile("s_waitcnt lgkmcnt(0)" : "+v"(alo[5]), "+v"(ahi[5]), "+v"(bfr[5]), "+v"(alo[6]), "+v"(ahi[6]), "+v"(bfr[6]), "+v"(alo[7]), "+v"(ahi[7]), "+v"(bfr[7]));
#pragma unroll
        for (int c1 = 0; c1 <= t; ++c1) {
          acc[t] = __builtin_amdgcn_mfma_f32_16x16x32_bf16(__builtin_bit_cast(bf16x8, u32x4{alo[c1].x, alo[c1].y, ahi[c1].x, ahi[c1].y}),
                                                           __builtin_bit_cast(bf16x8, bfr[t - c1]), acc[t], 0, 0, 0);
          const int n = t * (t + 1) / 2 + c1;
          if ((n % W_ST) == W_ST - 1 && W_N1 + (36 / W_ST) * cc + n / W_ST < W_N2) { SB(); WLOAD(W_N1 + (36 / W_ST) * cc + n / W_ST); SB(); }
          if (cc == 1 && t >= 5 && c1 >= 1) {
            const int u = (t == 5 ? c1 - 1 : (t == 6 ? (c1 == 1 ? 5 : -1) : (c1 == 1 ? 6 : -1)));
            if (u >= 0) { SB(); *(f32x4*)(dump + u * 5120) = acc[u]; SB(); }
          }
        }
        SB();
      }
    }
    *(f32x4*)(dump + 7 * 5120) = acc[7];
    STAMP(2);
    if (tid < 448) {
      const float be = rbf(1.f + rbf(tb[2] + tb[3]));
      *(u16*)(smem + TAB_SB + tid * 2) = (u16)(F2U(rbf(tb[0] + tb[1])) >> 16);
      *(u16*)(smem + TAB_SB + 896 + tid * 2) = (u16)(F2U(be) >> 16);
    }
    asm volatile("s_waitcnt lgkmcnt(0)\n\ts_barrier" ::: "memory");
    {
      const int ch = (lane >> 2) & 1;
      const float gac = ch ? ga[1] : ga[0];
      const float b2 = ch ? b2v[1] : b2v[0];
      const char* rp = smem + CV_RED + (ch * 40 + w * 5) * 1024 + (lane & 1) * 512 + ((2 * (lane >> 3) + ((lane >> 1) & 1)) ^ ((lane & 1) << 3)) * 16;
      u32x4 o, ob;
#pragma unroll
      for (int i = 0; i < 2; ++i) {
        const char* q = rp + i * 256;
        const f32x4 y = (*(const f32x4*)q + *(const f32x4*)(q + 1024)) + (*(const f32x4*)(q + 2048) + *(const f32x4*)(q + 3072));
        const f32x4 bs = *(const f32x4*)(q + 4096);
        o[2 * i] = pkbf(__builtin_fmaf(y[0], gac, bs[0]), __builtin_fmaf(y[1], gac, bs[1]));
        o[2 * i + 1] = pkbf(__builtin_fmaf(y[2], gac, bs[2]), __builtin_fmaf(y[3], gac, bs[3]));
      }
#pragma unroll
      for (int i = 0; i < 4; ++i) ob[i] = pkbf(bflo(o[i]) * b2, bfhi(o[i]) * b2);
      const int so = ((8 * w + (lane >> 3)) * 448 + 2 * b) * 64 + (lane & 7) * 16;
      __builtin_amdgcn_raw_buffer_store_b128(ob, rs, (int)WS_XB + so, 0, 16);
      __builtin_amdgcn_raw_buffer_store_b128(o, rs, (int)WS_X1T + so, 0, 16);
    }
    SB();
    NP0S(10);
  }
  u32x4 w2f[28];
  const int ke = w ^ ((dg & 1) << 2);
  const int rot = (r2 & 1) * 7168;
  const char* w2src = ws + WS_W2F + ((size_t)(2 * dg) * 112 + 14 * ke) * 1024;
  const char* wsrc = ws + WS_W13 + (size_t)ft * 28672;
#define WDEF(lo, hi_) static_for<lo, hi_>([&](auto ic) __attribute__((always_inline)) { constexpr int i = decltype(ic)::value; wf[i] = gload<(i & 3) * 1024>(voff, wsrc + (i & ~3) * 1024); })
  __builtin_amdgcn_s_waitcnt(0x0F72);
  WDEF(W_N2, W_N3);
  asm volatile("s_waitcnt vmcnt(%0)\n\ts_barrier" :: "i"(W_N3 - W_N2) : "memory");
  STAMP(3);
  if (w == 0 && lane < 16) __hip_atomic_fetch_add(ctl + C1_CNT + (lane * 9 + xcd) * 32, 1u, RLX_AGENT);
  SB();
  WDEF(W_N3, 28);
  f32x16 accw;
#pragma unroll
  for (int r = 0; r < 16; ++r) accw[r] = 0.f;
  const char* tsb = smem + TAB_SB + (G & 1) * 896 + hi * 16;
#pragma unroll
  for (int p = 0; p < W_N2; ++p) accw = MFMA32(*(const u32x4*)(tsb + p * 32), wf[p], accw);
  SB();
  if (w == 0) wait_cnt<9, 6>(ctl + C1_CNT + ((b & 15) * 9 + lane) * 32, lane == 8 ? 32u : 28u, pl0, pl1, pl2);
  asm volatile("s_barrier" ::: "memory");
  STAMP(4);
  const char* xsrc = ws + WS_XB + (size_t)(4 * rt) * 28672 + w * 1024;
#pragma unroll
  for (int i = 0; i < X_N0; ++i) glds16(voff, xsrc + i * 8192, lds0 + w * 1024 + i * 8192);
  {
    asm volatile("s_waitcnt vmcnt(%0)" :: "i"(X_N0) : "memory");
    PIN14(wf, 14);
#pragma unroll
    for (int p = W_N2; p < 28; ++p) accw = MFMA32(*(const u32x4*)(tsb + p * 32), wf[p], accw);
    const float us = accw[8], vs = accw[0];
    const float uh = rbf(us), vh = rbf(vs);
    wex = u32x4{hi ? 0u : pkbf(uh, us - uh), hi ? 0u : pkbf(vh, vs - vh), 0u, 0u};
    pin(wex);
  }
  GAS u32* c2p = ctl + C2_CNT + rt * 1024 + (ft / 14) * 32;
  GAS u32* c2e = ctl + C2_CNT + te * 256 + ke * 32;
  GAS u32* c2l = ctl + C2_CNT + tl * 256 + lane * 32;
  u32 pv = 14;
#define ANNOUNCE(n, tt) asm volatile("s_waitcnt vmcnt(%3)\n\ts_mov_b64 exec, 1\n\tglobal_atomic_add %0, %1, %2 offset:%4\n\ts_mov_b64 exec, -1" :: "v"(0), "v"(1), "s"(c2p), "i"(n), "i"((tt) * 1024) : "memory")
  const char* w2b[4] = {w2src + rot + 3072, w2src + 114688 + rot + 3072, w2src + 7168 - rot + 3072, w2src + 114688 + 7168 - rot + 3072};
  asm volatile("" : "+s"(w2b[0]), "+s"(w2b[1]), "+s"(w2b[2]), "+s"(w2b[3]));
  {
    u32 rb = lds0 + (8 * hi + qq) * 64 + (G & 1) * 32 + pp * 8;
    GAS unsigned long long* sgp = (GAS unsigned long long*)(ws + ST2_OFF) + 128 * rt + (lane & 31);

    u32x4 xf[4];
    f32x16 acc[2];
    float nal, s2, ex[8], pr[8];
    unsigned long long sg = 0;
#define G1_READ(q) do { const LAS char* p_ = (const LAS char*)(size_t)rb + (q) * 1024; u32x2 lo = trread3(p_), hh = trread3(p_ + 256); xf[(q) & 3] = u32x4{lo.x, lo.y, hh.x, hh.y}; } while (0)
#define SG_WAIT(g, ptr) do { _Pragma("unroll 1") for (unsigned spins = 0; spins < SPIN_CAP; ++spins) { if (__builtin_amdgcn_ballot_w64((u32)((g) >> 32) != 1u) == 0) break; sgreload(g, ptr); } } while (0)
#define SG_APPLY(g, A) do { const u32 st_ = (u32)(g); const u32 mm_ = __builtin_amdgcn_perm(st_, st_, 0x01000100u), ss_ = __builtin_amdgcn_perm(st_, st_, 0x03020302u); \
      const float sd_ = bfhi(st_); nal = -1.4426950408889634f * __builtin_amdgcn_rcpf(sd_); s2 = sd_ * sd_; \
      pinf(nal); pinf(s2); A = MFMA32(wex, (u32x4{mm_, ss_, mm_, ss_}), A); } while (0)
#define EPI_STAGE(A, st, v) do { if constexpr ((st) == 0) { ex[v] = A[v] * nal; pr[v] = A[v] * A[8 + (v)]; pinf(pr[v]); } \
      if constexpr ((st) == 1) ex[v] = __builtin_amdgcn_exp2f(ex[v]); if constexpr ((st) == 2) ex[v] = __builtin_fmaf(ex[v], s2, s2); \
      if constexpr ((st) == 3) ex[v] = __builtin_amdgcn_rcpf(ex[v]); if constexpr ((st) == 4) { pr[v] *= ex[v]; pinf(pr[v]); } else pinf(ex[v]); } while (0)
#define EPI_STORE(tt) __builtin_amdgcn_raw_buffer_store_b128(u32x4{pkbf(pr[0], pr[1]), pkbf(pr[2], pr[3]), pkbf(pr[4], pr[5]), pkbf(pr[6], pr[7])}, rs, hbase, (tt) * 112 * 1024, 16)
    asm volatile("s_waitcnt vmcnt(%0)\n\ts_barrier" :: "i"(X_N0 - 4) : "memory");
    POLL_KEEP();
    NP0S(12);
    G1_READ(0); G1_READ(1);
    static_for<0, 112>([&](auto nc) __attribute__((always_inline)) {
      constexpr int n = decltype(nc)::value, t = n / 28, q = n % 28;
      constexpr int wl = g1_w2(n);
      if constexpr (t >= 1 && q == g1_e0(t) - 2) {
        asm volatile("s_waitcnt vmcnt(%1)" : "+v"(sg) : "i"(g1_vm(g1_sg(t - 1), n)) : "memory");
        SG_WAIT(sg, sgp + 32 * (t - 1));
      }
      if constexpr (n == 24) asm volatile("s_waitcnt vmcnt(0)\n\ts_barrier" ::: "memory");
      if constexpr (t >= 2 && q == g1_an(t)) ANNOUNCE(g1_vm(28 * (t - 1) + g1_st(t - 1), n), t - 2);
      if constexpr (n == 108) POLL_ISSUE(pv, c2e, 1);
      if constexpr (n == g1_sg(t)) sgload(sg, sgp + 32 * t);
      if constexpr (q == 26 && (t & 1)) { rb += 57344; asm volatile("" : "+v"(rb)); }
      SB();
      if constexpr ((n & 1) == 0) {
        if constexpr (n < 110) { G1_READ((n + 2) % 56); G1_READ((n + 3) % 56); SB(); __builtin_amdgcn_s_waitcnt(0xC47F); }
        else __builtin_amdgcn_s_waitcnt(0xC07F);
      }
      SB();
      if constexpr (q == 0) { _Pragma("unroll") for (int r = 0; r < 16; ++r) acc[t & 1][r] = 0.f; }
      acc[t & 1] = MFMA32(wf[q], xf[q & 3], acc[t & 1]);
      if constexpr (t >= 1) {
        if constexpr (q == g1_e0(t) - 2) SG_APPLY(sg, acc[(t - 1) & 1]);
        static_for<0, 5>([&](auto sc) __attribute__((always_inline)) {
          constexpr int st = decltype(sc)::value, g = q - g1_e0(t) - st, v = g / 2;
          if constexpr (g >= 0 && (g & 1) == 0 && v < 8) EPI_STAGE(acc[(t - 1) & 1], st, v);
        });
        if constexpr (q == g1_st(t)) { SB(); EPI_STORE(t - 1); }
      }
      if constexpr (n + X_N0 < 14) { SB(); glds16(voff, xsrc + (n + X_N0) * 8192, lds0 + w * 1024 + (n + X_N0) * 8192); }
      if constexpr (n + X_N0 == 14) { SB(); glds16(voff, ws + WS_X1T + ((size_t)(w >> 2 ? tl : te) * 448 + 64 * dg) * 64 + (w & 3) * 1024, lds0 + G2_X1 + w * 1024); }
      if constexpr (wl >= 0) { SB(); w2f[wl] = gload<((wl >> 1) % 7) * 1024 - 3072>(voff, w2b[(wl & 1) + (wl >= 14 ? 2 : 0)]); }
    });
    SB();
    STAMP(5);
    asm volatile("s_waitcnt vmcnt(%1)" : "+v"(sg) : "i"(g1_vm(g1_sg(3), 112)) : "memory");
    SG_WAIT(sg, sgp + 96);
    SG_APPLY(sg, acc[1]);
    static_for<0, 5>([&](auto sc) __attribute__((always_inline)) {
      static_for<0, 8>([&](auto vc) __attribute__((always_inline)) { EPI_STAGE(acc[1], decltype(sc)::value, decltype(vc)::value); });
    });
    EPI_STORE(3);
    SB();
  }
  {
    u32x4 gq[2];
    u32x4 hf[2][14];
    const char* hsrc[2] = {ws + WS_HF + ((size_t)te * 112 + 14 * ke) * 1024, ws + WS_HF + ((size_t)tl * 112 + 14 * ke) * 1024};
#define H_LOAD(j, lo, hi_) static_for<lo, hi_>([&](auto ic) __attribute__((always_inline)) { constexpr int i = decltype(ic)::value; hf[j][i] = gload<((i % 7) & 3) * 1024>(voff, hsrc[j] + (i >= 7 ? 7168 - rot : rot) + ((i % 7) >> 2) * 4096); })
    pl0 = pl1 = pl2 = 0;
#define ANNOUNCE23(n) asm volatile("s_waitcnt vmcnt(%3)\n\ts_mov_b64 exec, 3\n\tglobal_atomic_add %0, %1, %2 offset:2048\n\ts_mov_b64 exec, -1" :: "v"(lane << 10), "v"(1), "s"(c2p), "i"(n) : "memory")
    asm volatile("s_waitcnt vmcnt(1)\n\ts_barrier" : "+v"(pv) :: "memory");
    const bool slow = __builtin_amdgcn_readfirstlane(pv) < 14u;
    if (slow) {
      ANNOUNCE23(0);
      wait_cnt<1, 6>(c2e, 14u, pl0, pl1, pl2);
    }
    STAMP(6);
    { const char* gp = ws + WS_MODC + (64 * dg + 32 * dt + 8 * kq) * 8; gq[0] = gload<0>(hi * 32, gp); gq[1] = gload<16>(hi * 32, gp); }
    H_LOAD(0, 0, G2_B1);
    if (!slow) ANNOUNCE23(G2_B1 + 2);
    static_for<0, 2>([&](auto jc) __attribute__((always_inline)) {
      constexpr int j = decltype(jc)::value;
      f32x16 acc[2];
#pragma unroll
      for (int c = 0; c < 2; ++c)
#pragma unroll
        for (int r = 0; r < 16; ++r) acc[c][r] = 0.f;
      static_for<0, 14>([&](auto sc) __attribute__((always_inline)) {
        constexpr int s = decltype(sc)::value;
        asm volatile("s_waitcnt vmcnt(%1)" : "+v"(hf[j][s]) : "i"((s + G2_B1 - 1 < 13 ? G2_B1 - 1 : 13 - s) + (j == 1 && s < G2_B1 ? 1 : 0)) : "memory");
        if constexpr (s == 0) POLL_KEEP();
        if constexpr (j == 0 && s == 0) { PIN14(w2f, 0); PIN14(w2f, 14); pin(gq[0]); pin(gq[1]); pl0 = pl1 = pl2 = 14u; }
        if constexpr (j == 0 && s == G2_PL) { if (w == 0) POLL_ISSUE(pl0, c2l, 255); }
        if constexpr (j == 0 && s == 13) { if (w == 0) POLL_ISSUE(pl1, c2l, 255); }
        acc[0] = MFMA32(w2f[2 * s], hf[j][s], acc[0]);
        acc[1] = MFMA32(w2f[2 * s + 1], hf[j][s], acc[1]);
        SB();
        if constexpr (s + G2_B1 < 14) H_LOAD(j, s + G2_B1, s + G2_B1 + 1);
      });
      if constexpr (j == 0) {
      } else {
        STAMP(7);
      }
      char* red = smem + j * 65536;
#pragma unroll
      for (int P = 0; P < 8; ++P)
        *(f32x4*)(red + (P * 8 + w) * 1024 + lane * 16) = f32x4{acc[P & 1][4 * (P >> 1)], acc[P & 1][4 * (P >> 1) + 1], acc[P & 1][4 * (P >> 1) + 2], acc[P & 1][4 * (P >> 1) + 3]};
      if constexpr (j == 0) {
        if (w == 0) poll_loop<8>(c2l, 14u, pl0, pl1, pl2);
        asm volatile("s_waitcnt lgkmcnt(0)\n\ts_barrier" ::: "memory");
        NP0S(13);
        H_LOAD(1, 0, G2_B1);
      }
      {
        const float gbv[4] = {rbf(U2F(gq[0].x) + U2F(gq[0].y)), rbf(U2F(gq[0].z) + U2F(gq[0].w)), rbf(U2F(gq[1].x) + U2F(gq[1].y)), rbf(U2F(gq[1].z) + U2F(gq[1].w))};
        if constexpr (j == 1) asm volatile("s_waitcnt lgkmcnt(0)\n\ts_barrier" ::: "memory");
        {
          constexpr int jj = j;
          const char* rp = smem + jj * 65536 + w * 8192 + lane * 16;
          const f32x4 y = ((*(const f32x4*)rp + *(const f32x4*)(rp + 1024)) + (*(const f32x4*)(rp + 2048) + *(const f32x4*)(rp + 3072))) +
                          ((*(const f32x4*)(rp + 4096) + *(const f32x4*)(rp + 5120)) + (*(const f32x4*)(rp + 6144) + *(const f32x4*)(rp + 7168)));
          const u32x2 xr = trread(smem + G2_X1 + jj * 4096 + (32 * dt + 8 * kq + 4 * hi + qq) * 64 + (G & 1) * 32 + pp * 8);
          u32x2 o; o.x = pkbf(__builtin_fmaf(y[0], gbv[0], bflo(xr.x)), __builtin_fmaf(y[1], gbv[1], bfhi(xr.x)));
          o.y = pkbf(__builtin_fmaf(y[2], gbv[2], bflo(xr.y)), __builtin_fmaf(y[3], gbv[3], bfhi(xr.y)));
          ostore(o, out + (size_t)(32 * (jj ? tl : te) + (lane & 31)) * D_ + 64 * dg + 32 * dt + 8 * kq + 4 * hi);
        }
      }
      SB();
    });
  }
  NP0S(15);
  if (b == DIAG_WG) { const unsigned long long lim = stv[15] + 4 * (stv[DIAG_B] - stv[DIAG_A]); while (RT() < lim) __builtin_amdgcn_s_sleep(1); }
}

extern "C" void kernel_launch(void* const* d_in, const int* in_sizes, int n_in, void* d_out, int out_size, void* d_ws, size_t ws_size,
                              hipStream_t stream) {
  const u16* x = (const u16*)d_in[0]; const u16* logA = (const u16*)d_in[1]; const u16* Aim = (const u16*)d_in[2];
  const u16* Cr = (const u16*)d_in[3]; const u16* Ci = (const u16*)d_in[4]; const u16* logdt = (const u16*)d_in[5]; const u16* Dsk = (const u16*)d_in[6];
  const u16* w1 = (const u16*)d_in[8]; const u16* w3 = (const u16*)d_in[9]; const u16* w2 = (const u16*)d_in[10];
  const u16* c = (const u16*)d_in[11]; const u16* wm = (const u16*)d_in[12];
  char* ws = (char*)d_ws;
  unsigned long long* stamps = (unsigned long long*)(ws + (64u << 20));
  kf_main<<<256, 512, 0, stream>>>(x, logA, Aim, Cr, Ci, logdt, Dsk, w1, w3, w2, c, wm, ws, (u16*)d_out, stamps);
}
```

```cpp
#include <hip/hip_runtime.h>

typedef unsigned short u16;
typedef unsigned u32;
typedef u32 u32x4 __attribute__((ext_vector_type(4)));
typedef u32 u32x2 __attribute__((ext_vector_type(2)));
typedef short s16x4 __attribute__((ext_vector_type(4)));
typedef __bf16 bf16x8 __attribute__((ext_vector_type(8)));
typedef __bf16 bf16x2 __attribute__((ext_vector_type(2)));
typedef _Float16 f16x8 __attribute__((ext_vector_type(8)));
typedef _Float16 f16x2 __attribute__((ext_vector_type(2)));
typedef float f32x2 __attribute__((ext_vector_type(2)));
typedef float f32x4 __attribute__((ext_vector_type(4)));
typedef float f32x16 __attribute__((ext_vector_type(16)));
#define DI __device__ __forceinline__
#define U2F(x) __builtin_bit_cast(float, (u32)(x))
#define F2U(x) __builtin_bit_cast(u32, (float)(x))
#define I2F(x) __builtin_bit_cast(float, (int)(x))
#define F2I(x) __builtin_bit_cast(int, (float)(x))
#if defined(__HIP_DEVICE_COMPILE__)
#define NOPK __attribute__((target("no-packed-fp32-ops")))
#else
#define NOPK
#endif
#define LDSP(T, p) ((__attribute__((address_space(3))) T*)(p))
#define GAS __attribute__((address_space(1)))
#define RLX_AGENT __ATOMIC_RELAXED, __HIP_MEMORY_SCOPE_AGENT
#define SB() __builtin_amdgcn_sched_barrier(0)
#define LDIN(p) __builtin_nontemporal_load(p)
template <int V> struct IC { static constexpr int value = V; };
template <int I, int N, class F> __device__ __forceinline__ void static_for(F&& f) { if constexpr (I < N) { f(IC<I>{}); static_for<I + 1, N>(f); } }

constexpr int L_ = 2048, D_ = 448, N_ = 256, F_ = 1792;

constexpr int EP_E = 0;
constexpr int EP_R = 64;
constexpr int C0_CNT = 1024;
constexpr int C1_CNT = 5120;
constexpr int C2_CNT = 14336;
constexpr int ST2_OFF = 122880;
constexpr int ZERO_LO = 20480, ZERO_HI = 139264;
constexpr size_t WS_MODA = 139264;
constexpr size_t WS_MODB = WS_MODA + 448 * 32;
constexpr size_t WS_MODC = WS_MODB + 448 * 16;
constexpr size_t WS_ST1 = 167936;
constexpr size_t WS_XT = WS_ST1 + 16384;
constexpr size_t WS_X1T = WS_XT + 64 * 448 * 64;
constexpr size_t WS_XB = WS_X1T + 64 * 448 * 64;
constexpr size_t WS_W13 = WS_XB + 64 * 448 * 64;
constexpr size_t WS_W2F = WS_W13 + 112 * 28 * 1024;
constexpr size_t WS_HF = WS_W2F + 14 * 112 * 1024;
constexpr size_t WS_END = WS_HF + 64 * 112 * 1024;

DI float bflo(u32 v) { return U2F(v << 16); }
DI float bfhi(u32 v) { return U2F(v & 0xffff0000u); }
DI float bf2f(u16 v) { return U2F(((u32)v) << 16); }
DI u32 pkbf(float lo, float hi) { f32x2 p = {lo, hi}; return __builtin_bit_cast(u32, __builtin_convertvector(p, bf16x2)); }
DI u32 pkh(f32x2 p) { return __builtin_bit_cast(u32, __builtin_convertvector(p, f16x2)); }
DI float sadd(float a, float b) { float r; asm("v_add_f32 %0, %1, %2" : "=v"(r) : "v"(a), "v"(b)); return r; }
DI float smul(float a, float b) { float r; asm("v_mul_f32 %0, %1, %2" : "=v"(r) : "v"(a), "v"(b)); return r; }
DI float sfma(float a, float b, float c) { float r; asm("v_fma_f32 %0, %1, %2, %3" : "=v"(r) : "v"(a), "v"(b), "v"(c)); return r; }
DI float rbf(float f) { return bflo(pkbf(f, f)); }
DI float dot2(u32 a, u32 b, float c) { return __builtin_amdgcn_fdot2_f32_bf16(__builtin_bit_cast(bf16x2, a), __builtin_bit_cast(bf16x2, b), c, false); }
DI float fexp(float x) { return __builtin_amdgcn_exp2f(x * 1.4426950408889634f); }
DI float silu_bf(float c) { return rbf(c * __builtin_amdgcn_rcpf(1.f + fexp(-c))); }
template <int CTRL> DI float dppf(float x) { return I2F(__builtin_amdgcn_update_dpp(0, F2I(x), CTRL, 0xf, 0xf, false)); }
DI float sum16(float x) { x += dppf<0x128>(x); x += dppf<0x124>(x); x += dppf<0x122>(x); x += dppf<0x121>(x); return x; }
DI float max16(float x) { x = __builtin_fmaxf(x, dppf<0x128>(x)); x = __builtin_fmaxf(x, dppf<0x124>(x)); x = __builtin_fmaxf(x, dppf<0x122>(x)); x = __builtin_fmaxf(x, dppf<0x121>(x)); return x; }
DI float add32(float x) { auto r = __builtin_amdgcn_permlane32_swap(F2U(x), F2U(x), false, false); return U2F(r[0]) + U2F(r[1]); }
DI float add16(float x) { auto r = __builtin_amdgcn_permlane16_swap(F2U(x), F2U(x), false, false); return U2F(r[0]) + U2F(r[1]); }
DI float max64(float x) {
  x = max16(x);
  auto r = __builtin_amdgcn_permlane16_swap(F2U(x), F2U(x), false, false);
  x = __builtin_fmaxf(U2F(r[0]), U2F(r[1]));
  auto q = __builtin_amdgcn_permlane32_swap(F2U(x), F2U(x), false, false);
  return __builtin_fmaxf(U2F(q[0]), U2F(q[1]));
}
DI u32x2 trread(const char* p) { return __builtin_bit_cast(u32x2, __builtin_amdgcn_ds_read_tr16_b64_v4i16(LDSP(s16x4, p))); }
DI u32x2 trread3(const __attribute__((address_space(3))) char* p) { return __builtin_bit_cast(u32x2, __builtin_amdgcn_ds_read_tr16_b64_v4i16((__attribute__((address_space(3))) s16x4*)p)); }
DI void pinf(float& v) { asm volatile("" : "+v"(v)); }
DI void pin(u32x4& v) { asm volatile("" : "+v"(v)); }
DI void ostore(u32x2 v, u16* p) { *(u32x2*)p = v; }
DI f32x2 cmulp(f32x2 a, f32x2 z, f32x2 zs) { return __builtin_elementwise_fma(a.yx, zs, a * z.xx); }

#define STAMP_DECL
#define STAMP(i)
#define CSTAMP(i)
#define STAMP_OUT(buf, slot)
#define STAMP_OUT2(buf, slot)

#define MFMA32(a, b, c) __builtin_amdgcn_mfma_f32_32x32x16_bf16(__builtin_bit_cast(bf16x8, a), __builtin_bit_cast(bf16x8, b), c, 0, 0, 0)
#define ST16(v, off) __builtin_amdgcn_raw_buffer_store_b128(v, rs, (int)(off), 0, 16)
template <int IMM> DI u32x4 gload(u32 voff, const char* sbase) {
  u32x4 r; asm volatile("global_load_dwordx4 %0, %1, %2 offset:%3 sc1" : "=v"(r) : "v"(voff), "s"(sbase), "i"(IMM)); return r;
}
DI void glds16(u32 voff, const char* sbase, u32 ldsaddr) {
  asm volatile("s_mov_b32 m0, %2\n\ts_nop 0\n\tglobal_load_lds_dwordx4 %0, %1 sc1" :: "v"(voff), "s"(sbase), "s"(ldsaddr) : "memory");
}
constexpr unsigned SPIN_CAP = 1u << 16;
template <int OFF> DI u32 ldu16(const u16* p) { u32 r; asm volatile("global_load_ushort %0, %1, off offset:%2" : "=v"(r) : "v"(p), "i"(OFF) : "memory"); return r; }
template <int OFF> DI u32x4 ldnt(const u16* p) { u32x4 r; asm volatile("global_load_dwordx4 %0, %1, off offset:%2 nt" : "=v"(r) : "v"(p), "i"(OFF) : "memory"); return r; }
DI void sgload(unsigned long long& g, GAS unsigned long long* p) { asm volatile("global_load_dwordx2 %0, %1, off sc1" : "+v"(g) : "v"(p) : "memory"); }
DI void sgwait(unsigned long long& g) { asm volatile("s_waitcnt vmcnt(1)" : "+v"(g) :: "memory"); }
DI void sgreload(unsigned long long& g, GAS unsigned long long* p) { asm volatile("global_load_dwordx2 %0, %1, off sc1\n\ts_waitcnt vmcnt(0)" : "+v"(g) : "v"(p) : "memory"); }
#define POLL_ISSUE(v, p, m) asm volatile("s_mov_b64 exec, %3\n\tglobal_atomic_or %0, %1, %2, off sc0\n\ts_mov_b64 exec, -1" : "+v"(v) : "v"(p), "v"(0), "i"(m) : "memory")
template <int NL> DI void poll_loop(GAS u32* p, u32 want, u32& v0, u32& v1, u32& v2) {
#pragma unroll 1
  for (unsigned spins = 0; spins < SPIN_CAP; ++spins) {
    POLL_ISSUE(v2, p, (1 << NL) - 1);
    asm volatile("s_waitcnt vmcnt(2)" : "+v"(v0) :: "memory");
    if (__builtin_amdgcn_ballot_w64(v0 < want) == 0) break;
    POLL_ISSUE(v0, p, (1 << NL) - 1);
    asm volatile("s_waitcnt vmcnt(2)" : "+v"(v1) :: "memory");
    if (__builtin_amdgcn_ballot_w64(v1 < want) == 0) break;
    POLL_ISSUE(v1, p, (1 << NL) - 1);
    asm volatile("s_waitcnt vmcnt(2)" : "+v"(v2) :: "memory");
    if (__builtin_amdgcn_ballot_w64(v2 < want) == 0) break;
  }
}
template <int NL, int SLP> DI void wait_cnt(GAS u32* p, u32 want, u32& v0, u32& v1, u32& v2) {
  v0 = want; v1 = want; v2 = want;
  POLL_ISSUE(v0, p, (1 << NL) - 1); __builtin_amdgcn_s_sleep(SLP);
  POLL_ISSUE(v1, p, (1 << NL) - 1); __builtin_amdgcn_s_sleep(SLP);
  poll_loop<NL>(p, want, v0, v1, v2);
}
#define POLL_KEEP() asm volatile("" :: "v"(pl0), "v"(pl1), "v"(pl2) : "memory")
#define WAIT_VM0() __builtin_amdgcn_s_waitcnt(0x0F70)
#define BAR() __builtin_amdgcn_s_barrier()

constexpr int CV_DSTR = 4 * 4160 + 4608;
constexpr int CV_RED = 2 * CV_DSTR;
constexpr int G1_ST = 4 * 28672;
constexpr int G2_RED = 4 * 28672;
constexpr int G2_X1 = 18 * 8192;
constexpr int TAB_GB = G2_X1 + 8192;
constexpr int TAB_SB = TAB_GB + 256;
constexpr int ARR_CNT = TAB_SB + 2688;
constexpr int KF_LDS = ARR_CNT + 32;
constexpr int KG_REG = 32 * 272;
constexpr int KG_IMG = 2 * CV_DSTR;
constexpr int TASK_LDS = KG_IMG + 8 * KG_REG;
constexpr int KG_TAB = TASK_LDS + 30464;
#define HSLOT stv[14]
#define WSLOT stv[0]
#define W2_N0 13
#define W13_SLP 1
#define G1MAP 0
#define CODEPF 32
#define X_N0 8
#define P0S(i)
#define NP0S(i) STAMP(i)
#define G2_B1 6
#define G2_PL 10
#define EPI0 7
#define W_N0 2
#define W_N1 6
#define W_NS 2
#define W_NA 8
#define W_NB2 7
#define UV_N1 20
#define SC_SIG 6
#define SC_POLL 21
#define LAS __attribute__((address_space(3)))
#define PIN14(a, o) asm volatile("" : "+v"(a[o]), "+v"(a[o + 1]), "+v"(a[o + 2]), "+v"(a[o + 3]), "+v"(a[o + 4]), "+v"(a[o + 5]), "+v"(a[o + 6]), "+v"(a[o + 7]), \
    "+v"(a[o + 8]), "+v"(a[o + 9]), "+v"(a[o + 10]), "+v"(a[o + 11]), "+v"(a[o + 12]), "+v"(a[o + 13]) :: "memory")

constexpr int g1_e0(int t) { return t == 1 ? EPI0 : 3; }
constexpr int g1_st(int t) { return g1_e0(t) + 19; }
constexpr int g1_an(int t) { return t == 2 ? 18 : 14; }
constexpr int g1_w2(int n) {
  if (n >= 34 && n < 34 + 14 * 3 && (n - 34) % 3 == 0) return (n - 34) / 3;
  if (n >= 84 && n < 84 + 23) { const int q = n - 84; if (q % 3 != 2) return 14 + q - q / 3 < 28 ? 14 + q - q / 3 : -1; }
  return -1;
}
constexpr int g1_sg(int t) { return t == 0 ? 24 : 28 * t + 10; }
constexpr int g1_vm(int a, int b) {
  int c = 0;
  for (int n = a; n < b; ++n) {
    const int t = n / 28, q = n % 28;
    if (n > a) { if (t >= 2 && q == g1_an(t)) ++c; if (n == 108) ++c; if (n == g1_sg(t)) ++c; }
    if (t >= 1 && q == g1_st(t) && n != a) ++c;
    if (n >= 24 && n < 32) ++c;
    if (g1_w2(n) >= 0) ++c;
  }
  return c;
}
__global__ __launch_bounds__(512) void kf_main(const u16* __restrict__ x, const u16* __restrict__ logA, const u16* __restrict__ Aim,
                                               const u16* __restrict__ Cr, const u16* __restrict__ Ci, const u16* __restrict__ logdt,
                                               const u16* __restrict__ Dsk, const u16* __restrict__ w1, const u16* __restrict__ w3,
                                               const u16* __restrict__ w2, const u16* __restrict__ cvec, const u16* __restrict__ wmod,
                                               char* __restrict__ ws, u16* __restrict__ out, unsigned long long* stamps) {
  __shared__ __attribute__((aligned(16))) char smem[KF_LDS];
  const int tid = __builtin_amdgcn_workitem_id_x(), lane = tid & 63;
  const int w = __builtin_amdgcn_readfirstlane(tid >> 6);
  const int b = __builtin_amdgcn_workgroup_id_x(), xcd = b & 7, idx = b >> 3;
  const int hi = lane >> 5;
  const int G = lane >> 4, qq = (lane & 15) >> 2, pp = lane & 3;
  const u32 voff = lane * 16;
  GAS u32* ctl = (GAS u32*)ws;
  auto rs = __builtin_amdgcn_make_buffer_rsrc(ws, 0, (int)WS_END, 0x00020000);
  const u32 lds0 = (u32)(size_t)LDSP(char, smem);
  STAMP_DECL;
  STAMP(0);
  u32 pl0 = 0, pl1 = 0, pl2 = 0;
  u32x4 cpf;
  { const char* pc; asm volatile("s_getpc_b64 %0" : "=s"(pc));
    const u32 o = (idx < CODEPF ? idx : 0) * 1024 + lane * 16;
    if (w == 7) asm volatile("global_load_dwordx4 %0, %1, %2" : "=v"(cpf) : "v"(o), "s"(pc) : "memory"); }
  const bool mainwg = b < 224;
  const int dl = w >> 2, q4 = w & 3, t256 = tid & 255;
  auto arrive0 = [&]() __attribute__((always_inline)) {
    u32 old;
    asm volatile("s_waitcnt vmcnt(0)\n\ts_mov_b64 exec, 1\n\tds_add_rtn_u32 %0, %1, %2\n\ts_mov_b64 exec, -1\n\ts_waitcnt lgkmcnt(0)" : "=&v"(old) : "v"(lds0 + ARR_CNT), "v"(1) : "memory");
    if (__builtin_amdgcn_readfirstlane(old) == (mainwg ? 4u : (b < 249 ? 8u : 0u)) && lane < 16) __hip_atomic_fetch_add(ctl + C0_CNT + (lane * 8 + xcd) * 32, 1u, RLX_AGENT);
  };
  GAS u32* c0p = ctl + C0_CNT + ((b & 15) * 8 + lane) * 32;
  int kg_wb, kg_wst;
  u32 kga[12];
  u32 dskr = 0;
  unsigned long long epoch;
  {
    asm volatile("" :: "s"(x), "s"(logA), "s"(Aim), "s"(Cr), "s"(Ci), "s"(logdt), "s"(Dsk), "s"(w1), "s"(w3), "s"(cvec), "s"(wmod), "s"(ws));
    u32 lar = 0, air = 0, crr = 0, cir = 0, ldt = 0;
    if (mainwg) {
      const int d = 2 * b + dl;
      asm volatile("global_load_ushort %0, %6, %7 nt\n\tglobal_load_ushort %1, %6, %8 nt\n\tglobal_load_ushort %4, %11, %12\n\tglobal_load_ushort %2, %6, %9 nt\n\tglobal_load_ushort %3, %6, %10 nt\n\tglobal_load_dword %5, %13, %14"
                   : "+v"(lar), "+v"(air), "+v"(crr), "+v"(cir), "+v"(ldt), "+v"(dskr) : "v"((d * N_ + t256) * 2), "s"(logA), "s"(Aim), "s"(Cr), "s"(Ci), "v"(d * 2), "s"(logdt), "v"(b * 4), "s"(Dsk) : "memory");
    }
    SB();
    unsigned long long ep = 0;
    u32 pe = 32u;
    const int task = b >= 249 ? 2 * b - 364 + dl : (dl || b >= 246 ? -1 : (b < 134 ? b : b + 14));
    char* stg = smem + (dl ? 0 : TASK_LDS);
    u32x4 v[7]; u32 cv[7];
    const bool isx = task < 64, isw = task >= 148;
    const char* tsb; u32 tstep, tvo;
    {
      const int m = task - (isw ? 148 : 64);
      const size_t px = (size_t)x, pw = q4 >= 2 ? (size_t)w3 : (size_t)w1, pm = (size_t)wmod;
      const u32 k1 = isw ? 28672u : 128u, k0 = isw ? 1792u : 1204224u;
      const u32 off = isx ? (u32)task * 28672u : (u32)(m >> 1) * k1 + (u32)(m & 1) * k0;
      tsb = (const char*)((isx ? px : (isw ? pw : pm)) + off);
      tstep = isx ? 128u : (isw ? 256u : 172032u);
      tvo = ((t256 >> (isw ? 4 : 3)) & (isw ? 7 : 31)) * (isx ? 896u : (isw ? 3584u : 5376u)) + (t256 & (isw ? 15 : 7)) * 16;
    }
#define taskload() do { _Pragma("unroll") for (int i = 0; i < 7; ++i) asm volatile("global_load_dwordx4 %0, %1, %2 nt" : "=v"(v[i]) : "v"(tvo), "s"(tsb + i * tstep) : "memory"); } while (0)
    auto cload = [&]() __attribute__((always_inline)) {
      const u16* cp = cvec + 224 * (task & 1) + (t256 >> 3);
      static_for<0, 7>([&](auto ic) __attribute__((always_inline)) { constexpr int i = decltype(ic)::value; cv[i] = ldu16<64 * i>(cp); });
    };
    if (task >= 0 && b < 249) taskload();
    if (task >= 64 && task < 134) {
      cload();
    } else if (task < 0 && !mainwg) {
      if (dl) {
        const int zo = ZERO_LO + (b - 224) * 8192 + t256 * 16;
        if (zo < ZERO_HI) ST16((u32x4{0, 0, 0, 0}), zo);
        if (zo + 4096 < ZERO_HI) ST16((u32x4{0, 0, 0, 0}), zo + 4096);
      }
      if (b == 248 && w == 0) {
        const unsigned long long e0 = __hip_atomic_load((GAS unsigned long long*)(ctl + EP_E), RLX_AGENT);
#pragma unroll
        for (int i = 0; i < 2; ++i) __hip_atomic_store(ctl + C0_CNT + i * 2048 + lane * 32, 0u, RLX_AGENT);
        WAIT_VM0();
        asm volatile("s_waitcnt vmcnt(0)" ::: "memory");
        if (lane < 16) __hip_atomic_store((GAS unsigned long long*)(ctl + EP_R + lane * 32), e0 + 1, RLX_AGENT);
      }
    }
    SB();
    P0S(15);
    if (tid < 8) *(u32*)(smem + ARR_CNT + tid * 4) = 0;
    asm volatile("s_waitcnt lgkmcnt(0)\n\ts_barrier" ::: "memory");
    SB();
    auto meet = [&](u32 want) __attribute__((always_inline)) {
      asm volatile("s_waitcnt lgkmcnt(0)\n\ts_mov_b64 exec, 1\n\tds_add_u32 %0, %1 offset:4\n\ts_mov_b64 exec, -1" :: "v"(lds0 + ARR_CNT + dl * 4), "v"(1) : "memory");
#pragma unroll 1
      for (unsigned spins = 0; spins < 16 * SPIN_CAP; ++spins) {
        if (*(volatile LAS u32*)(size_t)(lds0 + ARR_CNT + 4 + dl * 4) >= want) break;
        __builtin_amdgcn_s_sleep(1);
      }
      asm volatile("" ::: "memory");
    };
#pragma unroll
    for (int h = 0; h < 2; ++h) {
      const int lb = 2040 - 8 * t256 - 8 * h;
      const int kr = lb < 256 ? 0 : 24 - __builtin_clz(lb);
      const float mB = U2F((u32)(150 + kr) << 23) + (float)(lb < 0 ? 704 : lb);
      const u32 kpa = lds0 + (u32)(KG_TAB + dl * 1536) + 256u * kr - ((u32)(150 + kr) << 24);
#pragma unroll
      for (int e = 0; e < (h ? 4 : 8); ++e) { kga[8 * h + e] = (F2U(mB + (float)(7 - e)) << 1) + kpa; asm volatile("" : "+v"(kga[8 * h + e])); }
    }
    if (t256 == 0) *(u32x4*)(smem + KG_TAB + dl * 1536 + 1408) = u32x4{0, 0, 0, 0};
    {
      char* cp = smem + dl * CV_DSTR;
      if (mainwg) {
        if (t256 < 32) *(u32x4*)(cp + 4 * 4160 + t256 * 16) = u32x4{0, 0, 0, 0};
        else if (t256 < 40) *(u32x4*)(cp + ((t256 - 32) >> 1) * 4160 + 4096 + (t256 & 1) * 16) = u32x4{0, 0, 0, 0};
      }
    }
    const int s = lane & 31;
    {
      const int kw = s < 8 ? 0 : (s < 20 ? (s >> 2) - 1 : 3);
      int wb = s < 12 ? (s < 8 ? s : 248 + s) : (s < 16 ? 384 + ((s & 2) << 5) + (s & 1) : 32 * s);
      kg_wst = 16 >> kw;
      kg_wb = (wb > 672 ? 672 : wb) * 2 + KG_TAB + dl * 1536 + (8 * q4 + 4 * hi) * kg_wst;
    }
    char* reg = smem + KG_IMG + w * KG_REG;
    f16x8 bfr[8];
    f32x2 z8, z16; float scl = 0.f, fs = 0.f;
    unsigned long long tagr = 0;
    GAS u32* tagp = ctl + EP_R + (b & 15) * 32;
#define EP_ISSUE() asm volatile("s_mov_b64 exec, 1\n\tglobal_load_dwordx2 %1, %4, off sc1\n\tglobal_atomic_or_x2 %0, %2, %3, off sc0\n\ts_mov_b64 exec, -1" \
                     : "+&v"(tagr), "+&v"(ep) : "v"(tagp), "v"(0ull), "v"(ctl + EP_E) : "memory")
    auto epcheck = [&]() __attribute__((always_inline)) {
#pragma unroll 1
      for (unsigned spins = 0; spins < SPIN_CAP; ++spins) {
        asm volatile("s_waitcnt vmcnt(0)" : "+v"(tagr), "+v"(ep) :: "memory");
        if ((__builtin_amdgcn_ballot_w64(tagr != ep + 1) & 1) == 0) break;
        EP_ISSUE();
      }
      arrive0();
    };
    if (mainwg) {
      if (task < 0) asm volatile("s_waitcnt vmcnt(0)" ::: "memory"); else if (isx || isw) asm volatile("s_waitcnt vmcnt(7)" ::: "memory"); else asm volatile("s_waitcnt vmcnt(14)" ::: "memory");
      asm volatile("" : "+v"(lar), "+v"(air), "+v"(crr), "+v"(cir), "+v"(ldt), "+v"(dskr) :: "memory");
      P0S(12);
      if (w == 4) EP_ISSUE();
    }
    if (mainwg) {
      const float dt = rbf(fexp(bflo(ldt)));
      const float ar = -rbf(fexp(bflo(lar))) * dt * 1.4426950408889634f;
      const float ai = bflo(air) * dt;
      const float I2PH = 0.15915494f, I2PL = 6.4206382e-9f;
      const float th = ai * I2PH;
      const float tl = __builtin_fmaf(ai, I2PL, __builtin_fmaf(ai, I2PH, -th));
      auto zpow = [&](float sg) __attribute__((always_inline)) {
        float e = __builtin_amdgcn_exp2f(ar * sg);
        float rev = __builtin_fmaf(tl, sg, __builtin_amdgcn_fractf(th * sg));
        return f32x2{e * __builtin_amdgcn_cosf(rev), e * __builtin_amdgcn_sinf(rev)};
      };
      const f32x2 z1 = zpow(1.f), z256 = zpow(256.f);
      z16 = zpow(16.f);
      const f32x2 s1 = {-z1.y, z1.y}, s256 = {-z256.y, z256.y};
      u32 eb = ((crr & 0x7f80u) > (cir & 0x7f80u) ? (crr & 0x7f80u) : (cir & 0x7f80u));
      asm("s_nop 1\n\tv_max_u32_dpp %0, %0, %0 row_ror:8 row_mask:0xf bank_mask:0xf\n\ts_nop 1\n\tv_max_u32_dpp %0, %0, %0 row_ror:4 row_mask:0xf bank_mask:0xf\n\ts_nop 1\n\t"
          "v_max_u32_dpp %0, %0, %0 row_ror:2 row_mask:0xf bank_mask:0xf\n\ts_nop 1\n\tv_max_u32_dpp %0, %0, %0 row_ror:1 row_mask:0xf bank_mask:0xf\n\ts_nop 1" : "+v"(eb));
      {
        const u32 e0 = __builtin_amdgcn_readlane(eb, 0), e1 = __builtin_amdgcn_readlane(eb, 16), e2 = __builtin_amdgcn_readlane(eb, 32), e3 = __builtin_amdgcn_readlane(eb, 48);
        const u32 m0 = e0 > e1 ? e0 : e1, m1 = e2 > e3 ? e2 : e3;
        eb = (m0 > m1 ? m0 : m1) >> 7;
      }
      eb = eb < 1u ? 1u : (eb > 252u ? 252u : eb);
      scl = U2F((253u - eb) << 23);
      fs = 2.f * dt * U2F((eb + 1u) << 23);
      f32x2 lo[9], hp[9];
      lo[1] = z1; hp[1] = z256;
#pragma unroll
      for (int i = 2; i <= 8; ++i) { lo[i] = cmulp(lo[i - 1], z1, s1); hp[i] = cmulp(hp[i - 1], z256, s256); }
      const f32x2 a2 = cmulp(lo[2], z256, s256), a4 = cmulp(lo[4], z256, s256), a6 = cmulp(lo[6], z256, s256);
      const f32x2 b4 = cmulp(a4, z256, s256), c4 = cmulp(b4, z256, s256);
      const u32 col[21] = {0x00003c00u, pkh(lo[1]), pkh(lo[2]), pkh(lo[3]), pkh(lo[4]), pkh(lo[5]), pkh(lo[6]), pkh(lo[7]),
                           pkh(hp[1]), pkh(a2), pkh(a4), pkh(a6), pkh(hp[2]), pkh(b4), pkh(hp[3]), pkh(c4),
                           pkh(hp[4]), pkh(hp[5]), pkh(hp[6]), pkh(hp[7]), pkh(hp[8])};
#pragma unroll
      for (int i = 0; i < 21; ++i) *(u32*)(reg + i * 272 + lane * 4) = col[i];
      z8 = lo[8];
#pragma unroll
      for (int i = 0; i < 8; ++i) bfr[i] = *(const f16x8*)(reg + s * 272 + i * 32 + hi * 16);
      asm volatile("s_waitcnt lgkmcnt(0)" : "+v"(bfr[0]), "+v"(bfr[1]), "+v"(bfr[2]), "+v"(bfr[3]), "+v"(bfr[4]), "+v"(bfr[5]), "+v"(bfr[6]), "+v"(bfr[7]) :: "memory");
    }
    if (!mainwg) asm volatile("s_waitcnt vmcnt(0)" ::: "memory");
    asm volatile("" :: "v"(cpf));
    STAMP(8);
    if (w == 4) { if (!mainwg) EP_ISSUE(); if (mainwg || b >= 249) epcheck(); }
    if (b >= 249) { taskload(); cload(); }
#define PIN_V() asm volatile("s_waitcnt vmcnt(0)" : "+v"(v[0]), "+v"(v[1]), "+v"(v[2]), "+v"(v[3]), "+v"(v[4]), "+v"(v[5]), "+v"(v[6]) :: "memory")
    if (task >= 0 && task < 148) {
      PIN_V();
      P0S(13);
      if (task < 64) {
            const int row = t256 >> 3, p = t256 & 7;
            float sm = 0.f, sq = 0.f;
#pragma unroll
            for (int i = 0; i < 7; ++i) {
              pin(v[i]);
              *(u32x4*)(stg + row * 928 + (p + 8 * i) * 16) = v[i];
#pragma unroll
              for (int j = 0; j < 4; ++j) { sm = dot2(v[i][j], 0x3f803f80u, sm); sq = dot2(v[i][j], v[i][j], sq); }
            }
            sm += dppf<0xB1>(sm); sq += dppf<0xB1>(sq); sm += dppf<0x4E>(sm); sq += dppf<0x4E>(sq); sm += dppf<0x141>(sm); sq += dppf<0x141>(sq);
            const float m = sm * (1.f / D_);
            const float var = __builtin_fmaxf(sq * (1.f / D_) - m * m, 0.f);
            const float sd = rbf(__builtin_amdgcn_sqrtf(rbf(rbf(var) + 1e-5f)));
            const float rsd = __builtin_amdgcn_rcpf(sd);
            if (p < 2) __builtin_amdgcn_raw_buffer_store_b32(F2U(p ? -rbf(m) * rsd : rsd), rs, (int)WS_ST1 + p * 8192 + (task * 32 + row) * 4, 0, 16);
        meet(4u);
        const char* rb = stg + (8 * G + qq) * 928 + q4 * 224 + pp * 8;
#pragma unroll
        for (int i = 0; i < 7; ++i) {
          const u32x2 lo = trread(rb + i * 32), hh = trread(rb + i * 32 + 4 * 928);
          ST16((u32x4{lo.x, lo.y, hh.x, hh.y}), (int)WS_XT + task * 28672 + q4 * 7168 + (lane & 15) * 64 + G * 16 + i * 1024);
        }
      } else {
            float acc[8] = {0, 0, 0, 0, 0, 0, 0, 0};
#pragma unroll
            for (int i = 0; i < 7; ++i) {
              asm volatile("" : "+v"(cv[i]));
              const u32 sh = F2U(silu_bf(bflo(cv[i]))), sl = sh >> 16;
#pragma unroll
              for (int j = 0; j < 4; ++j) { acc[2 * j] = dot2(v[i][j], sl, acc[2 * j]); acc[2 * j + 1] = dot2(v[i][j], sh, acc[2 * j + 1]); }
            }
#pragma unroll
            for (int j = 0; j < 8; ++j) { acc[j] += dppf<0x128>(acc[j]); acc[j] = add32(acc[j]); }
            if ((lane & 40) == 0) {
              float* red = (float*)stg + (q4 * 2 + (lane >> 4)) * 64 + (lane & 7) * 8;
              *(f32x4*)red = f32x4{acc[0], acc[1], acc[2], acc[3]};
              *(f32x4*)(red + 4) = f32x4{acc[4], acc[5], acc[6], acc[7]};
            }
        meet(4u);
            if (q4 == 0) {
              const int m = task - 64, cg = m >> 1;
              float t = 0.f;
#pragma unroll
              for (int j = 0; j < 8; ++j) t += ((const float*)stg)[j * 64 + lane];
              const int sec = cg / 7, dd = (cg % 7) * 64 + lane;
              const int o1 = sec == 3 ? (int)WS_MODB + dd * 16 : (sec == 5 ? (int)WS_MODC + dd * 8 : (int)WS_MODA + dd * 32 + (sec == 4 ? 3 : sec) * 8);
              __builtin_amdgcn_raw_buffer_store_b32(F2U(t), rs, o1 + (m & 1) * 4, 0, 16);
              if (sec == 4) __builtin_amdgcn_raw_buffer_store_b32(F2U(t), rs, (int)WS_MODB + dd * 16 + 8 + (m & 1) * 4, 0, 16);
            }
      }
    }
    if (task >= 148) {
      PIN_V();
      P0S(13);
#pragma unroll
      for (int i = 0; i < 7; ++i) *(u32x4*)(stg + (t256 >> 4) * 1824 + ((t256 & 15) + 16 * i) * 16) = v[i];
      meet(4u);
      const int m = task - 148;
      const char* rb = stg + (8 * (G & 1) + qq) * 1824 + (14 * q4 + hi) * 32 + (8 * (pp & 1) + 4 * (pp >> 1)) * 2;
      const int dst = (int)WS_W13 + ((56 * (m & 1) + 14 * q4 + hi) * 28 + (m >> 2)) * 1024 + ((m >> 1) & 1) * 512 + (lane & 31) * 16;
#pragma unroll
      for (int i = 0; i < 7; ++i) {
        const u32x2 lo = trread(rb + i * 64), hh = trread(rb + i * 64 + 4 * 1824);
        ST16((u32x4{lo.x, lo.y, hh.x, hh.y}), dst + i * 57344);
      }
    }
    P0S(10);
    if (mainwg) {
        pinf(scl);
        f32x2 ra = {bflo(crr) * scl, -bflo(cir) * scl};
        f32x2 rb = cmulp(ra, z8, f32x2{z8.y, -z8.y});
        const f32x2 s16c = {z16.y, -z16.y};
#pragma unroll
        for (int r = 0; r < 32; r += 2) {
          *(u32*)(reg + r * 272 + lane * 4) = pkh(ra); *(u32*)(reg + (r + 1) * 272 + lane * 4) = pkh(rb);
          if (r < 30) { ra = cmulp(ra, z16, s16c); rb = cmulp(rb, z16, s16c); }
        }
        if (task >= 0) { WAIT_VM0(); arrive0(); STAMP(11); }
        f32x16 acc = {};
#pragma unroll
        for (int i = 0; i < 8; ++i) {
          const f16x8 af = *(const f16x8*)(reg + s * 272 + i * 32 + hi * 16);
          acc = __builtin_amdgcn_mfma_f32_32x32x16_f16(af, bfr[i], acc, 0, 0, 0);
        }
#pragma unroll
        for (int qd = 0; qd < 4; ++qd)
          *(f32x4*)(reg + qd * 1024 + lane * 16) = f32x4{acc[4 * qd] * fs, acc[4 * qd + 1] * fs, acc[4 * qd + 2] * fs, acc[4 * qd + 3] * fs};
    }
    if (mainwg) {
      if (w == 4) POLL_ISSUE(pe, c0p, 255);
      meet(dl ? 4u : 8u);
    {
      const char* rp = smem + KG_IMG + dl * 4 * KG_REG + q4 * 1024 + lane * 16;
      const f32x4 t = (*(const f32x4*)rp + *(const f32x4*)(rp + KG_REG)) + (*(const f32x4*)(rp + 2 * KG_REG) + *(const f32x4*)(rp + 3 * KG_REG));
      const u32 ta = pkbf(t[0], t[1]), tc = pkbf(t[2], t[3]);
      char* q = smem + kg_wb;
      *(u16*)q = (u16)ta; *(u16*)(q + kg_wst) = (u16)(ta >> 16); *(u16*)(q + 2 * kg_wst) = (u16)tc; *(u16*)(q + 3 * kg_wst) = (u16)(tc >> 16);
    }
    }
    if (!mainwg && b < 249) { WAIT_VM0(); arrive0(); }
    if (w == 4) {
      asm volatile("s_waitcnt vmcnt(0)" : "+v"(pe) :: "memory");
      if (mainwg) { if (__builtin_amdgcn_ballot_w64(pe < 32u) != 0) wait_cnt<8, 6>(c0p, 32u, pl0, pl1, pl2); }
      else if (b < 249) epcheck();
    }
    STAMP(14);
    epoch = ep + 1;
    asm volatile("s_waitcnt lgkmcnt(0)\n\ts_barrier" ::: "memory");
  }
  if (!mainwg) {
    const int fr = b - 224;
    {
      int u0 = fr < 25 ? 8 * fr + w : (w < 2 ? 160 + 2 * fr + w : 224);
      const int u1 = (fr >= 22 && fr < 25 && w < 4) ? 112 + 4 * fr + w : 224;
#pragma unroll 1
      for (int k = 0; k < 2; ++k) {
        if (u0 < (k ? 210 : 224)) {
          u32x4 v[7];
#pragma unroll
          for (int i = 0; i < 7; ++i) v[i] = LDIN((const u32x4*)(w2 + (size_t)(8 * u0) * D_ + lane * 8 + i * 512));
          char* img = smem + w * 7296;
#pragma unroll
          for (int i = 0; i < 7; ++i) { const int c = lane + 64 * i; *(u32x4*)(img + c * 16 + (c / 56) * 16) = v[i]; }
          const char* rb = img + qq * 912 + G * 32 + pp * 8;
#pragma unroll
          for (int i = 0; i < 7; ++i) {
            const u32x2 lo = trread(rb + i * 128), hh = trread(rb + i * 128 + 4 * 912);
            ST16((u32x4{lo.x, lo.y, hh.x, hh.y}), (int)WS_W2F + ((2 * i + hi) * 112 + (u0 >> 1)) * 1024 + (u0 & 1) * 512 + (lane & 31) * 16);
          }
        }
        u0 = u1;
      }
    }
    asm volatile("s_waitcnt vmcnt(0)\n\ts_barrier" ::: "memory");
    if (w == 4) {
      wait_cnt<8, 6>(c0p, 32u, pl0, pl1, pl2);
      asm volatile("s_waitcnt vmcnt(0)" ::: "memory");
      POLL_KEEP();
      if (b == 248 && lane == 0) __hip_atomic_store((GAS unsigned long long*)(ctl + EP_E), epoch, RLX_AGENT);
      STAMP(3);
      if (lane < 16) __hip_atomic_fetch_add(ctl + C1_CNT + 4608 + (lane * 9 + 8) * 32, 1u, RLX_AGENT);
    }
    asm volatile("s_barrier" ::: "memory");
    auto meet2 = [&](u32 want) __attribute__((always_inline)) {
      asm volatile("s_waitcnt lgkmcnt(0)\n\ts_mov_b64 exec, 1\n\tds_add_u32 %0, %1 offset:16\n\ts_mov_b64 exec, -1" :: "v"(lds0 + ARR_CNT + dl * 4), "v"(1) : "memory");
#pragma unroll 1
      for (unsigned spins = 0; spins < 64 * SPIN_CAP; ++spins) {
        if (*(volatile LAS u32*)(size_t)(lds0 + ARR_CNT + 16 + dl * 4) >= want) break;
        __builtin_amdgcn_s_sleep(1);
      }
      asm volatile("" ::: "memory");
    };
    if (q4 == 0) {
      wait_cnt<8, 6>(ctl + C1_CNT + dl * 4608 + ((b & 15) * 9 + lane) * 32, 28u, pl0, pl1, pl2);
    }
    meet2(4u);
    STAMP(4);
    {
      const int vt = 4 * (fr >> 1) + 2 * dl + (fr & 1);
      const char* xsrc = ws + WS_X1T + (size_t)vt * 28672 + q4 * 1024;
#pragma unroll
      for (int i = 0; i < 7; ++i) glds16(voff, xsrc + i * 4096, lds0 + w * 7168 + i * 1024);
      const char* sp = smem + w * 7168 + (8 * hi + qq) * 64 + (G & 1) * 32 + pp * 8;
      float s0 = 0.f, q0 = 0.f, s1 = 0.f, q1 = 0.f;
      static_for<0, 7>([&](auto ic) __attribute__((always_inline)) {
        constexpr int i = decltype(ic)::value;
        asm volatile("s_waitcnt vmcnt(%0)" :: "i"(6 - i) : "memory");
        const u32x2 lo = trread(sp + i * 1024), hh = trread(sp + i * 1024 + 256);
        s0 = dot2(lo.x, 0x3f803f80u, s0); q0 = dot2(lo.x, lo.x, q0); s1 = dot2(lo.y, 0x3f803f80u, s1); q1 = dot2(lo.y, lo.y, q1);
        s0 = dot2(hh.x, 0x3f803f80u, s0); q0 = dot2(hh.x, hh.x, q0); s1 = dot2(hh.y, 0x3f803f80u, s1); q1 = dot2(hh.y, hh.y, q1);
      });
      POLL_KEEP();
      auto r = __builtin_amdgcn_permlane32_swap(F2U(s0 + s1), F2U(q0 + q1), false, false);
      *(float*)(smem + 65536 + w * 256 + (lane & 31) * 8 + hi * 4) = U2F(r[0]) + U2F(r[1]);
      meet2(8u);
      if (q4 == 0) {
        const char* pt = smem + 65536 + dl * 1024 + (lane & 31) * 8;
        const f32x2 ts = (*(const f32x2*)pt + *(const f32x2*)(pt + 256)) + (*(const f32x2*)(pt + 512) + *(const f32x2*)(pt + 768));
        const float m = ts[0] * (1.f / D_);
        const float var = __builtin_fmaxf(ts[1] * (1.f / D_) - m * m, 0.f);
        const float sd = __builtin_amdgcn_sqrtf(rbf(rbf(var) + 1e-5f));
        if (lane < 32) __hip_atomic_store((GAS unsigned long long*)(ws + ST2_OFF) + 32 * vt + lane, (1ull << 32) | pkbf(-m, sd), RLX_AGENT);
      }
    }
    STAMP(5);
    return;
  }
  const int rt = 4 * (xcd >> 1) + idx / 7, ct = 7 * (xcd & 1) + idx % 7;
  const int ft = 8 * ct + w;
  const int r2 = 4 * xcd + idx / 7, dg = idx % 7, kq = w >> 1, dt = w & 1;
  const int te = 4 * (r2 >> 1) + (r2 & 1), tl = 4 * (r2 >> 1) + 3 - (r2 & 1);

  u32x4 wf[28];
  u32x4 wex;
  const int hbase = (int)WS_HF + (4 * rt * 112 + ft) * 1024 + lane * 16;

  u32x4 w2f[28];
  const int ke = w ^ ((dg & 1) << 2);
  const int rot = (r2 & 1) * 7168;
  const char* w2src = ws + WS_W2F + ((size_t)(2 * dg) * 112 + 14 * ke) * 1024;
  const char* wsrc = ws + WS_W13 + (size_t)ft * 28672;
#define WDEF(lo, hi_) static_for<lo, hi_>([&](auto ic) __attribute__((always_inline)) { constexpr int i = decltype(ic)::value; wf[i] = gload<(i & 3) * 1024>(voff, wsrc + (i & ~3) * 1024); })
#define ARRIVE1(n, stage) do { u32 old_; \
    asm volatile("s_waitcnt vmcnt(%3)\n\ts_mov_b64 exec, 1\n\tds_add_rtn_u32 %0, %1, %2 offset:%4\n\ts_mov_b64 exec, -1\n\ts_waitcnt lgkmcnt(0)" : "=&v"(old_) : "v"(lds0 + ARR_CNT), "v"(1), "i"(n), "i"(16 + 4 * (stage)) : "memory"); \
    if (__builtin_amdgcn_readfirstlane(old_) == 3u && lane < 16) __hip_atomic_fetch_add(ctl + C1_CNT + (stage) * 4608 + (lane * 9 + xcd) * 32, 1u, RLX_AGENT); } while (0)
  {
    const int a = lane & 15;
    const int l0 = 256 * w + 16 * a + 4 * G;
    const int xoff = ((l0 >> 5) * 448 + 2 * b) * 64 + (l0 & 31) * 2;
    STAMP(9);
    u32x2 xx[2]; u32x4 st[2]; float msv; f32x4 tb;
    asm volatile("global_load_dword %0, %6, %7 sc1\n\tglobal_load_dwordx2 %1, %8, %9 sc1\n\tglobal_load_dwordx4 %3, %10, %11 sc1\n\t"
                 "global_load_dwordx2 %2, %8, %9 offset:64 sc1\n\tglobal_load_dwordx4 %4, %10, %12 sc1\n\tglobal_load_dwordx4 %5, %13, %14 sc1"
                 : "=&v"(msv), "=&v"(xx[0]), "=&v"(xx[1]), "=&v"(st[0]), "=&v"(st[1]), "=&v"(tb)
                 : "v"((lane & 15) * 4), "s"(ws + WS_MODA + b * 64), "v"(xoff), "s"(ws + WS_XT), "v"(l0 * 4), "s"(ws + WS_ST1), "s"(ws + WS_ST1 + 8192),
                   "v"((tid < 448 ? tid : 447) * 16), "s"(ws + WS_MODB) : "memory");
    SB();
    {
      u32 kw[6];
#pragma unroll
      for (int e = 0; e < 6; ++e) kw[e] = (u32)*(const LAS u16*)(size_t)kga[2 * e] | ((u32)*(const LAS u16*)(size_t)kga[2 * e + 1] << 16);
      char* cp = smem + dl * CV_DSTR;
      u32 al[5];
#pragma unroll
      for (int i = 0; i < 5; ++i) al[i] = __builtin_amdgcn_alignbit(kw[i + 1], kw[i], 16);
      char* q = cp + t256 * 16;
      *(u32x4*)(q) = u32x4{kw[0], kw[1], kw[2], kw[3]};
      *(u32x4*)(q + 4160) = u32x4{al[0], al[1], al[2], al[3]};
      *(u32x4*)(q + 8320) = u32x4{kw[1], kw[2], kw[3], kw[4]};
      *(u32x4*)(q + 12480) = u32x4{al[1], al[2], al[3], al[4]};
    }
    SB();
    WDEF(0, W_N1);
    asm volatile("s_waitcnt vmcnt(%6)" : "+v"(msv), "+v"(xx[0]), "+v"(xx[1]), "+v"(st[0]), "+v"(st[1]), "+v"(tb) : "i"(W_N1) : "memory");
    msv = rbf(msv + dppf<0xB1>(msv));
    if (lane & 2) msv = rbf(1.f + msv);
#define MSV(i) U2F(__builtin_amdgcn_readlane(F2U(msv), i))
    float ga[2], b2v[2];
    const int swl = (lane ^ ((lane >> 5) << 3)) * 16;
#pragma unroll
    for (int i = 0; i < 2; ++i) {
      const float sa = MSV(8 * i), beta = MSV(8 * i + 2);
      ga[i] = MSV(8 * i + 4); b2v[i] = MSV(8 * i + 6);
      const float gd = ga[i] * (i ? bfhi(dskr) : bflo(dskr));
      const float xv[4] = {bflo(xx[i].x), bfhi(xx[i].x), bflo(xx[i].y), bfhi(xx[i].y)};
      float hv[4];
#pragma unroll
      for (int e = 0; e < 4; ++e)
        hv[e] = __builtin_fmaf(__builtin_fmaf(xv[e], U2F(st[0][e]), U2F(st[1][e])), beta, sa);
      *(u32x2*)(smem + i * CV_DSTR + 4 * 4160 + 512 + l0 * 2) = u32x2{pkbf(hv[0], hv[1]), pkbf(hv[2], hv[3])};
      f32x4 base;
#pragma unroll
      for (int e = 0; e < 4; ++e) base[e] = __builtin_fmaf(hv[e], gd, xv[e]);
      *(f32x4*)(smem + CV_RED + (dl * 40 + i * 20 + q4 * 5 + 4) * 1024 + swl) = base;
    }
    SB();
    POLL_KEEP();
    asm volatile("s_waitcnt lgkmcnt(0)\n\ts_barrier" ::: "memory");
    STAMP(1);
    f32x4 acc[8];
#pragma unroll
    for (int t = 0; t < 8; ++t) acc[t] = f32x4{0, 0, 0, 0};
    const int im = 15 - a;
    const u32 abase = lds0 + dl * CV_DSTR + (im & 3) * 4160 + (2016 + 8 * G + 4 * (im >> 2)) * 2 - q4 * 128 - 3648;
    const u32 bbase = lds0 + dl * CV_DSTR + 4 * 4160 + (256 - 16 + 16 * a + 8 * G) * 2 - q4 * 128 - 64;
    char* dump = smem + CV_RED + (dl * 20 + q4) * 1024 + swl;
    u32x2 alo[2][8], ahi[2][8]; u32x4 bfr[2][8];
#define CV_READ(cc, c1) asm volatile("ds_read_b64 %0, %3 offset:%5\n\tds_read_b64 %1, %3 offset:%6\n\tds_read_b128 %2, %4 offset:%7" \
      : "=&v"(alo[cc][c1]), "=&v"(ahi[cc][c1]), "=&v"(bfr[cc][c1]) \
      : "v"(abase), "v"(bbase), "i"(3648 - (8 * (c1) + (cc)) * 64), "i"(3656 - (8 * (c1) + (cc)) * 64), "i"(64 + (8 * (c1) - (cc)) * 64) : "memory")
#define CV_WAIT(n, cc, c1) asm volatile("s_waitcnt lgkmcnt(" #n ")" : "+v"(alo[cc][c1]), "+v"(ahi[cc][c1]), "+v"(bfr[cc][c1]), "+v"(alo[cc][(c1) + 1]), "+v"(ahi[cc][(c1) + 1]), "+v"(bfr[cc][(c1) + 1]))
#define CV_MM(cc, t, c1) acc[t] = __builtin_amdgcn_mfma_f32_16x16x32_bf16(__builtin_bit_cast(bf16x8, u32x4{alo[cc][c1].x, alo[cc][c1].y, ahi[cc][c1].x, ahi[cc][c1].y}), \
      __builtin_bit_cast(bf16x8, bfr[cc][(t) - (c1)]), acc[t], 0, 0, 0)
    const int ch = (lane >> 2) & 1;
    const float ga0 = ga[0], ga1 = ga[1], b20 = b2v[0], b21 = b2v[1];
    const float gac = ch ? ga1 : ga0, b2 = ch ? b21 : b20;
    auto finish = [&]() __attribute__((always_inline)) {
      const char* rp = smem + CV_RED + (dl * 40 + ch * 20 + q4 * 5) * 1024 + (lane & 1) * 512 + ((2 * (lane >> 3) + ((lane >> 1) & 1)) ^ ((lane & 1) << 3)) * 16;
      u32x4 o, ob;
#pragma unroll
      for (int i = 0; i < 2; ++i) {
        const char* q = rp + i * 256;
        const f32x4 p0 = *(const f32x4*)q, p1 = *(const f32x4*)(q + 1024), p2 = *(const f32x4*)(q + 2048), p3 = *(const f32x4*)(q + 3072), bs = *(const f32x4*)(q + 4096);
        float y[4];
#pragma unroll
        for (int e = 0; e < 4; ++e) y[e] = sfma(sadd(sadd(p0[e], p1[e]), sadd(p2[e], p3[e])), gac, bs[e]);
        o[2 * i] = pkbf(y[0], y[1]); o[2 * i + 1] = pkbf(y[2], y[3]);
      }
#pragma unroll
      for (int i = 0; i < 4; ++i) ob[i] = pkbf(smul(bflo(o[i]), b2), smul(bfhi(o[i]), b2));
      const int vt = 16 * q4 + 4 * (lane >> 4) + 2 * dl + ((lane >> 3) & 1);
      const int so = (vt * 448 + 2 * b) * 64 + (lane & 7) * 16;
      __builtin_amdgcn_raw_buffer_store_b128(ob, rs, (int)WS_XB + so, 0, 16);
      __builtin_amdgcn_raw_buffer_store_b128(o, rs, (int)WS_X1T + so, 0, 16);
      asm volatile("s_waitcnt lgkmcnt(0)" ::: "memory");
    };
#pragma unroll
    for (int cc = 0; cc < 2; ++cc)
#pragma unroll
      for (int c1 = 0; c1 < 4; ++c1) CV_READ(cc, c1);
    SB();
    WDEF(W_N1, W_N1 + 1);
    CV_WAIT(15, 0, 0);
    CV_MM(0, 0, 0); CV_MM(0, 1, 0); CV_MM(0, 1, 1); SB();
    CV_WAIT(12, 0, 2);
#pragma unroll
    for (int t = 2; t < 4; ++t)
#pragma unroll
      for (int c1 = 0; c1 <= t; ++c1) CV_MM(0, t, c1);
    SB();
    WDEF(W_N1 + 1, W_N1 + 2);
    CV_WAIT(6, 1, 0);
    CV_MM(1, 0, 0); CV_MM(1, 1, 0); CV_MM(1, 1, 1); SB();
    CV_WAIT(0, 1, 2);
    CV_MM(1, 2, 0); CV_MM(1, 2, 1); SB();
    *(f32x4*)(dump) = acc[0]; *(f32x4*)(dump + 5120) = acc[1]; SB();
    CV_MM(1, 2, 2); CV_MM(1, 3, 0); CV_MM(1, 3, 1); CV_MM(1, 3, 2); CV_MM(1, 3, 3);
    SB();
#pragma unroll
    for (int t = 2; t < 4; ++t) *(f32x4*)(dump + t * 5120) = acc[t];
    SB();
#pragma unroll
    for (int c1 = 4; c1 < 8; ++c1) CV_READ(1, c1);
    asm volatile("s_waitcnt lgkmcnt(12)\n\ts_barrier" ::: "memory");
    STAMP(2);
    if (dl == 0) finish();
    CV_WAIT(0, 1, 4); CV_WAIT(0, 1, 6);
    NP0S(13);
    SB();
    static_for<0, 2>([&](auto hc) __attribute__((always_inline)) {
      constexpr int h = decltype(hc)::value, cc = 1 - h;
      static_for<4, 8>([&](auto tc) __attribute__((always_inline)) {
        constexpr int t = decltype(tc)::value;
        if constexpr (h == 1 && t == 4) { CV_WAIT(6, 0, 0); CV_WAIT(6, 0, 2); CV_WAIT(6, 0, 4); }
        if constexpr (h == 1 && t == 6) CV_WAIT(0, 0, 6);
        static_for<0, t + 1>([&](auto cq) __attribute__((always_inline)) {
          constexpr int c1 = decltype(cq)::value, n = t * (t + 1) / 2 - 10 + c1;
          CV_MM(cc, t, c1);
          if constexpr (n % 3 == 2 && n / 3 < (h ? W_NB2 : W_NA)) { SB(); constexpr int i = W_N1 + W_NS + (h ? W_NA : 0) + n / 3; wf[i] = gload<(i & 3) * 1024>(voff, wsrc + (i & ~3) * 1024); SB(); }
          if constexpr (h == 1 && t >= 6 && c1 >= 1 && c1 <= (t == 6 ? 2 : 1)) { SB(); constexpr int u = t == 6 ? 3 + c1 : 6; *(f32x4*)(dump + 40960 + (u & 3) * 5120) = acc[u]; SB(); }
        });
        SB();
      });
      if constexpr (h == 0) {
#pragma unroll
        for (int c1 = 0; c1 < 8; ++c1) CV_READ(0, c1);
        if (dl == 0) { ARRIVE1(W_NA, 0); STAMP(3); }
        SB();
      }
    });
    *(f32x4*)(dump + 40960 + 3 * 5120) = acc[7];
    if (tid < 448) {
      const float be = rbf(1.f + rbf(tb[2] + tb[3]));
      *(u16*)(smem + TAB_SB + tid * 2) = (u16)(F2U(rbf(tb[0] + tb[1])) >> 16);
      *(u16*)(smem + TAB_SB + 896 + tid * 2) = (u16)(F2U(be) >> 16);
      *(u16*)(smem + TAB_SB + 1792 + tid * 2) = 0;
    }
    asm volatile("s_waitcnt lgkmcnt(0)\n\ts_barrier" ::: "memory");
    if (dl) finish();
    SB();
    NP0S(10);
  }
  constexpr int W_N2 = W_N1 + W_NS + W_NA + W_NB2;
  GAS u32* c1p = ctl + C1_CNT + ((b & 15) * 9 + lane) * 32;
  u32 pa = 28u;
  if (w == 0) POLL_ISSUE(pa, c1p, 255);
  const char* xsrc = ws + WS_XB + (size_t)(4 * rt) * 28672;
  {
    f32x4 accw = {0.f, 0.f, 0.f, 0.f};
    const char* tsb = smem + TAB_SB + (((lane ^ G) & 1) ? 1792 : ((lane >> 1) & 1) * 896) + hi * 16;
#define UVMM(p) accw = __builtin_amdgcn_mfma_f32_16x16x32_bf16(*(const bf16x8*)(tsb + (p) * 32), __builtin_bit_cast(bf16x8, wf[p]), accw, 0, 0, 0)
    asm volatile("s_waitcnt vmcnt(%0)" :: "i"(W_NB2) : "memory");
    PIN14(wf, 0); PIN14(wf, (W_N2 - W_NB2 - 14));
    static_for<0, W_N2 - W_NB2>([&](auto pc) __attribute__((always_inline)) {
      constexpr int p = decltype(pc)::value;
      UVMM(p);
      if constexpr (p % 2 == 1 && W_N2 + p / 2 < 28) { SB(); WDEF(W_N2 + p / 2, W_N2 + p / 2 + 1); SB(); }
    });
    SB();
    if (dl) {
      ARRIVE1(0, 1);
    } else {
      if (w == 0) {
        asm volatile("s_waitcnt vmcnt(%1)" : "+v"(pa) : "i"(28 - W_N2) : "memory");
        if (__builtin_amdgcn_ballot_w64(pa < 28u) != 0) wait_cnt<8, 6>(c1p, 28u, pl0, pl1, pl2);
        *(volatile LAS u32*)(size_t)(lds0 + ARR_CNT + 24) = 1u;
      } else {
#pragma unroll 1
        for (unsigned spins = 0; spins < 64 * SPIN_CAP; ++spins) {
          if (*(volatile LAS u32*)(size_t)(lds0 + ARR_CNT + 24) != 0u) break;
          __builtin_amdgcn_s_sleep(1);
        }
      }
      STAMP(4);
#pragma unroll
      for (int i = 0; i < X_N0; ++i) glds16(voff, xsrc + q4 * 1024 + i * 4096, lds0 + q4 * 1024 + i * 4096);
      asm volatile("s_waitcnt vmcnt(%0)" :: "i"(X_N0) : "memory");
    }
    PIN14(wf, 14);
#pragma unroll
    for (int p = W_N2 - W_NB2; p < 28; ++p) UVMM(p);
    const float us = (G & 1) ? accw[3] : accw[2], vs = (G & 1) ? accw[1] : accw[0];
    const float uh = rbf(us), vh = rbf(vs);
    wex = u32x4{hi ? 0u : pkbf(uh, us - uh), hi ? 0u : pkbf(vh, vs - vh), 0u, 0u};
    pin(wex);
  }
  xsrc += w * 1024;
  GAS u32* c2p = ctl + C2_CNT + rt * 1024 + (ft / 14) * 32;
  GAS u32* c2e = ctl + C2_CNT + te * 256 + ke * 32;
  GAS u32* c2l = ctl + C2_CNT + tl * 256 + lane * 32;
  u32 pv = 14;
#define ANNOUNCE(n, tt) asm volatile("s_waitcnt vmcnt(%3)\n\ts_mov_b64 exec, 1\n\tglobal_atomic_add %0, %1, %2 offset:%4\n\ts_mov_b64 exec, -1" :: "v"(0), "v"(1), "s"(c2p), "i"(n), "i"((tt) * 1024) : "memory")
  const char* w2b[4] = {w2src + rot + 3072, w2src + 114688 + rot + 3072, w2src + 7168 - rot + 3072, w2src + 114688 + 7168 - rot + 3072};
  asm volatile("" : "+s"(w2b[0]), "+s"(w2b[1]), "+s"(w2b[2]), "+s"(w2b[3]));
  {
    u32 rb = lds0 + (8 * hi + qq) * 64 + (G & 1) * 32 + pp * 8;
    GAS unsigned long long* sgp = (GAS unsigned long long*)(ws + ST2_OFF) + 128 * rt + (lane & 31);

    u32x4 xf[4];
    f32x16 acc[2];
    float nal, s2, ex[8], pr[8];
    unsigned long long sg = 0;
#define G1_READ(q) do { const LAS char* p_ = (const LAS char*)(size_t)rb + (q) * 1024; u32x2 lo = trread3(p_), hh = trread3(p_ + 256); xf[(q) & 3] = u32x4{lo.x, lo.y, hh.x, hh.y}; } while (0)
#define SG_WAIT(g, ptr) do { _Pragma("unroll 1") for (unsigned spins = 0; spins < SPIN_CAP; ++spins) { if (__builtin_amdgcn_ballot_w64((u32)((g) >> 32) != 1u) == 0) break; sgreload(g, ptr); } } while (0)
#define SG_APPLY(g, A) do { const u32 st_ = (u32)(g); const u32 mm_ = __builtin_amdgcn_perm(st_, st_, 0x01000100u), ss_ = __builtin_amdgcn_perm(st_, st_, 0x03020302u); \
      const float sd_ = bfhi(st_); nal = -1.4426950408889634f * __builtin_amdgcn_rcpf(sd_); s2 = sd_ * sd_; \
      pinf(nal); pinf(s2); A = MFMA32(wex, (u32x4{mm_, ss_, mm_, ss_}), A); } while (0)
#define EPI_STAGE(A, st, v) do { if constexpr ((st) == 0) { ex[v] = A[v] * nal; pr[v] = A[v] * A[8 + (v)]; pinf(pr[v]); } \
      if constexpr ((st) == 1) ex[v] = __builtin_amdgcn_exp2f(ex[v]); if constexpr ((st) == 2) ex[v] = __builtin_fmaf(ex[v], s2, s2); \
      if constexpr ((st) == 3) ex[v] = __builtin_amdgcn_rcpf(ex[v]); if constexpr ((st) == 4) { pr[v] *= ex[v]; pinf(pr[v]); } else pinf(ex[v]); } while (0)
#define EPI_STORE(tt) __builtin_amdgcn_raw_buffer_store_b128(u32x4{pkbf(pr[0], pr[1]), pkbf(pr[2], pr[3]), pkbf(pr[4], pr[5]), pkbf(pr[6], pr[7])}, rs, hbase, (tt) * 112 * 1024, 16)
    asm volatile("s_waitcnt vmcnt(%0)\n\ts_barrier" :: "i"(X_N0 - 7) : "memory");
    POLL_KEEP();
    NP0S(12);
    G1_READ(0); G1_READ(1);
    static_for<0, 112>([&](auto nc) __attribute__((always_inline)) {
      constexpr int n = decltype(nc)::value, t = n / 28, q = n % 28;
      constexpr int wl = g1_w2(n);
      if constexpr (t >= 1 && q == g1_e0(t) - 2) {
        asm volatile("s_waitcnt vmcnt(%1)" : "+v"(sg) : "i"(g1_vm(g1_sg(t - 1), n)) : "memory");
        if constexpr (t == 1) POLL_KEEP();
        SG_WAIT(sg, sgp + 32 * (t - 1));
      }
      if constexpr (n == 24) {
        asm volatile("s_waitcnt vmcnt(0)" ::: "memory");
        if (w == 0) wait_cnt<9, 6>(c1p + 4608, lane == 8 ? 32u : 28u, pl0, pl1, pl2);
        asm volatile("s_barrier" ::: "memory");
      }
      if constexpr (n == 52) asm volatile("s_waitcnt vmcnt(%0)\n\ts_barrier" :: "i"(g1_vm(31, n) - 1) : "memory");
      if constexpr (t >= 2 && q == g1_an(t)) ANNOUNCE(g1_vm(28 * (t - 1) + g1_st(t - 1), n), t - 2);
      if constexpr (n == 108) POLL_ISSUE(pv, c2e, 1);
      if constexpr (n == g1_sg(t)) sgload(sg, sgp + 32 * t);
      if constexpr (q == 26 && (t & 1)) { rb += 57344; asm volatile("" : "+v"(rb)); }
      SB();
      if constexpr ((n & 1) == 0) {
        if constexpr (n < 110) { G1_READ((n + 2) % 56); G1_READ((n + 3) % 56); SB(); __builtin_amdgcn_s_waitcnt(0xC47F); }
        else __builtin_amdgcn_s_waitcnt(0xC07F);
      }
      SB();
      if constexpr (q == 0) { _Pragma("unroll") for (int r = 0; r < 16; ++r) acc[t & 1][r] = 0.f; }
      acc[t & 1] = MFMA32(wf[q], xf[q & 3], acc[t & 1]);
      if constexpr (t >= 1) {
        if constexpr (q == g1_e0(t) - 2) SG_APPLY(sg, acc[(t - 1) & 1]);
        static_for<0, 5>([&](auto sc) __attribute__((always_inline)) {
          constexpr int st = decltype(sc)::value, g = q - g1_e0(t) - st, v = g / 2;
          if constexpr (g >= 0 && (g & 1) == 0 && v < 8) EPI_STAGE(acc[(t - 1) & 1], st, v);
        });
        if constexpr (q == g1_st(t)) { SB(); EPI_STORE(t - 1); }
      }
      if constexpr (n < 3) { SB(); glds16(voff, xsrc + (n + 4) * 8192, lds0 + w * 1024 + (n + 4) * 8192); }
      if constexpr (n >= 24 && n < 31) { SB(); glds16(voff, xsrc + (n - 17) * 8192, lds0 + w * 1024 + (n - 17) * 8192); }
      if constexpr (n == 31) { SB(); glds16(voff, ws + WS_X1T + ((size_t)(w >> 2 ? tl : te) * 448 + 64 * dg) * 64 + (w & 3) * 1024, lds0 + G2_X1 + w * 1024); }
      if constexpr (wl >= 0) { SB(); w2f[wl] = gload<((wl >> 1) % 7) * 1024 - 3072>(voff, w2b[(wl & 1) + (wl >= 14 ? 2 : 0)]); }
    });
    SB();
    STAMP(5);
    asm volatile("s_waitcnt vmcnt(%1)" : "+v"(sg) : "i"(g1_vm(g1_sg(3), 112)) : "memory");
    SG_WAIT(sg, sgp + 96);
    SG_APPLY(sg, acc[1]);
    static_for<0, 5>([&](auto sc) __attribute__((always_inline)) {
      static_for<0, 8>([&](auto vc) __attribute__((always_inline)) { EPI_STAGE(acc[1], decltype(sc)::value, decltype(vc)::value); });
    });
    EPI_STORE(3);
    SB();
  }
  {
    u32x4 gq[2];
    u32x4 hf[2][14];
    const char* hsrc[2] = {ws + WS_HF + ((size_t)te * 112 + 14 * ke) * 1024, ws + WS_HF + ((size_t)tl * 112 + 14 * ke) * 1024};
#define H_LOAD(j, lo, hi_) static_for<lo, hi_>([&](auto ic) __attribute__((always_inline)) { constexpr int i = decltype(ic)::value; hf[j][i] = gload<((i % 7) & 3) * 1024>(voff, hsrc[j] + (i >= 7 ? 7168 - rot : rot) + ((i % 7) >> 2) * 4096); })
    pl0 = pl1 = pl2 = 0;
#define ANNOUNCE23(n) asm volatile("s_waitcnt vmcnt(%3)\n\ts_mov_b64 exec, 3\n\tglobal_atomic_add %0, %1, %2 offset:2048\n\ts_mov_b64 exec, -1" :: "v"(lane << 10), "v"(1), "s"(c2p), "i"(n) : "memory")
    asm volatile("s_waitcnt vmcnt(1)\n\ts_barrier" : "+v"(pv) :: "memory");
    const bool slow = __builtin_amdgcn_readfirstlane(pv) < 14u;
    if (slow) {
      ANNOUNCE23(0);
      wait_cnt<1, 6>(c2e, 14u, pl0, pl1, pl2);
    }
    STAMP(6);
    { const char* gp = ws + WS_MODC + (64 * dg + 32 * dt + 8 * kq) * 8; gq[0] = gload<0>(hi * 32, gp); gq[1] = gload<16>(hi * 32, gp); }
    H_LOAD(0, 0, G2_B1);
    if (!slow) ANNOUNCE23(G2_B1 + 2);
    static_for<0, 2>([&](auto jc) __attribute__((always_inline)) {
      constexpr int j = decltype(jc)::value;
      f32x16 acc[2];
#pragma unroll
      for (int c = 0; c < 2; ++c)
#pragma unroll
        for (int r = 0; r < 16; ++r) acc[c][r] = 0.f;
      static_for<0, 14>([&](auto sc) __attribute__((always_inline)) {
        constexpr int s = decltype(sc)::value;
        asm volatile("s_waitcnt vmcnt(%1)" : "+v"(hf[j][s]) : "i"((s + G2_B1 - 1 < 13 ? G2_B1 - 1 : 13 - s) + (j == 1 && s < G2_B1 ? 1 : 0)) : "memory");
        if constexpr (s == 0) POLL_KEEP();
        if constexpr (j == 0 && s == 0) { PIN14(w2f, 0); PIN14(w2f, 14); pin(gq[0]); pin(gq[1]); pl0 = pl1 = pl2 = 14u; }
        if constexpr (j == 0 && s == G2_PL) { if (w == 0) POLL_ISSUE(pl0, c2l, 255); }
        if constexpr (j == 0 && s == 13) { if (w == 0) POLL_ISSUE(pl1, c2l, 255); }
        acc[0] = MFMA32(w2f[2 * s], hf[j][s], acc[0]);
        acc[1] = MFMA32(w2f[2 * s + 1], hf[j][s], acc[1]);
        SB();
        if constexpr (s + G2_B1 < 14) H_LOAD(j, s + G2_B1, s + G2_B1 + 1);
      });
      if constexpr (j == 0) {
      } else {
        STAMP(7);
      }
      char* red = smem + j * 65536;
#pragma unroll
      for (int P = 0; P < 8; ++P)
        *(f32x4*)(red + (P * 8 + w) * 1024 + lane * 16) = f32x4{acc[P & 1][4 * (P >> 1)], acc[P & 1][4 * (P >> 1) + 1], acc[P & 1][4 * (P >> 1) + 2], acc[P & 1][4 * (P >> 1) + 3]};
      if constexpr (j == 0) {
        if (w == 0) poll_loop<8>(c2l, 14u, pl0, pl1, pl2);
        asm volatile("s_waitcnt lgkmcnt(0)\n\ts_barrier" ::: "memory");
        H_LOAD(1, 0, G2_B1);
      }
      {
        const float gbv[4] = {rbf(U2F(gq[0].x) + U2F(gq[0].y)), rbf(U2F(gq[0].z) + U2F(gq[0].w)), rbf(U2F(gq[1].x) + U2F(gq[1].y)), rbf(U2F(gq[1].z) + U2F(gq[1].w))};
        if constexpr (j == 1) asm volatile("s_waitcnt lgkmcnt(0)\n\ts_barrier" ::: "memory");
        {
          constexpr int jj = j;
          const char* rp = smem + jj * 65536 + w * 8192 + lane * 16;
          const f32x4 y = ((*(const f32x4*)rp + *(const f32x4*)(rp + 1024)) + (*(const f32x4*)(rp + 2048) + *(const f32x4*)(rp + 3072))) +
                          ((*(const f32x4*)(rp + 4096) + *(const f32x4*)(rp + 5120)) + (*(const f32x4*)(rp + 6144) + *(const f32x4*)(rp + 7168)));
          const u32x2 xr = trread(smem + G2_X1 + jj * 4096 + (32 * dt + 8 * kq + 4 * hi + qq) * 64 + (G & 1) * 32 + pp * 8);
          u32x2 o; o.x = pkbf(__builtin_fmaf(y[0], gbv[0], bflo(xr.x)), __builtin_fmaf(y[1], gbv[1], bfhi(xr.x)));
          o.y = pkbf(__builtin_fmaf(y[2], gbv[2], bflo(xr.y)), __builtin_fmaf(y[3], gbv[3], bfhi(xr.y)));
          ostore(o, out + (size_t)(32 * (jj ? 2 * (r2 >> 1) + 33 - (r2 & 1) : r2) + (lane & 31)) * D_ + 64 * dg + 32 * dt + 8 * kq + 4 * hi);
        }
      }
      SB();
    });
  }
  NP0S(15);
  STAMP_OUT(stamps, 1024 + b);
  STAMP_OUT2(stamps, 2048 + b);
}

extern "C" void kernel_launch(void* const* d_in, const int* in_sizes, int n_in, void* d_out, int out_size, void* d_ws, size_t ws_size,
                              hipStream_t stream) {
  const u16* x = (const u16*)d_in[0]; const u16* logA = (const u16*)d_in[1]; const u16* Aim = (const u16*)d_in[2];
  const u16* Cr = (const u16*)d_in[3]; const u16* Ci = (const u16*)d_in[4]; const u16* logdt = (const u16*)d_in[5]; const u16* Dsk = (const u16*)d_in[6];
  const u16* w1 = (const u16*)d_in[8]; const u16* w3 = (const u16*)d_in[9]; const u16* w2 = (const u16*)d_in[10];
  const u16* c = (const u16*)d_in[11]; const u16* wm = (const u16*)d_in[12];
  char* ws = (char*)d_ws;
  unsigned long long* stamps = (unsigned long long*)(ws + (64u << 20));
  kf_main<<<256, 512, 0, stream>>>(x, logA, Aim, Cr, Ci, logdt, Dsk, w1, w3, w2, c, wm, ws, (u16*)d_out, stamps);
}
```

```cpp
#include <hip/hip_runtime.h>

typedef unsigned short u16;
typedef unsigned u32;
typedef u32 u32x4 __attribute__((ext_vector_type(4)));
typedef u32 u32x2 __attribute__((ext_vector_type(2)));
typedef short s16x4 __attribute__((ext_vector_type(4)));
typedef __bf16 bf16x8 __attribute__((ext_vector_type(8)));
typedef __bf16 bf16x2 __attribute__((ext_vector_type(2)));
typedef _Float16 f16x8 __attribute__((ext_vector_type(8)));
typedef _Float16 f16x2 __attribute__((ext_vector_type(2)));
typedef float f32x2 __attribute__((ext_vector_type(2)));
typedef float f32x4 __attribute__((ext_vector_type(4)));
typedef float f32x16 __attribute__((ext_vector_type(16)));
#define DI __device__ __forceinline__
#define U2F(x) __builtin_bit_cast(float, (u32)(x))
#define F2U(x) __builtin_bit_cast(u32, (float)(x))
#define I2F(x) __builtin_bit_cast(float, (int)(x))
#define F2I(x) __builtin_bit_cast(int, (float)(x))
#if defined(__HIP_DEVICE_COMPILE__)
#define NOPK __attribute__((target("no-packed-fp32-ops")))
#else
#define NOPK
#endif
#define LDSP(T, p) ((__attribute__((address_space(3))) T*)(p))
#define GAS __attribute__((address_space(1)))
#define RLX_AGENT __ATOMIC_RELAXED, __HIP_MEMORY_SCOPE_AGENT
#define SB() __builtin_amdgcn_sched_barrier(0)
#define LDIN(p) __builtin_nontemporal_load(p)
template <int V> struct IC { static constexpr int value = V; };
template <int I, int N, class F> __device__ __forceinline__ void static_for(F&& f) { if constexpr (I < N) { f(IC<I>{}); static_for<I + 1, N>(f); } }

constexpr int L_ = 2048, D_ = 448, N_ = 256, F_ = 1792;

constexpr int EP_E = 0;
constexpr int EP_R = 64;
constexpr int C0_CNT = 1024;
constexpr int C1_CNT = 5120;
constexpr int C2_CNT = 14336;
constexpr int ST2_OFF = 122880;
constexpr int ZERO_LO = 20480, ZERO_HI = 139264;
constexpr size_t WS_MOD = 139264;
constexpr size_t WS_ST1 = 167936;
constexpr size_t WS_XT = WS_ST1 + 16384;
constexpr size_t WS_X1T = WS_XT + 64 * 448 * 64;
constexpr size_t WS_XB = WS_X1T + 64 * 448 * 64;
constexpr size_t WS_W13 = WS_XB + 64 * 448 * 64;
constexpr size_t WS_W2F = WS_W13 + 112 * 28 * 1024;
constexpr size_t WS_HF = WS_W2F + 14 * 112 * 1024;
constexpr size_t WS_END = WS_HF + 64 * 112 * 1024;

DI float bflo(u32 v) { return U2F(v << 16); }
DI float bfhi(u32 v) { return U2F(v & 0xffff0000u); }
DI float bf2f(u16 v) { return U2F(((u32)v) << 16); }
DI u32 pkbf(float lo, float hi) { f32x2 p = {lo, hi}; return __builtin_bit_cast(u32, __builtin_convertvector(p, bf16x2)); }
DI u32 pkh(f32x2 p) { return __builtin_bit_cast(u32, __builtin_convertvector(p, f16x2)); }
DI float sadd(float a, float b) { float r; asm("v_add_f32 %0, %1, %2" : "=v"(r) : "v"(a), "v"(b)); return r; }
DI float smul(float a, float b) { float r; asm("v_mul_f32 %0, %1, %2" : "=v"(r) : "v"(a), "v"(b)); return r; }
DI float sfma(float a, float b, float c) { float r; asm("v_fma_f32 %0, %1, %2, %3" : "=v"(r) : "v"(a), "v"(b), "v"(c)); return r; }
DI float rbf(float f) { return bflo(pkbf(f, f)); }
DI float dot2(u32 a, u32 b, float c) { return __builtin_amdgcn_fdot2_f32_bf16(__builtin_bit_cast(bf16x2, a), __builtin_bit_cast(bf16x2, b), c, false); }
DI float fexp(float x) { return __builtin_amdgcn_exp2f(x * 1.4426950408889634f); }
DI float silu_bf(float c) { return rbf(c * __builtin_amdgcn_rcpf(1.f + fexp(-c))); }
template <int CTRL> DI float dppf(float x) { return I2F(__builtin_amdgcn_update_dpp(0, F2I(x), CTRL, 0xf, 0xf, false)); }
DI float sum16(float x) { x += dppf<0x128>(x); x += dppf<0x124>(x); x += dppf<0x122>(x); x += dppf<0x121>(x); return x; }
DI float max16(float x) { x = __builtin_fmaxf(x, dppf<0x128>(x)); x = __builtin_fmaxf(x, dppf<0x124>(x)); x = __builtin_fmaxf(x, dppf<0x122>(x)); x = __builtin_fmaxf(x, dppf<0x121>(x)); return x; }
DI float add32(float x) { auto r = __builtin_amdgcn_permlane32_swap(F2U(x), F2U(x), false, false); return U2F(r[0]) + U2F(r[1]); }
DI float add16(float x) { auto r = __builtin_amdgcn_permlane16_swap(F2U(x), F2U(x), false, false); return U2F(r[0]) + U2F(r[1]); }
DI float max64(float x) {
  x = max16(x);
  auto r = __builtin_amdgcn_permlane16_swap(F2U(x), F2U(x), false, false);
  x = __builtin_fmaxf(U2F(r[0]), U2F(r[1]));
  auto q = __builtin_amdgcn_permlane32_swap(F2U(x), F2U(x), false, false);
  return __builtin_fmaxf(U2F(q[0]), U2F(q[1]));
}
DI u32x2 trread(const char* p) { return __builtin_bit_cast(u32x2, __builtin_amdgcn_ds_read_tr16_b64_v4i16(LDSP(s16x4, p))); }
DI u32x2 trread3(const __attribute__((address_space(3))) char* p) { return __builtin_bit_cast(u32x2, __builtin_amdgcn_ds_read_tr16_b64_v4i16((__attribute__((address_space(3))) s16x4*)p)); }
DI void pinf(float& v) { asm volatile("" : "+v"(v)); }
DI void pin(u32x4& v) { asm volatile("" : "+v"(v)); }
DI void ostore(u32x2 v, u16* p) { *(u32x2*)p = v; }
DI f32x2 cmulp(f32x2 a, f32x2 z, f32x2 zs) { return __builtin_elementwise_fma(a.yx, zs, a * z.xx); }

#define STAMP_DECL
#define STAMP(i)
#define CSTAMP(i)
#define STAMP_OUT(buf, slot)
#define STAMP_OUT2(buf, slot)

#define MFMA32(a, b, c) __builtin_amdgcn_mfma_f32_32x32x16_bf16(__builtin_bit_cast(bf16x8, a), __builtin_bit_cast(bf16x8, b), c, 0, 0, 0)
#define ST16(v, off) __builtin_amdgcn_raw_buffer_store_b128(v, rs, (int)(off), 0, 16)
template <int IMM> DI u32x4 gload(u32 voff, const char* sbase) {
  u32x4 r; asm volatile("global_load_dwordx4 %0, %1, %2 offset:%3 sc1" : "=v"(r) : "v"(voff), "s"(sbase), "i"(IMM)); return r;
}
DI void glds16(u32 voff, const char* sbase, u32 ldsaddr) {
  asm volatile("s_mov_b32 m0, %2\n\ts_nop 0\n\tglobal_load_lds_dwordx4 %0, %1 sc1" :: "v"(voff), "s"(sbase), "s"(ldsaddr) : "memory");
}
#define PSLP 4
#define ARR_R 30
constexpr unsigned SPIN_CAP = 1u << 16;
template <int OFF> DI u32 ldu16(const u16* p) { u32 r; asm volatile("global_load_ushort %0, %1, off offset:%2" : "=v"(r) : "v"(p), "i"(OFF) : "memory"); return r; }
template <int OFF> DI u32x4 ldnt(const u16* p) { u32x4 r; asm volatile("global_load_dwordx4 %0, %1, off offset:%2 nt" : "=v"(r) : "v"(p), "i"(OFF) : "memory"); return r; }
DI void sgload(unsigned long long& g, GAS unsigned long long* p) { asm volatile("global_load_dwordx2 %0, %1, off sc1" : "+v"(g) : "v"(p) : "memory"); }
DI void sgwait(unsigned long long& g) { asm volatile("s_waitcnt vmcnt(1)" : "+v"(g) :: "memory"); }
DI void sgreload(unsigned long long& g, GAS unsigned long long* p) { asm volatile("global_load_dwordx2 %0, %1, off sc1\n\ts_waitcnt vmcnt(0)" : "+v"(g) : "v"(p) : "memory"); }
#define POLL_ISSUE(v, p, m) asm volatile("s_mov_b64 exec, %3\n\tglobal_atomic_or %0, %1, %2, off sc0\n\ts_mov_b64 exec, -1" : "+v"(v) : "v"(p), "v"(0), "i"(m) : "memory")
template <int NL> DI void poll_loop(GAS u32* p, u32 want, u32& v0, u32& v1, u32& v2) {
#pragma unroll 1
  for (unsigned spins = 0; spins < SPIN_CAP; ++spins) {
    POLL_ISSUE(v2, p, (1 << NL) - 1);
    asm volatile("s_waitcnt vmcnt(2)" : "+v"(v0) :: "memory");
    if (__builtin_amdgcn_ballot_w64(v0 < want) == 0) break;
    POLL_ISSUE(v0, p, (1 << NL) - 1);
    asm volatile("s_waitcnt vmcnt(2)" : "+v"(v1) :: "memory");
    if (__builtin_amdgcn_ballot_w64(v1 < want) == 0) break;
    POLL_ISSUE(v1, p, (1 << NL) - 1);
    asm volatile("s_waitcnt vmcnt(2)" : "+v"(v2) :: "memory");
    if (__builtin_amdgcn_ballot_w64(v2 < want) == 0) break;
  }
}
template <int NL, int SLP> DI void wait_cnt(GAS u32* p, u32 want, u32& v0, u32& v1, u32& v2) {
  v0 = want; v1 = want; v2 = want;
  POLL_ISSUE(v0, p, (1 << NL) - 1); __builtin_amdgcn_s_sleep(SLP);
  POLL_ISSUE(v1, p, (1 << NL) - 1); __builtin_amdgcn_s_sleep(SLP);
  poll_loop<NL>(p, want, v0, v1, v2);
}
#define POLL_KEEP() asm volatile("" :: "v"(pl0), "v"(pl1), "v"(pl2) : "memory")
#define WAIT_VM0() __builtin_amdgcn_s_waitcnt(0x0F70)
#define BAR() __builtin_amdgcn_s_barrier()

constexpr int CV_DSTR = 4 * 4160 + 4608;
constexpr int CV_RED = 2 * CV_DSTR;
constexpr int G1_ST = 4 * 28672;
constexpr int G2_RED = 4 * 28672;
constexpr int G2_X1 = 18 * 8192;
constexpr int TAB_GB = G2_X1 + 8192;
constexpr int TAB_SB = TAB_GB + 256;
constexpr int ARR_CNT = TAB_SB + 2688;
constexpr int KF_LDS = ARR_CNT + 32;
constexpr int KG_REG = 32 * 272;
constexpr int KG_IMG = 2 * CV_DSTR;
constexpr int TASK_LDS = KG_IMG + 8 * KG_REG;
constexpr int KG_TAB = TASK_LDS + 30464;
#define HSLOT stv[14]
#define WSLOT stv[0]
#define W2_N0 13
#define W13_SLP 1
#define G1MAP 0
#define CODEPF 32
#define X_N0 8
#define P0S(i)
#define NP0S(i) STAMP(i)
#define S2_POLL 10
#define FAIR 7
#define AN3 10
#define PV_N 105
#define G2_B1 5
#define G2_PL 12
#define EPI0 5
#define W_N0 2
#define W_N1 6
#define W_NS 2
#define W_NA 8
#define W_NB2 7
#define UV_N1 20
#define SC_SIG 6
#define SC_POLL 21
#define LAS __attribute__((address_space(3)))
#define PIN14(a, o) asm volatile("" : "+v"(a[o]), "+v"(a[o + 1]), "+v"(a[o + 2]), "+v"(a[o + 3]), "+v"(a[o + 4]), "+v"(a[o + 5]), "+v"(a[o + 6]), "+v"(a[o + 7]), \
    "+v"(a[o + 8]), "+v"(a[o + 9]), "+v"(a[o + 10]), "+v"(a[o + 11]), "+v"(a[o + 12]), "+v"(a[o + 13]) :: "memory")

constexpr int g1_e0(int t) { return t == 1 ? EPI0 : 3; }
constexpr int g1_st(int t) { return g1_e0(t) + 19; }
constexpr int g1_an(int t) { return t == 2 ? 14 : AN3; }
constexpr int g1_w2(int n) {
  if (n >= 34 && n < 34 + 14 * 3 && (n - 34) % 3 == 0) return (n - 34) / 3;
  if (n >= 84 && n < 84 + 23) { const int q = n - 84; if (q % 3 != 2) return 14 + q - q / 3 < 28 ? 14 + q - q / 3 : -1; }
  return -1;
}
constexpr int g1_sg(int t) { return t == 0 ? 24 : 28 * t + 10; }
constexpr int g1_vm(int a, int b) {
  int c = 0;
  for (int n = a; n < b; ++n) {
    const int t = n / 28, q = n % 28;
    if (n > a) { if (t >= 2 && q == g1_an(t)) ++c; if (n == PV_N) ++c; if (n == g1_sg(t)) ++c; }
    if (t >= 1 && q == g1_st(t) && n != a) ++c;
    if (n >= 24 && n < 32) ++c;
    if (g1_w2(n) >= 0) ++c;
  }
  return c;
}
__global__ __launch_bounds__(512) void kf_main(const u16* __restrict__ x, const u16* __restrict__ logA, const u16* __restrict__ Aim,
                                               const u16* __restrict__ Cr, const u16* __restrict__ Ci, const u16* __restrict__ logdt,
                                               const u16* __restrict__ Dsk, const u16* __restrict__ w1, const u16* __restrict__ w3,
                                               const u16* __restrict__ w2, const u16* __restrict__ cvec, const u16* __restrict__ wmod,
                                               char* __restrict__ ws, u16* __restrict__ out, unsigned long long* stamps) {
  __shared__ __attribute__((aligned(16))) char smem[KF_LDS];
  const int tid = __builtin_amdgcn_workitem_id_x(), lane = tid & 63;
  const int w = __builtin_amdgcn_readfirstlane(tid >> 6);
  const int b = __builtin_amdgcn_workgroup_id_x(), xcd = b & 7, idx = b >> 3;
  const int hi = lane >> 5;
  const int G = lane >> 4, qq = (lane & 15) >> 2, pp = lane & 3;
  const u32 voff = lane * 16;
  GAS u32* ctl = (GAS u32*)ws;
  auto rs = __builtin_amdgcn_make_buffer_rsrc(ws, 0, (int)WS_END, 0x00020000);
  const u32 lds0 = (u32)(size_t)LDSP(char, smem);
  STAMP_DECL;
  STAMP(0);
  u32 pl0 = 0, pl1 = 0, pl2 = 0;
  u32x4 cpf;
  { const char* pc; asm volatile("s_getpc_b64 %0" : "=s"(pc));
    const u32 o = (idx < CODEPF ? idx : 0) * 1024 + lane * 16;
    if (w == 7) asm volatile("global_load_dwordx4 %0, %1, %2" : "=v"(cpf) : "v"(o), "s"(pc) : "memory"); }
  const bool mainwg = b < 224;
  const int dl = w >> 2, q4 = w & 3, t256 = tid & 255;
  auto arrive0 = [&]() __attribute__((always_inline)) {
    u32 old;
    asm volatile("s_waitcnt vmcnt(0)\n\ts_mov_b64 exec, 1\n\tds_add_rtn_u32 %0, %1, %2\n\ts_mov_b64 exec, -1\n\ts_waitcnt lgkmcnt(0)" : "=&v"(old) : "v"(lds0 + ARR_CNT), "v"(1) : "memory");
    if (__builtin_amdgcn_readfirstlane(old) == (mainwg ? 4u : (b < 249 ? 8u : 0u)) && lane < 16) __hip_atomic_fetch_add(ctl + C0_CNT + (lane * 8 + xcd) * 32, 1u, RLX_AGENT);
  };
  GAS u32* c0p = ctl + C0_CNT + ((b & 15) * 8 + lane) * 32;
  int kg_wb, kg_wst;
  u32 kga[12];
  u32 dskr = 0;
  unsigned long long epoch;
  {
    asm volatile("" :: "s"(x), "s"(logA), "s"(Aim), "s"(Cr), "s"(Ci), "s"(logdt), "s"(Dsk), "s"(w1), "s"(w3), "s"(cvec), "s"(wmod), "s"(ws));
    u32 lar = 0, air = 0, crr = 0, cir = 0, ldt = 0;
    if (mainwg) {
      const int d = 2 * b + dl;
      asm volatile("global_load_ushort %0, %6, %7 nt\n\tglobal_load_ushort %1, %6, %8 nt\n\tglobal_load_ushort %4, %11, %12\n\tglobal_load_ushort %2, %6, %9 nt\n\tglobal_load_ushort %3, %6, %10 nt\n\tglobal_load_dword %5, %13, %14"
                   : "+v"(lar), "+v"(air), "+v"(crr), "+v"(cir), "+v"(ldt), "+v"(dskr) : "v"((d * N_ + t256) * 2), "s"(logA), "s"(Aim), "s"(Cr), "s"(Ci), "v"(d * 2), "s"(logdt), "v"(b * 4), "s"(Dsk) : "memory");
    }
    SB();
    unsigned long long ep = 0;
    u32 pe = 32u;
    const int task = b >= 249 ? 2 * b - 364 + dl : (dl || b >= 246 ? -1 : (b < 134 ? b : b + 14));
    char* stg = smem + (dl ? 0 : TASK_LDS);
    u32x4 v[7]; u32 cv[7];
    const bool isx = task < 64, isw = task >= 148;
    const char* tsb; u32 tstep, tvo;
    {
      const int m = task - (isw ? 148 : 64);
      const size_t px = (size_t)x, pw = q4 >= 2 ? (size_t)w3 : (size_t)w1, pm = (size_t)wmod;
      const u32 k1 = isw ? 28672u : 128u, k0 = isw ? 1792u : 1204224u;
      const u32 off = isx ? (u32)task * 28672u : (u32)(m >> 1) * k1 + (u32)(m & 1) * k0;
      tsb = (const char*)((isx ? px : (isw ? pw : pm)) + off);
      tstep = isx ? 128u : (isw ? 256u : 172032u);
      tvo = ((t256 >> (isw ? 4 : 3)) & (isw ? 7 : 31)) * (isx ? 896u : (isw ? 3584u : 5376u)) + (t256 & (isw ? 15 : 7)) * 16;
    }
#define taskload() do { _Pragma("unroll") for (int i = 0; i < 7; ++i) asm volatile("global_load_dwordx4 %0, %1, %2 nt" : "=v"(v[i]) : "v"(tvo), "s"(tsb + i * tstep) : "memory"); } while (0)
    auto cload = [&]() __attribute__((always_inline)) {
      const u16* cp = cvec + 224 * (task & 1) + (t256 >> 3);
      static_for<0, 7>([&](auto ic) __attribute__((always_inline)) { constexpr int i = decltype(ic)::value; cv[i] = ldu16<64 * i>(cp); });
    };
    if (task >= 0 && b < 249) taskload();
    if (task >= 64 && task < 134) {
      cload();
    } else if (task < 0 && !mainwg) {
      if (dl) {
        const int zo = ZERO_LO + (b - 224) * 8192 + t256 * 16;
        if (zo < ZERO_HI) ST16((u32x4{0, 0, 0, 0}), zo);
        if (zo + 4096 < ZERO_HI) ST16((u32x4{0, 0, 0, 0}), zo + 4096);
      }
      if (b == 248 && w == 0) {
        const unsigned long long e0 = __hip_atomic_load((GAS unsigned long long*)(ctl + EP_E), RLX_AGENT);
#pragma unroll
        for (int i = 0; i < 2; ++i) __hip_atomic_store(ctl + C0_CNT + i * 2048 + lane * 32, 0u, RLX_AGENT);
        WAIT_VM0();
        asm volatile("s_waitcnt vmcnt(0)" ::: "memory");
        if (lane < 16) __hip_atomic_store((GAS unsigned long long*)(ctl + EP_R + lane * 32), e0 + 1, RLX_AGENT);
      }
    }
    SB();
    P0S(15);
    if (tid < 8) *(u32*)(smem + ARR_CNT + tid * 4) = 0;
    asm volatile("s_waitcnt lgkmcnt(0)\n\ts_barrier" ::: "memory");
    SB();
    auto meet = [&](u32 want) __attribute__((always_inline)) {
      asm volatile("s_waitcnt lgkmcnt(0)\n\ts_mov_b64 exec, 1\n\tds_add_u32 %0, %1 offset:4\n\ts_mov_b64 exec, -1" :: "v"(lds0 + ARR_CNT + dl * 4), "v"(1) : "memory");
#pragma unroll 1
      for (unsigned spins = 0; spins < 16 * SPIN_CAP; ++spins) {
        if (*(volatile LAS u32*)(size_t)(lds0 + ARR_CNT + 4 + dl * 4) >= want) break;
        __builtin_amdgcn_s_sleep(1);
      }
      asm volatile("" ::: "memory");
    };
#pragma unroll
    for (int h = 0; h < 2; ++h) {
      const int lb = 2040 - 8 * t256 - 8 * h;
      const int kr = lb < 256 ? 0 : 24 - __builtin_clz(lb);
      const float mB = U2F((u32)(150 + kr) << 23) + (float)(lb < 0 ? 704 : lb);
      const u32 kpa = lds0 + (u32)(KG_TAB + dl * 1536) + 256u * kr - ((u32)(150 + kr) << 24);
#pragma unroll
      for (int e = 0; e < (h ? 4 : 8); ++e) { kga[8 * h + e] = (F2U(mB + (float)(7 - e)) << 1) + kpa; asm volatile("" : "+v"(kga[8 * h + e])); }
    }
    if (t256 == 0) *(u32x4*)(smem + KG_TAB + dl * 1536 + 1408) = u32x4{0, 0, 0, 0};
    {
      char* cp = smem + dl * CV_DSTR;
      if (mainwg) {
        if (t256 < 32) *(u32x4*)(cp + 4 * 4160 + t256 * 16) = u32x4{0, 0, 0, 0};
        else if (t256 < 40) *(u32x4*)(cp + ((t256 - 32) >> 1) * 4160 + 4096 + (t256 & 1) * 16) = u32x4{0, 0, 0, 0};
      }
    }
    const int s = lane & 31;
    {
      const int kw = s < 8 ? 0 : (s < 20 ? (s >> 2) - 1 : 3);
      int wb = s < 12 ? (s < 8 ? s : 248 + s) : (s < 16 ? 384 + ((s & 2) << 5) + (s & 1) : 32 * s);
      kg_wst = 16 >> kw;
      kg_wb = (wb > 672 ? 672 : wb) * 2 + KG_TAB + dl * 1536 + (8 * q4 + 4 * hi) * kg_wst;
    }
    char* reg = smem + KG_IMG + w * KG_REG;
    f16x8 bfr[8];
    f32x2 z8, z16; float scl = 0.f, fs = 0.f;
    unsigned long long tagr = 0;
    GAS u32* tagp = ctl + EP_R + (b & 15) * 32;
#define EP_ISSUE() asm volatile("s_mov_b64 exec, 1\n\tglobal_load_dwordx2 %1, %4, off sc1\n\tglobal_atomic_or_x2 %0, %2, %3, off sc0\n\ts_mov_b64 exec, -1" \
                     : "+&v"(tagr), "+&v"(ep) : "v"(tagp), "v"(0ull), "v"(ctl + EP_E) : "memory")
    auto epcheck = [&]() __attribute__((always_inline)) {
#pragma unroll 1
      for (unsigned spins = 0; spins < SPIN_CAP; ++spins) {
        asm volatile("s_waitcnt vmcnt(0)" : "+v"(tagr), "+v"(ep) :: "memory");
        if ((__builtin_amdgcn_ballot_w64(tagr != ep + 1) & 1) == 0) break;
        EP_ISSUE();
      }
      arrive0();
    };
    if (mainwg) {
      if (task < 0) asm volatile("s_waitcnt vmcnt(0)" ::: "memory"); else if (isx || isw) asm volatile("s_waitcnt vmcnt(7)" ::: "memory"); else asm volatile("s_waitcnt vmcnt(14)" ::: "memory");
      asm volatile("" : "+v"(lar), "+v"(air), "+v"(crr), "+v"(cir), "+v"(ldt), "+v"(dskr) :: "memory");
      P0S(12);
      if (w == 4) EP_ISSUE();
    }
    if (mainwg) {
      const float dt = rbf(fexp(bflo(ldt)));
      const float ar = -rbf(fexp(bflo(lar))) * dt * 1.4426950408889634f;
      const float ai = bflo(air) * dt;
      const float I2PH = 0.15915494f, I2PL = 6.4206382e-9f;
      const float th = ai * I2PH;
      const float tl = __builtin_fmaf(ai, I2PL, __builtin_fmaf(ai, I2PH, -th));
      auto zpow = [&](float sg) __attribute__((always_inline)) {
        float e = __builtin_amdgcn_exp2f(ar * sg);
        float rev = __builtin_fmaf(tl, sg, __builtin_amdgcn_fractf(th * sg));
        return f32x2{e * __builtin_amdgcn_cosf(rev), e * __builtin_amdgcn_sinf(rev)};
      };
      const f32x2 z1 = zpow(1.f), z256 = zpow(256.f);
      z16 = zpow(16.f);
      const f32x2 s1 = {-z1.y, z1.y}, s256 = {-z256.y, z256.y};
      u32 eb = ((crr & 0x7f80u) > (cir & 0x7f80u) ? (crr & 0x7f80u) : (cir & 0x7f80u));
      asm("s_nop 1\n\tv_max_u32_dpp %0, %0, %0 row_ror:8 row_mask:0xf bank_mask:0xf\n\ts_nop 1\n\tv_max_u32_dpp %0, %0, %0 row_ror:4 row_mask:0xf bank_mask:0xf\n\ts_nop 1\n\t"
          "v_max_u32_dpp %0, %0, %0 row_ror:2 row_mask:0xf bank_mask:0xf\n\ts_nop 1\n\tv_max_u32_dpp %0, %0, %0 row_ror:1 row_mask:0xf bank_mask:0xf\n\ts_nop 1" : "+v"(eb));
      {
        const u32 e0 = __builtin_amdgcn_readlane(eb, 0), e1 = __builtin_amdgcn_readlane(eb, 16), e2 = __builtin_amdgcn_readlane(eb, 32), e3 = __builtin_amdgcn_readlane(eb, 48);
        const u32 m0 = e0 > e1 ? e0 : e1, m1 = e2 > e3 ? e2 : e3;
        eb = (m0 > m1 ? m0 : m1) >> 7;
      }
      eb = eb < 1u ? 1u : (eb > 252u ? 252u : eb);
      scl = U2F((253u - eb) << 23);
      fs = 2.f * dt * U2F((eb + 1u) << 23);
      f32x2 lo[9], hp[9];
      lo[1] = z1; hp[1] = z256;
#pragma unroll
      for (int i = 2; i <= 8; ++i) { lo[i] = cmulp(lo[i - 1], z1, s1); hp[i] = cmulp(hp[i - 1], z256, s256); }
      const f32x2 a2 = cmulp(lo[2], z256, s256), a4 = cmulp(lo[4], z256, s256), a6 = cmulp(lo[6], z256, s256);
      const f32x2 b4 = cmulp(a4, z256, s256), c4 = cmulp(b4, z256, s256);
      const u32 col[21] = {0x00003c00u, pkh(lo[1]), pkh(lo[2]), pkh(lo[3]), pkh(lo[4]), pkh(lo[5]), pkh(lo[6]), pkh(lo[7]),
                           pkh(hp[1]), pkh(a2), pkh(a4), pkh(a6), pkh(hp[2]), pkh(b4), pkh(hp[3]), pkh(c4),
                           pkh(hp[4]), pkh(hp[5]), pkh(hp[6]), pkh(hp[7]), pkh(hp[8])};
#pragma unroll
      for (int i = 0; i < 21; ++i) *(u32*)(reg + i * 272 + lane * 4) = col[i];
      z8 = lo[8];
#pragma unroll
      for (int i = 0; i < 8; ++i) bfr[i] = *(const f16x8*)(reg + s * 272 + i * 32 + hi * 16);
      asm volatile("s_waitcnt lgkmcnt(0)" : "+v"(bfr[0]), "+v"(bfr[1]), "+v"(bfr[2]), "+v"(bfr[3]), "+v"(bfr[4]), "+v"(bfr[5]), "+v"(bfr[6]), "+v"(bfr[7]) :: "memory");
    }
    if (!mainwg) asm volatile("s_waitcnt vmcnt(0)" ::: "memory");
    asm volatile("" :: "v"(cpf));
    STAMP(8);
    if (w == 4) { if (!mainwg) EP_ISSUE(); if (mainwg || b >= 249) epcheck(); }
    if (b >= 249) { taskload(); cload(); }
#define PIN_V() asm volatile("s_waitcnt vmcnt(0)" : "+v"(v[0]), "+v"(v[1]), "+v"(v[2]), "+v"(v[3]), "+v"(v[4]), "+v"(v[5]), "+v"(v[6]) :: "memory")
    if (task >= 0 && task < 148) {
      PIN_V();
      P0S(13);
      if (task < 64) {
            const int row = t256 >> 3, p = t256 & 7;
            float sm = 0.f, sq = 0.f;
#pragma unroll
            for (int i = 0; i < 7; ++i) {
              pin(v[i]);
              *(u32x4*)(stg + row * 928 + (p + 8 * i) * 16) = v[i];
#pragma unroll
              for (int j = 0; j < 4; ++j) { sm = dot2(v[i][j], 0x3f803f80u, sm); sq = dot2(v[i][j], v[i][j], sq); }
            }
            sm += dppf<0xB1>(sm); sq += dppf<0xB1>(sq); sm += dppf<0x4E>(sm); sq += dppf<0x4E>(sq); sm += dppf<0x141>(sm); sq += dppf<0x141>(sq);
            const float m = sm * (1.f / D_);
            const float var = __builtin_fmaxf(sq * (1.f / D_) - m * m, 0.f);
            const float sd = rbf(__builtin_amdgcn_sqrtf(rbf(rbf(var) + 1e-5f)));
            const float rsd = __builtin_amdgcn_rcpf(sd);
            if (p < 2) *(float*)(stg + 29696 + p * 128 + row * 4) = p ? -rbf(m) * rsd : rsd;
        meet(4u);
        if (q4 == 0) __builtin_amdgcn_raw_buffer_store_b32(*(const u32*)(stg + 29696 + lane * 4), rs, (int)WS_ST1 + hi * 8192 + (task * 32 + (lane & 31)) * 4, 0, 16);
        const char* rb = stg + (8 * G + qq) * 928 + q4 * 224 + pp * 8;
#pragma unroll
        for (int i = 0; i < 7; ++i) {
          const u32x2 lo = trread(rb + i * 32), hh = trread(rb + i * 32 + 4 * 928);
          ST16((u32x4{lo.x, lo.y, hh.x, hh.y}), (int)WS_XT + task * 28672 + q4 * 7168 + (lane & 15) * 64 + G * 16 + i * 1024);
        }
      } else {
            float acc[8] = {0, 0, 0, 0, 0, 0, 0, 0};
#pragma unroll
            for (int i = 0; i < 7; ++i) {
              asm volatile("" : "+v"(cv[i]));
              const u32 sh = F2U(silu_bf(bflo(cv[i]))), sl = sh >> 16;
#pragma unroll
              for (int j = 0; j < 4; ++j) { acc[2 * j] = dot2(v[i][j], sl, acc[2 * j]); acc[2 * j + 1] = dot2(v[i][j], sh, acc[2 * j + 1]); }
            }
#pragma unroll
            for (int j = 0; j < 8; ++j) { acc[j] += dppf<0x128>(acc[j]); acc[j] = add32(acc[j]); }
            if ((lane & 40) == 0) {
              float* red = (float*)stg + (q4 * 2 + (lane >> 4)) * 64 + (lane & 7) * 8;
              *(f32x4*)red = f32x4{acc[0], acc[1], acc[2], acc[3]};
              *(f32x4*)(red + 4) = f32x4{acc[4], acc[5], acc[6], acc[7]};
            }
        meet(4u);
            if (q4 == 0) {
              const int m = task - 64, cg = m >> 1;
              float t = 0.f;
#pragma unroll
              for (int j = 0; j < 8; ++j) t += ((const float*)stg)[j * 64 + lane];
              __builtin_amdgcn_raw_buffer_store_b32(F2U(t), rs, (int)WS_MOD + ((cg / 7) * 896 + (m & 1) * 448 + (cg % 7) * 64 + lane) * 4, 0, 16);
            }
      }
    }
    if (task >= 148) {
      PIN_V();
      P0S(13);
#pragma unroll
      for (int i = 0; i < 7; ++i) *(u32x4*)(stg + (t256 >> 4) * 1824 + ((t256 & 15) + 16 * i) * 16) = v[i];
      meet(4u);
      const int m = task - 148;
      const char* rb = stg + (8 * (G & 1) + qq) * 1824 + (14 * q4 + hi) * 32 + (8 * (pp & 1) + 4 * (pp >> 1)) * 2;
      const int dst = (int)WS_W13 + ((56 * (m & 1) + 14 * q4 + hi) * 28 + (m >> 2)) * 1024 + ((m >> 1) & 1) * 512 + (lane & 31) * 16;
#pragma unroll
      for (int i = 0; i < 7; ++i) {
        const u32x2 lo = trread(rb + i * 64), hh = trread(rb + i * 64 + 4 * 1824);
        ST16((u32x4{lo.x, lo.y, hh.x, hh.y}), dst + i * 57344);
      }
    }
    P0S(10);
    if (mainwg) {
        pinf(scl);
        f32x2 ra = {bflo(crr) * scl, -bflo(cir) * scl};
        f32x2 rb = cmulp(ra, z8, f32x2{z8.y, -z8.y});
        const f32x2 s16c = {z16.y, -z16.y};
#pragma unroll
        for (int r = 0; r < 32; r += 2) {
          *(u32*)(reg + r * 272 + lane * 4) = pkh(ra); *(u32*)(reg + (r + 1) * 272 + lane * 4) = pkh(rb);
          if (r < 30) { ra = cmulp(ra, z16, s16c); rb = cmulp(rb, z16, s16c); }
          if (r == ARR_R) { if (task >= 0) { WAIT_VM0(); arrive0(); STAMP(11); } }
        }
        f32x16 acc = {};
#pragma unroll
        for (int i = 0; i < 8; ++i) {
          const f16x8 af = *(const f16x8*)(reg + s * 272 + i * 32 + hi * 16);
          acc = __builtin_amdgcn_mfma_f32_32x32x16_f16(af, bfr[i], acc, 0, 0, 0);
        }
#pragma unroll
        for (int qd = 0; qd < 4; ++qd)
          *(f32x4*)(reg + qd * 1024 + lane * 16) = f32x4{acc[4 * qd] * fs, acc[4 * qd + 1] * fs, acc[4 * qd + 2] * fs, acc[4 * qd + 3] * fs};
    }
    if (mainwg) {
      if (w == 4) POLL_ISSUE(pe, c0p, 255);
      meet(dl ? 4u : 8u);
    {
      const char* rp = smem + KG_IMG + dl * 4 * KG_REG + q4 * 1024 + lane * 16;
      const f32x4 t = (*(const f32x4*)rp + *(const f32x4*)(rp + KG_REG)) + (*(const f32x4*)(rp + 2 * KG_REG) + *(const f32x4*)(rp + 3 * KG_REG));
      const u32 ta = pkbf(t[0], t[1]), tc = pkbf(t[2], t[3]);
      char* q = smem + kg_wb;
      *(u16*)q = (u16)ta; *(u16*)(q + kg_wst) = (u16)(ta >> 16); *(u16*)(q + 2 * kg_wst) = (u16)tc; *(u16*)(q + 3 * kg_wst) = (u16)(tc >> 16);
    }
    }
    if (!mainwg && b < 249) { WAIT_VM0(); arrive0(); }
    if (w == 4) {
      asm volatile("s_waitcnt vmcnt(0)" : "+v"(pe) :: "memory");
      if (mainwg) { if (__builtin_amdgcn_ballot_w64(pe < 32u) != 0) wait_cnt<8, PSLP>(c0p, 32u, pl0, pl1, pl2); }
      else if (b < 249) epcheck();
    }
    STAMP(14);
    epoch = ep + 1;
    asm volatile("s_waitcnt lgkmcnt(0)\n\ts_barrier" ::: "memory");
  }
  if (!mainwg) {
    const int fr = b - 224;
    {
      int u0 = fr < 25 ? 8 * fr + w : (w < 2 ? 160 + 2 * fr + w : 224);
      const int u1 = (fr >= 22 && fr < 25 && w < 4) ? 112 + 4 * fr + w : 224;
#pragma unroll 1
      for (int k = 0; k < 2; ++k) {
        if (u0 < (k ? 210 : 224)) {
          u32x4 v[7];
#pragma unroll
          for (int i = 0; i < 7; ++i) v[i] = LDIN((const u32x4*)(w2 + (size_t)(8 * u0) * D_ + lane * 8 + i * 512));
          char* img = smem + w * 7296;
#pragma unroll
          for (int i = 0; i < 7; ++i) { const int c = lane + 64 * i; *(u32x4*)(img + c * 16 + (c / 56) * 16) = v[i]; }
          const char* rb = img + qq * 912 + G * 32 + pp * 8;
#pragma unroll
          for (int i = 0; i < 7; ++i) {
            const u32x2 lo = trread(rb + i * 128), hh = trread(rb + i * 128 + 4 * 912);
            ST16((u32x4{lo.x, lo.y, hh.x, hh.y}), (int)WS_W2F + ((2 * i + hi) * 112 + (u0 >> 1)) * 1024 + (u0 & 1) * 512 + (lane & 31) * 16);
          }
        }
        u0 = u1;
      }
    }
    asm volatile("s_waitcnt vmcnt(0)\n\ts_barrier" ::: "memory");
    if (w == 4) {
      wait_cnt<8, PSLP>(c0p, 32u, pl0, pl1, pl2);
      asm volatile("s_waitcnt vmcnt(0)" ::: "memory");
      POLL_KEEP();
      if (b == 248 && lane == 0) __hip_atomic_store((GAS unsigned long long*)(ctl + EP_E), epoch, RLX_AGENT);
      STAMP(3);
      if (lane < 16) __hip_atomic_fetch_add(ctl + C1_CNT + 4608 + (lane * 9 + 8) * 32, 1u, RLX_AGENT);
    }
    asm volatile("s_barrier" ::: "memory");
    auto meet2 = [&](u32 want) __attribute__((always_inline)) {
      asm volatile("s_waitcnt lgkmcnt(0)\n\ts_mov_b64 exec, 1\n\tds_add_u32 %0, %1 offset:16\n\ts_mov_b64 exec, -1" :: "v"(lds0 + ARR_CNT + dl * 4), "v"(1) : "memory");
#pragma unroll 1
      for (unsigned spins = 0; spins < 64 * SPIN_CAP; ++spins) {
        if (*(volatile LAS u32*)(size_t)(lds0 + ARR_CNT + 16 + dl * 4) >= want) break;
        __builtin_amdgcn_s_sleep(1);
      }
      asm volatile("" ::: "memory");
    };
    if (q4 == 0) {
      wait_cnt<8, PSLP>(ctl + C1_CNT + dl * 4608 + ((b & 15) * 9 + lane) * 32, 28u, pl0, pl1, pl2);
    }
    meet2(4u);
    STAMP(4);
    {
      const int vt = 4 * (fr >> 1) + 2 * dl + (fr & 1);
      const char* xsrc = ws + WS_X1T + (size_t)vt * 28672 + q4 * 1024;
#pragma unroll
      for (int i = 0; i < 7; ++i) glds16(voff, xsrc + i * 4096, lds0 + w * 7168 + i * 1024);
      const char* sp = smem + w * 7168 + (8 * hi + qq) * 64 + (G & 1) * 32 + pp * 8;
      float s0 = 0.f, q0 = 0.f, s1 = 0.f, q1 = 0.f;
      static_for<0, 7>([&](auto ic) __attribute__((always_inline)) {
        constexpr int i = decltype(ic)::value;
        asm volatile("s_waitcnt vmcnt(%0)" :: "i"(6 - i) : "memory");
        const u32x2 lo = trread(sp + i * 1024), hh = trread(sp + i * 1024 + 256);
        s0 = dot2(lo.x, 0x3f803f80u, s0); q0 = dot2(lo.x, lo.x, q0); s1 = dot2(lo.y, 0x3f803f80u, s1); q1 = dot2(lo.y, lo.y, q1);
        s0 = dot2(hh.x, 0x3f803f80u, s0); q0 = dot2(hh.x, hh.x, q0); s1 = dot2(hh.y, 0x3f803f80u, s1); q1 = dot2(hh.y, hh.y, q1);
      });
      POLL_KEEP();
      auto r = __builtin_amdgcn_permlane32_swap(F2U(s0 + s1), F2U(q0 + q1), false, false);
      *(float*)(smem + 65536 + w * 256 + (lane & 31) * 8 + hi * 4) = U2F(r[0]) + U2F(r[1]);
      meet2(8u);
      if (q4 == 0) {
        const char* pt = smem + 65536 + dl * 1024 + (lane & 31) * 8;
        const f32x2 ts = (*(const f32x2*)pt + *(const f32x2*)(pt + 256)) + (*(const f32x2*)(pt + 512) + *(const f32x2*)(pt + 768));
        const float m = ts[0] * (1.f / D_);
        const float var = __builtin_fmaxf(ts[1] * (1.f / D_) - m * m, 0.f);
        const float sd = __builtin_amdgcn_sqrtf(rbf(rbf(var) + 1e-5f));
        if (lane < 32) __hip_atomic_store((GAS unsigned long long*)(ws + ST2_OFF) + 32 * vt + lane, (1ull << 32) | pkbf(-m, sd), RLX_AGENT);
      }
    }
    STAMP(5);
    return;
  }
  const int rt = 4 * (xcd >> 1) + idx / 7, ct = 7 * (xcd & 1) + idx % 7;
  const int ft = 8 * ct + w;
  const int r2 = 4 * xcd + idx / 7, dg = idx % 7, kq = w >> 1, dt = w & 1;
  const int te = 4 * (r2 >> 1) + (r2 & 1), tl = 4 * (r2 >> 1) + 3 - (r2 & 1);

  u32x4 wf[28];
  u32x4 wex;
  const int hbase = (int)WS_HF + (4 * rt * 112 + ft) * 1024 + lane * 16;

  u32x4 w2f[28];
  const int ke = w ^ ((dg & 1) << 2);
  const int rot = (r2 & 1) * 7168;
  const char* w2src = ws + WS_W2F + ((size_t)(2 * dg) * 112 + 14 * ke) * 1024;
  const char* wsrc = ws + WS_W13 + (size_t)ft * 28672;
#define WDEF(lo, hi_) static_for<lo, hi_>([&](auto ic) __attribute__((always_inline)) { constexpr int i = decltype(ic)::value; wf[i] = gload<(i & 3) * 1024>(voff, wsrc + (i & ~3) * 1024); })
#define ARRIVE1(n, stage) do { u32 old_; \
    asm volatile("s_waitcnt vmcnt(%3)\n\ts_mov_b64 exec, 1\n\tds_add_rtn_u32 %0, %1, %2 offset:%4\n\ts_mov_b64 exec, -1\n\ts_waitcnt lgkmcnt(0)" : "=&v"(old_) : "v"(lds0 + ARR_CNT), "v"(1), "i"(n), "i"(16 + 4 * (stage)) : "memory"); \
    if (__builtin_amdgcn_readfirstlane(old_) == 3u && lane < 16) __hip_atomic_fetch_add(ctl + C1_CNT + (stage) * 4608 + (lane * 9 + xcd) * 32, 1u, RLX_AGENT); } while (0)
  {
    const int a = lane & 15;
    const int l0 = 256 * w + 16 * a + 4 * G;
    const int xoff = ((l0 >> 5) * 448 + 2 * b) * 64 + (l0 & 31) * 2;
    STAMP(9);
    const int mso = ((lane & 6) == 6 ? 8 + (lane & 1) : (lane & 7)) * 1792 + ((lane >> 3) & 1) * 4;
    u32x2 xx[2]; u32x4 st[2]; float msv, tb[4];
    asm volatile("global_load_dword %0, %5, %6 sc1\n\tglobal_load_dwordx2 %1, %7, %8 sc1\n\tglobal_load_dwordx4 %3, %9, %10 sc1\n\t"
                 "global_load_dwordx2 %2, %7, %8 offset:64 sc1\n\tglobal_load_dwordx4 %4, %9, %11 sc1"
                 : "=&v"(msv), "=&v"(xx[0]), "=&v"(xx[1]), "=&v"(st[0]), "=&v"(st[1])
                 : "v"(mso), "s"(ws + WS_MOD + b * 8), "v"(xoff), "s"(ws + WS_XT), "v"(l0 * 4), "s"(ws + WS_ST1), "s"(ws + WS_ST1 + 8192) : "memory");
    SB();
    {
      u32 kw[6];
#pragma unroll
      for (int e = 0; e < 6; ++e) kw[e] = (u32)*(const LAS u16*)(size_t)kga[2 * e] | ((u32)*(const LAS u16*)(size_t)kga[2 * e + 1] << 16);
      char* cp = smem + dl * CV_DSTR;
      u32 al[5];
#pragma unroll
      for (int i = 0; i < 5; ++i) al[i] = __builtin_amdgcn_alignbit(kw[i + 1], kw[i], 16);
      char* q = cp + t256 * 16;
      *(u32x4*)(q) = u32x4{kw[0], kw[1], kw[2], kw[3]};
      *(u32x4*)(q + 4160) = u32x4{al[0], al[1], al[2], al[3]};
      *(u32x4*)(q + 8320) = u32x4{kw[1], kw[2], kw[3], kw[4]};
      *(u32x4*)(q + 12480) = u32x4{al[1], al[2], al[3], al[4]};
    }
    SB();
    asm volatile("global_load_dword %0, %4, %5 offset:-3584 sc1\n\tglobal_load_dword %1, %4, %5 offset:-1792 sc1\n\tglobal_load_dword %2, %4, %5 sc1\n\tglobal_load_dword %3, %4, %5 offset:1792 sc1"
                 : "=&v"(tb[0]), "=&v"(tb[1]), "=&v"(tb[2]), "=&v"(tb[3]) : "v"((tid < 448 ? tid : 447) * 4), "s"(ws + WS_MOD + 8 * 1792) : "memory");
    WDEF(0, W_N1);
    asm volatile("s_waitcnt vmcnt(%5)" : "+v"(msv), "+v"(xx[0]), "+v"(xx[1]), "+v"(st[0]), "+v"(st[1]) : "i"(W_N1 + 4) : "memory");
    msv = rbf(msv + dppf<0xB1>(msv));
    if (lane & 2) msv = rbf(1.f + msv);
#define MSV(i) U2F(__builtin_amdgcn_readlane(F2U(msv), i))
    float ga[2], b2v[2];
    const int swl = (lane ^ ((lane >> 5) << 3)) * 16;
#pragma unroll
    for (int i = 0; i < 2; ++i) {
      const float sa = MSV(8 * i), beta = MSV(8 * i + 2);
      ga[i] = MSV(8 * i + 4); b2v[i] = MSV(8 * i + 6);
      const float gd = ga[i] * (i ? bfhi(dskr) : bflo(dskr));
      const float xv[4] = {bflo(xx[i].x), bfhi(xx[i].x), bflo(xx[i].y), bfhi(xx[i].y)};
      float hv[4];
#pragma unroll
      for (int e = 0; e < 4; ++e)
        hv[e] = __builtin_fmaf(__builtin_fmaf(xv[e], U2F(st[0][e]), U2F(st[1][e])), beta, sa);
      *(u32x2*)(smem + i * CV_DSTR + 4 * 4160 + 512 + l0 * 2) = u32x2{pkbf(hv[0], hv[1]), pkbf(hv[2], hv[3])};
      f32x4 base;
#pragma unroll
      for (int e = 0; e < 4; ++e) base[e] = __builtin_fmaf(hv[e], gd, xv[e]);
      *(f32x4*)(smem + CV_RED + (dl * 40 + i * 20 + q4 * 5 + 4) * 1024 + swl) = base;
    }
    SB();
    POLL_KEEP();
    asm volatile("s_waitcnt lgkmcnt(0)\n\ts_barrier" ::: "memory");
    STAMP(1);
    f32x4 acc[8];
#pragma unroll
    for (int t = 0; t < 8; ++t) acc[t] = f32x4{0, 0, 0, 0};
    const int im = 15 - a;
    const u32 abase = lds0 + dl * CV_DSTR + (im & 3) * 4160 + (2016 + 8 * G + 4 * (im >> 2)) * 2 - q4 * 128 - 3648;
    const u32 bbase = lds0 + dl * CV_DSTR + 4 * 4160 + (256 - 16 + 16 * a + 8 * G) * 2 - q4 * 128 - 64;
    char* dump = smem + CV_RED + (dl * 20 + q4) * 1024 + swl;
    u32x2 alo[2][8], ahi[2][8]; u32x4 bfr[2][8];
#define CV_READ(cc, c1) asm volatile("ds_read_b64 %0, %3 offset:%5\n\tds_read_b64 %1, %3 offset:%6\n\tds_read_b128 %2, %4 offset:%7" \
      : "=&v"(alo[cc][c1]), "=&v"(ahi[cc][c1]), "=&v"(bfr[cc][c1]) \
      : "v"(abase), "v"(bbase), "i"(3648 - (8 * (c1) + (cc)) * 64), "i"(3656 - (8 * (c1) + (cc)) * 64), "i"(64 + (8 * (c1) - (cc)) * 64) : "memory")
#define CV_WAIT(n, cc, c1) asm volatile("s_waitcnt lgkmcnt(" #n ")" : "+v"(alo[cc][c1]), "+v"(ahi[cc][c1]), "+v"(bfr[cc][c1]), "+v"(alo[cc][(c1) + 1]), "+v"(ahi[cc][(c1) + 1]), "+v"(bfr[cc][(c1) + 1]))
#define CV_MM(cc, t, c1) acc[t] = __builtin_amdgcn_mfma_f32_16x16x32_bf16(__builtin_bit_cast(bf16x8, u32x4{alo[cc][c1].x, alo[cc][c1].y, ahi[cc][c1].x, ahi[cc][c1].y}), \
      __builtin_bit_cast(bf16x8, bfr[cc][(t) - (c1)]), acc[t], 0, 0, 0)
    const int ch = (lane >> 2) & 1;
    const float ga0 = ga[0], ga1 = ga[1], b20 = b2v[0], b21 = b2v[1];
    const float gac = ch ? ga1 : ga0, b2 = ch ? b21 : b20;
    auto finish = [&]() __attribute__((always_inline)) {
      const char* rp = smem + CV_RED + (dl * 40 + ch * 20 + q4 * 5) * 1024 + (lane & 1) * 512 + ((2 * (lane >> 3) + ((lane >> 1) & 1)) ^ ((lane & 1) << 3)) * 16;
      u32x4 o, ob;
#pragma unroll
      for (int i = 0; i < 2; ++i) {
        const char* q = rp + i * 256;
        const f32x4 p0 = *(const f32x4*)q, p1 = *(const f32x4*)(q + 1024), p2 = *(const f32x4*)(q + 2048), p3 = *(const f32x4*)(q + 3072), bs = *(const f32x4*)(q + 4096);
        float y[4];
#pragma unroll
        for (int e = 0; e < 4; ++e) y[e] = sfma(sadd(sadd(p0[e], p1[e]), sadd(p2[e], p3[e])), gac, bs[e]);
        o[2 * i] = pkbf(y[0], y[1]); o[2 * i + 1] = pkbf(y[2], y[3]);
      }
#pragma unroll
      for (int i = 0; i < 4; ++i) ob[i] = pkbf(smul(bflo(o[i]), b2), smul(bfhi(o[i]), b2));
      const int vt = 16 * q4 + 4 * (lane >> 4) + 2 * dl + ((lane >> 3) & 1);
      const int so = (vt * 448 + 2 * b) * 64 + (lane & 7) * 16;
      __builtin_amdgcn_raw_buffer_store_b128(ob, rs, (int)WS_XB + so, 0, 16);
      __builtin_amdgcn_raw_buffer_store_b128(o, rs, (int)WS_X1T + so, 0, 16);
      asm volatile("s_waitcnt lgkmcnt(0)" ::: "memory");
    };
#pragma unroll
    for (int cc = 0; cc < 2; ++cc)
#pragma unroll
      for (int c1 = 0; c1 < 4; ++c1) CV_READ(cc, c1);
    SB();
    WDEF(W_N1, W_N1 + 1);
    CV_WAIT(15, 0, 0);
    CV_MM(0, 0, 0); CV_MM(0, 1, 0); CV_MM(0, 1, 1); SB();
    CV_WAIT(12, 0, 2);
#pragma unroll
    for (int t = 2; t < 4; ++t)
#pragma unroll
      for (int c1 = 0; c1 <= t; ++c1) CV_MM(0, t, c1);
    SB();
    WDEF(W_N1 + 1, W_N1 + 2);
    CV_WAIT(6, 1, 0);
    CV_MM(1, 0, 0); CV_MM(1, 1, 0); CV_MM(1, 1, 1); SB();
    CV_WAIT(0, 1, 2);
    CV_MM(1, 2, 0); CV_MM(1, 2, 1); SB();
    *(f32x4*)(dump) = acc[0]; *(f32x4*)(dump + 5120) = acc[1]; SB();
    CV_MM(1, 2, 2); CV_MM(1, 3, 0); CV_MM(1, 3, 1); CV_MM(1, 3, 2); CV_MM(1, 3, 3);
    SB();
#pragma unroll
    for (int t = 2; t < 4; ++t) *(f32x4*)(dump + t * 5120) = acc[t];
    SB();
#pragma unroll
    for (int c1 = 4; c1 < 8; ++c1) CV_READ(1, c1);
    asm volatile("s_waitcnt lgkmcnt(12)\n\ts_barrier" ::: "memory");
    STAMP(2);
    if (dl == 0) finish();
    CV_WAIT(0, 1, 4); CV_WAIT(0, 1, 6);
    NP0S(13);
    SB();
    static_for<0, 2>([&](auto hc) __attribute__((always_inline)) {
      constexpr int h = decltype(hc)::value, cc = 1 - h;
      static_for<4, 8>([&](auto tc) __attribute__((always_inline)) {
        constexpr int t = decltype(tc)::value;
        if constexpr (h == 1 && t == 4) { CV_WAIT(6, 0, 0); CV_WAIT(6, 0, 2); CV_WAIT(6, 0, 4); }
        if constexpr (h == 1 && t == 6) CV_WAIT(0, 0, 6);
        static_for<0, t + 1>([&](auto cq) __attribute__((always_inline)) {
          constexpr int c1 = decltype(cq)::value, n = t * (t + 1) / 2 - 10 + c1;
          CV_MM(cc, t, c1);
          if constexpr (n % 3 == 2 && n / 3 < (h ? W_NB2 : W_NA)) { SB(); constexpr int i = W_N1 + W_NS + (h ? W_NA : 0) + n / 3; wf[i] = gload<(i & 3) * 1024>(voff, wsrc + (i & ~3) * 1024); SB(); }
          if constexpr (h == 1 && t >= 6 && c1 >= 1 && c1 <= (t == 6 ? 2 : 1)) { SB(); constexpr int u = t == 6 ? 3 + c1 : 6; *(f32x4*)(dump + 40960 + (u & 3) * 5120) = acc[u]; SB(); }
        });
        SB();
      });
      if constexpr (h == 0) {
#pragma unroll
        for (int c1 = 0; c1 < 8; ++c1) CV_READ(0, c1);
        if (dl == 0) { ARRIVE1(W_NA, 0); STAMP(3); }
        SB();
      }
    });
    *(f32x4*)(dump + 40960 + 3 * 5120) = acc[7];
    asm volatile("s_waitcnt vmcnt(%4)" : "+v"(tb[0]), "+v"(tb[1]), "+v"(tb[2]), "+v"(tb[3]) : "i"(W_NA + W_NB2) : "memory");
    if (tid < 448) {
      const float be = rbf(1.f + rbf(tb[2] + tb[3]));
      *(u16*)(smem + TAB_SB + tid * 2) = (u16)(F2U(rbf(tb[0] + tb[1])) >> 16);
      *(u16*)(smem + TAB_SB + 896 + tid * 2) = (u16)(F2U(be) >> 16);
      *(u16*)(smem + TAB_SB + 1792 + tid * 2) = 0;
    }
    asm volatile("s_waitcnt lgkmcnt(0)\n\ts_barrier" ::: "memory");
    if (dl) finish();
    SB();
    NP0S(10);
  }
  constexpr int W_N2 = W_N1 + W_NS + W_NA + W_NB2;
  GAS u32* c1p = ctl + C1_CNT + ((b & 15) * 9 + lane) * 32;
  u32 pa = 28u;
  if (w == 0) POLL_ISSUE(pa, c1p, 255);
  const char* xsrc = ws + WS_XB + (size_t)(4 * rt) * 28672;
  {
    f32x4 accw = {0.f, 0.f, 0.f, 0.f};
    const char* tsb = smem + TAB_SB + (((lane ^ G) & 1) ? 1792 : ((lane >> 1) & 1) * 896) + hi * 16;
#define UVMM(p) accw = __builtin_amdgcn_mfma_f32_16x16x32_bf16(*(const bf16x8*)(tsb + (p) * 32), __builtin_bit_cast(bf16x8, wf[p]), accw, 0, 0, 0)
    asm volatile("s_waitcnt vmcnt(%0)" :: "i"(W_NB2) : "memory");
    PIN14(wf, 0); PIN14(wf, (W_N2 - W_NB2 - 14));
    static_for<0, W_N2 - W_NB2>([&](auto pc) __attribute__((always_inline)) {
      constexpr int p = decltype(pc)::value;
      UVMM(p);
      if constexpr (p % 2 == 1 && W_N2 + p / 2 < 28) { SB(); WDEF(W_N2 + p / 2, W_N2 + p / 2 + 1); SB(); }
    });
    SB();
    if (dl) {
      ARRIVE1(0, 1);
    } else {
      if (w == 0) {
        asm volatile("s_waitcnt vmcnt(%1)" : "+v"(pa) : "i"(28 - W_N2) : "memory");
        if (__builtin_amdgcn_ballot_w64(pa < 28u) != 0) wait_cnt<8, PSLP>(c1p, 28u, pl0, pl1, pl2);
        *(volatile LAS u32*)(size_t)(lds0 + ARR_CNT + 24) = 1u;
      } else {
#pragma unroll 1
        for (unsigned spins = 0; spins < 64 * SPIN_CAP; ++spins) {
          if (*(volatile LAS u32*)(size_t)(lds0 + ARR_CNT + 24) != 0u) break;
          __builtin_amdgcn_s_sleep(1);
        }
      }
      STAMP(4);
#pragma unroll
      for (int i = 0; i < X_N0; ++i) glds16(voff, xsrc + q4 * 1024 + i * 4096, lds0 + q4 * 1024 + i * 4096);
      asm volatile("s_waitcnt vmcnt(%0)" :: "i"(X_N0) : "memory");
    }
    PIN14(wf, 14);
#pragma unroll
    for (int p = W_N2 - W_NB2; p < 28; ++p) UVMM(p);
    const float us = (G & 1) ? accw[3] : accw[2], vs = (G & 1) ? accw[1] : accw[0];
    const float uh = rbf(us), vh = rbf(vs);
    wex = u32x4{hi ? 0u : pkbf(uh, us - uh), hi ? 0u : pkbf(vh, vs - vh), 0u, 0u};
    pin(wex);
  }
  xsrc += w * 1024;
  const u32 c1want = lane == 8 ? 32u : 28u;
  pa = c1want;
  GAS u32* c2p = ctl + C2_CNT + rt * 1024 + (ft / 14) * 32;
  GAS u32* c2e = ctl + C2_CNT + te * 256 + ke * 32;
  GAS u32* c2l = ctl + C2_CNT + tl * 256 + lane * 32;
  u32 pv = 14;
#define ANNOUNCE(n, tt) asm volatile("s_waitcnt vmcnt(%3)\n\ts_mov_b64 exec, 1\n\tglobal_atomic_add %0, %1, %2 offset:%4\n\ts_mov_b64 exec, -1" :: "v"(0), "v"(1), "s"(c2p), "i"(n), "i"((tt) * 1024) : "memory")
  const char* w2b[4] = {w2src + rot + 3072, w2src + 114688 + rot + 3072, w2src + 7168 - rot + 3072, w2src + 114688 + 7168 - rot + 3072};
  asm volatile("" : "+s"(w2b[0]), "+s"(w2b[1]), "+s"(w2b[2]), "+s"(w2b[3]));
  {
    u32 rb = lds0 + (8 * hi + qq) * 64 + (G & 1) * 32 + pp * 8;
    GAS unsigned long long* sgp = (GAS unsigned long long*)(ws + ST2_OFF) + 128 * rt + (lane & 31);

    u32x4 xf[4];
    f32x16 acc[2];
    float nal, s2, ex[8], pr[8];
    unsigned long long sg = 0;
#define G1_READ(q) do { const LAS char* p_ = (const LAS char*)(size_t)rb + (q) * 1024; u32x2 lo = trread3(p_), hh = trread3(p_ + 256); xf[(q) & 3] = u32x4{lo.x, lo.y, hh.x, hh.y}; } while (0)
#define SG_WAIT(g, ptr) do { _Pragma("unroll 1") for (unsigned spins = 0; spins < SPIN_CAP; ++spins) { if (__builtin_amdgcn_ballot_w64((u32)((g) >> 32) != 1u) == 0) break; sgreload(g, ptr); } } while (0)
#define SG_APPLY(g, A) do { const u32 st_ = (u32)(g); const u32 mm_ = __builtin_amdgcn_perm(st_, st_, 0x01000100u), ss_ = __builtin_amdgcn_perm(st_, st_, 0x03020302u); \
      const float sd_ = bfhi(st_); nal = -1.4426950408889634f * __builtin_amdgcn_rcpf(sd_); s2 = sd_ * sd_; \
      pinf(nal); pinf(s2); A = MFMA32(wex, (u32x4{mm_, ss_, mm_, ss_}), A); } while (0)
#define EPI_STAGE(A, st, v) do { if constexpr ((st) == 0) { ex[v] = A[v] * nal; pr[v] = A[v] * A[8 + (v)]; pinf(pr[v]); } \
      if constexpr ((st) == 1) ex[v] = __builtin_amdgcn_exp2f(ex[v]); if constexpr ((st) == 2) ex[v] = __builtin_fmaf(ex[v], s2, s2); \
      if constexpr ((st) == 3) ex[v] = __builtin_amdgcn_rcpf(ex[v]); if constexpr ((st) == 4) { pr[v] *= ex[v]; pinf(pr[v]); } else pinf(ex[v]); } while (0)
#define EPI_STORE(tt) __builtin_amdgcn_raw_buffer_store_b128(u32x4{pkbf(pr[0], pr[1]), pkbf(pr[2], pr[3]), pkbf(pr[4], pr[5]), pkbf(pr[6], pr[7])}, rs, hbase + (tt) * 112 * 1024, 0, 16)
    asm volatile("s_waitcnt vmcnt(%0)\n\ts_barrier" :: "i"(X_N0 - 7) : "memory");
    POLL_KEEP();
    NP0S(12);
    G1_READ(0); G1_READ(1);
    static_for<0, 112>([&](auto nc) __attribute__((always_inline)) {
      constexpr int n = decltype(nc)::value, t = n / 28, q = n % 28;
      constexpr int wl = g1_w2(n);
      if constexpr (n % FAIR == 0) __builtin_amdgcn_s_setprio(3 - ((n / FAIR) & 3));
      if constexpr (t >= 1 && q == g1_e0(t) - 2) {
        asm volatile("s_waitcnt vmcnt(%1)" : "+v"(sg) : "i"(g1_vm(g1_sg(t - 1), n)) : "memory");
        if constexpr (t == 1) POLL_KEEP();
        SG_WAIT(sg, sgp + 32 * (t - 1));
      }
      if constexpr (n == S2_POLL) { if (w == 0) POLL_ISSUE(pa, c1p + 4608, 511); }
      if constexpr (n == 24) {
        asm volatile("s_waitcnt vmcnt(0)" : "+v"(pa) :: "memory");
        if (w == 0) { if (__builtin_amdgcn_ballot_w64(pa < c1want) != 0) wait_cnt<9, PSLP>(c1p + 4608, c1want, pl0, pl1, pl2); }
        asm volatile("s_barrier" ::: "memory");
      }
      if constexpr (n == 52) asm volatile("s_waitcnt vmcnt(%0)\n\ts_barrier" :: "i"(g1_vm(31, n) - 1) : "memory");
      if constexpr (t >= 2 && q == g1_an(t)) ANNOUNCE(g1_vm(28 * (t - 1) + g1_st(t - 1), n), t - 2);
      if constexpr (n == PV_N) POLL_ISSUE(pv, c2e, 1);
      if constexpr (n == g1_sg(t)) sgload(sg, sgp + 32 * t);
      if constexpr (q == 26 && (t & 1)) { rb += 57344; asm volatile("" : "+v"(rb)); }
      SB();
      if constexpr ((n & 1) == 0) {
        if constexpr (n < 110) { G1_READ((n + 2) % 56); G1_READ((n + 3) % 56); SB(); __builtin_amdgcn_s_waitcnt(0xC47F); }
        else __builtin_amdgcn_s_waitcnt(0xC07F);
      }
      SB();
      if constexpr (q == 0) { _Pragma("unroll") for (int r = 0; r < 16; ++r) acc[t & 1][r] = 0.f; }
      acc[t & 1] = MFMA32(wf[q], xf[q & 3], acc[t & 1]);
      if constexpr (t >= 1) {
        if constexpr (q == g1_e0(t) - 2) SG_APPLY(sg, acc[(t - 1) & 1]);
        static_for<0, 5>([&](auto sc) __attribute__((always_inline)) {
          constexpr int st = decltype(sc)::value, g = q - g1_e0(t) - st, v = g / 2;
          if constexpr (g >= 0 && (g & 1) == 0 && v < 8) EPI_STAGE(acc[(t - 1) & 1], st, v);
        });
        if constexpr (q == g1_st(t)) { SB(); EPI_STORE(t - 1); }
      }
      if constexpr (n == 108) {
        asm volatile("s_waitcnt vmcnt(%1)" : "+v"(sg) : "i"(g1_vm(g1_sg(3), n + 1)) : "memory");
        SG_WAIT(sg, sgp + 96);
        SG_APPLY(sg, acc[1]);
      }
      if constexpr (n < 3) { SB(); glds16(voff, xsrc + (n + 4) * 8192, lds0 + w * 1024 + (n + 4) * 8192); }
      if constexpr (n >= 24 && n < 31) { SB(); glds16(voff, xsrc + (n - 17) * 8192, lds0 + w * 1024 + (n - 17) * 8192); }
      if constexpr (n == 31) { SB(); glds16(voff, ws + WS_X1T + ((size_t)(w >> 2 ? tl : te) * 448 + 64 * dg) * 64 + (w & 3) * 1024, lds0 + G2_X1 + w * 1024); }
      if constexpr (wl >= 0) { SB(); w2f[wl] = gload<((wl >> 1) % 7) * 1024 - 3072>(voff, w2b[(wl & 1) + (wl >= 14 ? 2 : 0)]); }
    });
    SB();
    __builtin_amdgcn_s_setprio(0);
    STAMP(5);
    static_for<0, 5>([&](auto sc) __attribute__((always_inline)) {
      static_for<0, 8>([&](auto vc) __attribute__((always_inline)) { EPI_STAGE(acc[1], decltype(sc)::value, decltype(vc)::value); });
    });
    EPI_STORE(3);
    SB();
  }
  {
    u32x4 gq[2];
    u32x4 hf[2][14];
    const char* hsrc[2] = {ws + WS_HF + ((size_t)te * 112 + 14 * ke) * 1024, ws + WS_HF + ((size_t)tl * 112 + 14 * ke) * 1024};
#define H_LOAD(j, lo, hi_) static_for<lo, hi_>([&](auto ic) __attribute__((always_inline)) { constexpr int i = decltype(ic)::value; hf[j][i] = gload<((i % 7) & 3) * 1024>(voff, hsrc[j] + (i >= 7 ? 7168 - rot : rot) + ((i % 7) >> 2) * 4096); })
    pl0 = pl1 = pl2 = 0;
#define ANNOUNCE23(n) asm volatile("s_waitcnt vmcnt(%3)\n\ts_mov_b64 exec, 3\n\tglobal_atomic_add %0, %1, %2 offset:2048\n\ts_mov_b64 exec, -1" :: "v"(lane << 10), "v"(1), "s"(c2p), "i"(n) : "memory")
    ANNOUNCE(1, 2);
    asm volatile("s_barrier" : "+v"(pv) :: "memory");
#undef ANNOUNCE23
#define ANNOUNCE23(n) ANNOUNCE((n) + 1, 3)
    const bool slow = __builtin_amdgcn_readfirstlane(pv) < 14u;
    if (slow) {
      ANNOUNCE23(0);
      wait_cnt<1, PSLP>(c2e, 14u, pl0, pl1, pl2);
    }
    STAMP(6);
    { const char* gp = ws + WS_MOD + 10 * 1792 + (64 * dg + 32 * dt + 8 * kq) * 4; gq[0] = gload<0>(hi * 16, gp); gq[1] = gload<1792>(hi * 16, gp); }
    H_LOAD(0, 0, G2_B1);
    if (!slow) ANNOUNCE23(G2_B1 + 2);
    static_for<0, 2>([&](auto jc) __attribute__((always_inline)) {
      constexpr int j = decltype(jc)::value;
      f32x16 acc[2];
#pragma unroll
      for (int c = 0; c < 2; ++c)
#pragma unroll
        for (int r = 0; r < 16; ++r) acc[c][r] = 0.f;
      static_for<0, 14>([&](auto sc) __attribute__((always_inline)) {
        constexpr int s = decltype(sc)::value;
        asm volatile("s_waitcnt vmcnt(%1)" : "+v"(hf[j][s]) : "i"((s + G2_B1 - 1 < 13 ? G2_B1 - 1 : 13 - s) + (j == 1 && s < G2_B1 ? 1 : 0)) : "memory");
        if constexpr (s == 0) POLL_KEEP();
        if constexpr (j == 0 && s == 0) { PIN14(w2f, 0); PIN14(w2f, 14); pin(gq[0]); pin(gq[1]); pl0 = pl1 = pl2 = 14u; }
        if constexpr (j == 0 && s == G2_PL) { if (w == 0) POLL_ISSUE(pl0, c2l, 255); }
        if constexpr (j == 0 && s == 13) { if (w == 0) POLL_ISSUE(pl1, c2l, 255); }
        acc[0] = MFMA32(w2f[2 * s], hf[j][s], acc[0]);
        acc[1] = MFMA32(w2f[2 * s + 1], hf[j][s], acc[1]);
        SB();
        if constexpr (s + G2_B1 < 14) H_LOAD(j, s + G2_B1, s + G2_B1 + 1);
      });
      if constexpr (j == 0) {
      } else {
        STAMP(7);
      }
      char* red = smem + j * 65536;
#pragma unroll
      for (int P = 0; P < 8; ++P)
        *(f32x4*)(red + (P * 8 + w) * 1024 + lane * 16) = f32x4{acc[P & 1][4 * (P >> 1)], acc[P & 1][4 * (P >> 1) + 1], acc[P & 1][4 * (P >> 1) + 2], acc[P & 1][4 * (P >> 1) + 3]};
      if constexpr (j == 0) {
        if (w == 0) poll_loop<8>(c2l, 14u, pl0, pl1, pl2);
        asm volatile("s_waitcnt lgkmcnt(0)\n\ts_barrier" ::: "memory");
        H_LOAD(1, 0, G2_B1);
      }
      {
        const float gbv[4] = {rbf(U2F(gq[0].x) + U2F(gq[1].x)), rbf(U2F(gq[0].y) + U2F(gq[1].y)), rbf(U2F(gq[0].z) + U2F(gq[1].z)), rbf(U2F(gq[0].w) + U2F(gq[1].w))};
        if constexpr (j == 1) asm volatile("s_waitcnt lgkmcnt(0)\n\ts_barrier" ::: "memory");
        {
          constexpr int jj = j;
          const char* rp = smem + jj * 65536 + w * 8192 + lane * 16;
          const f32x4 y = ((*(const f32x4*)rp + *(const f32x4*)(rp + 1024)) + (*(const f32x4*)(rp + 2048) + *(const f32x4*)(rp + 3072))) +
                          ((*(const f32x4*)(rp + 4096) + *(const f32x4*)(rp + 5120)) + (*(const f32x4*)(rp + 6144) + *(const f32x4*)(rp + 7168)));
          const u32x2 xr = trread(smem + G2_X1 + jj * 4096 + (32 * dt + 8 * kq + 4 * hi + qq) * 64 + (G & 1) * 32 + pp * 8);
          u32x2 o; o.x = pkbf(__builtin_fmaf(y[0], gbv[0], bflo(xr.x)), __builtin_fmaf(y[1], gbv[1], bfhi(xr.x)));
          o.y = pkbf(__builtin_fmaf(y[2], gbv[2], bflo(xr.y)), __builtin_fmaf(y[3], gbv[3], bfhi(xr.y)));
          *(u32x2*)(smem + 131072 + jj * 8192 + (lane & 31) * 144 + (32 * dt + 8 * kq + 4 * hi) * 2) = o;
          asm volatile("s_waitcnt lgkmcnt(0)\n\ts_barrier" ::: "memory");
          const int tok = 4 * w + ((lane >> 3) & 3);
          const u32x4 row = *(const u32x4*)(smem + 131072 + jj * 8192 + tok * 144 + (lane & 7) * 16);
          if (lane < 32) *(u32x4*)(out + (size_t)(32 * (jj ? 2 * (r2 >> 1) + 33 - (r2 & 1) : r2) + tok) * D_ + 64 * dg + 8 * (lane & 7)) = row;
        }
      }
      SB();
    });
  }
  NP0S(15);
  STAMP_OUT(stamps, 1024 + b);
  STAMP_OUT2(stamps, 2048 + b);
}

extern "C" void kernel_launch(void* const* d_in, const int* in_sizes, int n_in, void* d_out, int out_size, void* d_ws, size_t ws_size,
                              hipStream_t stream) {
  const u16* x = (const u16*)d_in[0]; const u16* logA = (const u16*)d_in[1]; const u16* Aim = (const u16*)d_in[2];
  const u16* Cr = (const u16*)d_in[3]; const u16* Ci = (const u16*)d_in[4]; const u16* logdt = (const u16*)d_in[5]; const u16* Dsk = (const u16*)d_in[6];
  const u16* w1 = (const u16*)d_in[8]; const u16* w3 = (const u16*)d_in[9]; const u16* w2 = (const u16*)d_in[10];
  const u16* c = (const u16*)d_in[11]; const u16* wm = (const u16*)d_in[12];
  char* ws = (char*)d_ws;
  unsigned long long* stamps = (unsigned long long*)(ws + (64u << 20));
  kf_main<<<256, 512, 0, stream>>>(x, logA, Aim, Cr, Ci, logdt, Dsk, w1, w3, w2, c, wm, ws, (u16*)d_out, stamps);
}
```
